```python
import torch

def _softmax(x, axis=-1):
    return torch.softmax(x, dim=axis)
def _gelu(x):
    return torch.nn.functional.gelu(x, approximate="tanh")
def _silu(x):
    return torch.nn.functional.silu(x)
def _sigmoid(x):
    return torch.sigmoid(x)
def _layernorm(x, axis=-1, eps=1e-5):
    m = x.mean(dim=axis, keepdim=True)
    v = x.var(dim=axis, keepdim=True, unbiased=False)
    return (x - m) / torch.sqrt(v + eps)
def _scatter_add(tgt, idx, src, axis=0):
    return tgt.clone().index_add_(axis, idx, src)
def _segment_sum(src, seg, n):
    out = torch.zeros((n,) + src.shape[1:], dtype=src.dtype)
    return out.index_add_(0, seg, src)
def _pad(x, pw):
    flat = tuple(v for pair in reversed(pw) for v in pair)
    return torch.nn.functional.pad(x, flat)

kernel_name = 'parallel_sum__linear_attn_block__moe_arch_mod__prompt'

def setup_inputs():

    _w = (lambda a: (lambda t: t.to(torch.bfloat16) if t.is_floating_point() else t)(torch.as_tensor(a)))
    def _A_setup():
        B, S, D = 1, 2048, 1024
        return {
            "x": _w((lambda *s: torch.randn(*s, dtype=torch.bfloat16) * 0.05)(B, S, D)),
            "wqkv": _w((lambda *s: torch.randn(*s, dtype=torch.bfloat16) * 0.4)(D, 3 * D)),
            "wo": _w((lambda *s: torch.randn(*s, dtype=torch.bfloat16) * 0.2)(D, D)),
        }
    _A_in = _A_setup()

    def _B_setup():
        B, S, D, F, C = 1, 2048, 1024, 3584, 8
        return {
            "x": _w((lambda *s: torch.randn(*s, dtype=torch.bfloat16) * 0.05)(B, S, D)),
            "w_r": _w((lambda *s: torch.randn(*s, dtype=torch.bfloat16) * 0.05)(D)),
            "w_gate": _w((lambda *s: torch.randn(*s, dtype=torch.bfloat16) * 0.05)(D, F)),
            "w_up": _w((lambda *s: torch.randn(*s, dtype=torch.bfloat16) * 0.05)(D, F)),
            "w_down": _w((lambda *s: torch.randn(*s, dtype=torch.bfloat16) * 0.05)(F, D)),
        }
    _B_in = _B_setup()

    return {
        "A_x": _A_in["x"],
        "A_wqkv": _A_in["wqkv"],
        "A_wo": _A_in["wo"],
        "B_x": _B_in["x"],
        "B_w_r": _B_in["w_r"],
        "B_w_gate": _B_in["w_gate"],
        "B_w_up": _B_in["w_up"],
        "B_w_down": _B_in["w_down"],
    }

def reference(A_x, A_wqkv, A_wo, B_x, B_w_r, B_w_gate, B_w_up, B_w_down):
    def _A_ref(x, wqkv, wo):
        B, S, D = x.shape
        H = 32; Dh = D // H
        qkv = (lambda x, *d: x.permute(*d))((x @ wqkv).reshape(B, S, 3, H, Dh), 2, 0, 3, 1, 4)
        q, k, v = qkv[0], qkv[1], qkv[2]
        phi = lambda t: torch.where(t > 0, t + 1, torch.exp(t)) + 1e-6
        pq, pk = phi(q), phi(k)
        kv = torch.einsum("bhsd,bhse->bhde", pk, v)
        z = torch.einsum("bhsd->bhd", pk)
        num = torch.einsum("bhqd,bhde->bhqe", pq, kv)
        den = torch.einsum("bhqd,bhd->bhq", pq, z)[..., None] + 1e-6
        o = num / den
        return (lambda x, *d: x.permute(*d))(o, 0, 2, 1, 3).reshape(B, S, D) @ wo

    def _B_ref(x, w_r, w_gate, w_up, w_down):
        B, S, D = x.shape
        C = 8
        r = x @ w_r
        idx = torch.argsort(-r, axis=-1)[:, :C]
        idx_e = (lambda x, n, axis: torch.repeat_interleave(x, n, dim=axis))(idx.reshape(B, C, 1), D, axis=2)
        x_sel = (lambda x, i, axis: torch.take_along_dim(x, i, dim=axis))(x, idx_e, axis=1)
        g = _silu(x_sel @ w_gate)
        u = x_sel @ w_up
        y = (g * u) @ w_down
        r_sel = _sigmoid((lambda x, i, axis: torch.take_along_dim(x, i, dim=axis))(r, idx, axis=1))
        y = y * r_sel.reshape(B, C, 1)
        b_off = (torch.arange(B) * S).reshape(B, 1)
        flat_idx = (idx + b_off).reshape(B * C)
        zero = (x * 0.0).reshape(B * S, D)
        upd = _scatter_add(zero, flat_idx, y.reshape(B * C, D), axis=0)
        return x + upd.reshape(B, S, D)

    x = A_x
    xn = _layernorm(x, axis=-1)
    return x + _A_ref(xn, A_wqkv, A_wo) + _B_ref(xn, B_w_r, B_w_gate, B_w_up, B_w_down)
```

```cpp
#include <hip/hip_runtime.h>
#include <hip/hip_bf16.h>
#include <stdint.h>

typedef unsigned short u16;
typedef unsigned long long u64;
typedef short s4 __attribute__((ext_vector_type(4)));
typedef short s8 __attribute__((ext_vector_type(8)));
typedef float f4 __attribute__((ext_vector_type(4)));
typedef float f2 __attribute__((ext_vector_type(2)));
typedef unsigned u4 __attribute__((ext_vector_type(4)));
typedef unsigned u2 __attribute__((ext_vector_type(2)));
typedef __bf16 bf2 __attribute__((ext_vector_type(2)));
#define LAS __attribute__((address_space(3)))
#if defined(__HIP_DEVICE_COMPILE__)
#define NOPK __attribute__((target("no-packed-fp32-ops")))
#else
#define NOPK
#endif
#define DEV __device__ __forceinline__ NOPK

DEV float U2F(unsigned u) { return __builtin_bit_cast(float, u); }
DEV unsigned F2U(float f) { return __builtin_bit_cast(unsigned, f); }
DEV float I2F(int u) { return __builtin_bit_cast(float, u); }
DEV int F2I(float f) { return __builtin_bit_cast(int, f); }

constexpr int S = 2048, D = 1024, F = 3584;

constexpr unsigned WS_EP = 0;
constexpr unsigned WS_FLAG = 4096;
constexpr unsigned WS_MSG = 6144;
constexpr unsigned WS_MS = 16384;
constexpr unsigned WS_CS = 32768;
constexpr unsigned WS_R = 245760;
constexpr unsigned WS_YP = 1280u << 10;
constexpr unsigned WS_KVP = 3328u << 10;
constexpr unsigned WS_OF = 6u << 20;
constexpr unsigned WS_OFL = 10u << 20;
constexpr unsigned WS_END = (10u << 20) + (128u << 10);
constexpr unsigned WS_OFF = 0x7FFF0000u;
constexpr unsigned KVS = 2304;
constexpr unsigned POISON = 0xFFFFFFFFu;

DEV unsigned pk2(float a, float b) { bf2 v; v[0] = (__bf16)a; v[1] = (__bf16)b; return __builtin_bit_cast(unsigned, v); }
DEV float lo(unsigned u) { return U2F(u << 16); }
DEV float hi(unsigned u) { return U2F(u & 0xFFFF0000u); }
DEV float rbf(float a) { return lo(pk2(a, a)); }
DEV unsigned san(unsigned u) { return min(u, 0xFFFFFFFEu); }
DEV u4 san4(u4 v) { v[0] = san(v[0]); v[1] = san(v[1]); v[2] = san(v[2]); v[3] = san(v[3]); return v; }
DEV unsigned mx3(unsigned a, unsigned b, unsigned c) { return max(max(a, b), c); }
DEV unsigned mx4(unsigned m, u4 v) { return mx3(mx3(m, v[0], v[1]), v[2], v[3]); }
DEV u4 tag4(f4 v, unsigned t) { u4 r; _Pragma("unroll") for (int e = 0; e < 4; ++e) r[e] = (F2U(v[e]) & 0xFFFFFF00u) | ((t >> (8 * e)) & 0xFFu); return r; }
DEV f4 untag4(u4 v, unsigned t, unsigned& bad) {
  f4 r; _Pragma("unroll") for (int e = 0; e < 4; ++e) { const unsigned u = v[e] ^ ((t >> (8 * e)) & 0xFFu); bad |= u; r[e] = U2F(u); } return r; }
DEV bool tagok(unsigned bad) { return __builtin_amdgcn_ballot_w64((bad & 0xFFu) != 0u) == 0ull; }
DEV bool clean(unsigned m) { return __builtin_amdgcn_ballot_w64(m == POISON) == 0ull; }
DEV bool all64(bool c) { return __builtin_amdgcn_ballot_w64(!c) == 0ull; }
DEV float phi1(float a) { return __builtin_fmaf(fmaxf(a, 1.44269504e-6f), 0.693147181f, __builtin_amdgcn_fmed3f(__builtin_amdgcn_exp2f(a), 0.f, 1.f)); }
#define RD128(dst, addr, OFF) asm volatile("ds_read_b128 %0, %1 offset:%2" : "=v"(dst) : "v"(addr), "i"(OFF) : "memory")
#define RD64(dst, addr, OFF) asm volatile("ds_read_b64 %0, %1 offset:%2" : "=v"(dst) : "v"(addr), "i"(OFF) : "memory")
#define RD32(dst, addr, OFF) asm volatile("ds_read_b32 %0, %1 offset:%2" : "=v"(dst) : "v"(addr), "i"(OFF) : "memory")
#define RDTR(dst, addr, OFF) asm volatile("ds_read_b64_tr_b16 %0, %1 offset:%2" : "=v"(dst) : "v"(addr), "i"(OFF) : "memory")
#define SBAR() __builtin_amdgcn_sched_barrier(0)
#define CAT(x, y) __builtin_shufflevector(x, y, 0, 1, 2, 3, 4, 5, 6, 7)
#define WAITV(n) asm volatile("s_waitcnt vmcnt(%0)" ::"i"(n) : "memory")
#define WAITL(n) asm volatile("s_waitcnt lgkmcnt(%0)" ::"i"(n) : "memory")
#define CLOB() asm volatile("" ::: "memory")
#define BAR() asm volatile("s_barrier" ::: "memory")
#define LBAR() asm volatile("s_waitcnt lgkmcnt(0)\n s_barrier" ::: "memory")
#define PIN(v) asm volatile("" : "+v"(v))
#define PACE(n) do { SBAR(); __builtin_amdgcn_s_sleep(n); SBAR(); } while (0)
#define RFL(v) ((unsigned)__builtin_amdgcn_readfirstlane(v))
DEV const char* uptr(const char* p) {
  u64 v = (u64)p;
  return (const char*)(((u64)RFL((unsigned)(v >> 32)) << 32) | RFL((unsigned)v));
}
DEV u4 mkrs(const char* p, unsigned bytes) {
  const u64 v = (u64)uptr(p);
  u4 r; r[0] = (unsigned)v; r[1] = (unsigned)(v >> 32) & 0xFFFFu; r[2] = RFL(bytes); r[3] = 0x00020000u;
  return r;
}
#define BLDS(voff, rs, so, OFF) asm volatile("buffer_load_dwordx4 %0, %1, %2 offen offset:%3 lds" ::"v"(voff), "s"(rs), "s"(so), "i"(OFF) : "memory")
#define MFMA16(a, b, c) __builtin_amdgcn_mfma_f32_16x16x32_bf16(a, b, c, 0, 0, 0)
#define MFMAH(a, b, c) __builtin_amdgcn_mfma_f32_16x16x16bf16_1k(__builtin_bit_cast(s4, (u2){(a), 0u}), __builtin_bit_cast(s4, (u2){(b), 0u}), c, 0, 0, 0)
#define DOT2(a, b, c) __builtin_amdgcn_fdot2_f32_bf16(__builtin_bit_cast(bf2, (unsigned)(a)), __builtin_bit_cast(bf2, (unsigned)(b)), c, false)
#define BC8(v) __builtin_bit_cast(s8, v)
#define LD128(off) __builtin_amdgcn_raw_buffer_load_b128(wrs, (off), 0, 16)
#define LD64(off) __builtin_amdgcn_raw_buffer_load_b64(wrs, (off), 0, 16)
#define LD32(off) __builtin_amdgcn_raw_buffer_load_b32(wrs, (off), 0, 16)
#define ST128(v, off) __builtin_amdgcn_raw_buffer_store_b128((v), wrs, (off), 0, 16)
#define ST64(v, off) __builtin_amdgcn_raw_buffer_store_b64((v), wrs, (off), 0, 16)
#define ST32(v, off) __builtin_amdgcn_raw_buffer_store_b32((v), wrs, (off), 0, 16)
#define ALD128(dst, off) asm volatile("buffer_load_dwordx4 %0, %1, %2, 0 offen sc1" : "=v"(dst) : "v"(off), "s"(rsW) : "memory")
#define ALD64(dst, off) asm volatile("buffer_load_dwordx2 %0, %1, %2, 0 offen sc1" : "=v"(dst) : "v"(off), "s"(rsW) : "memory")
#define ALD32(dst, off) asm volatile("buffer_load_dword %0, %1, %2, 0 offen sc1" : "=v"(dst) : "v"(off), "s"(rsW) : "memory")
constexpr unsigned SPIN_CAP = 1u << 20;

DEV void lds_arrive(LAS unsigned* c) {
  asm volatile("s_mov_b64 exec, 1\n ds_add_u32 %0, %1\n s_mov_b64 exec, -1" ::"v"((unsigned)(size_t)c), "v"(1u) : "memory");
}
template <int SL> DEV void lds_wait(LAS unsigned* c, unsigned target) {
#pragma nounroll
  while (RFL(__hip_atomic_load(c, __ATOMIC_RELAXED, __HIP_MEMORY_SCOPE_WORKGROUP)) < target) { if (SL) __builtin_amdgcn_s_sleep(SL); }
  CLOB();
}

#define STMP(idx)
#define STMC(idx)
#define STDUMP()
#define DPPF(v, ctrl, rm) I2F(__builtin_amdgcn_update_dpp(0, F2I(v), ctrl, rm, 0xf, false))
DEV unsigned wave_max(unsigned v) {
  v = max(v, (unsigned)__builtin_amdgcn_update_dpp(0, (int)v, 0x111, 0xf, 0xf, false));
  v = max(v, (unsigned)__builtin_amdgcn_update_dpp(0, (int)v, 0x112, 0xf, 0xf, false));
  v = max(v, (unsigned)__builtin_amdgcn_update_dpp(0, (int)v, 0x114, 0xf, 0xf, false));
  v = max(v, (unsigned)__builtin_amdgcn_update_dpp(0, (int)v, 0x118, 0xf, 0xf, false));
  v = max(v, (unsigned)__builtin_amdgcn_update_dpp(0, (int)v, 0x142, 0xa, 0xf, false));
  v = max(v, (unsigned)__builtin_amdgcn_update_dpp(0, (int)v, 0x143, 0xc, 0xf, false));
  return (unsigned)__builtin_amdgcn_readlane((int)v, 63);
}

constexpr int SLOT = 49152, L_B = 32768;
constexpr int L_ROWP = 147456;
constexpr int L_ROWS = L_ROWP + 1024;
constexpr int L_COL = L_ROWS + 1024;
constexpr int L_EX = SLOT;
constexpr int L_XT = 0;
constexpr int L_KX = 16384;
constexpr int L_ZX = L_KX + 8192;
constexpr int L_YS = L_ZX + 256;
constexpr int L_TAG = L_YS + 2048;
constexpr int L_CNT = L_TAG + 64;
constexpr int L_WO = 32768;
constexpr int L_RED = 65536;
constexpr int L_H = L_RED + 32768;
constexpr int L_TOP = L_H + 1024;
constexpr int K_LDS = 163840;

#define NTL(p) __builtin_nontemporal_load(p)
#define TRFRAG(f, W, c) do { _Pragma("unroll") for (int q_ = 0; q_ < 4; ++q_) { unsigned e_ = W[2 * q_][(c) >> 1], o_ = W[2 * q_ + 1][(c) >> 1]; \
    f[q_] = ((c) & 1) ? __builtin_amdgcn_perm(o_, e_, 0x07060302u) : __builtin_amdgcn_perm(o_, e_, 0x05040100u); } } while (0)
constexpr u64 TAG_STEP = 0x9E3779B97F4A7C15ull;

struct FW { s8 a[8]; s4 b[8]; };
struct FQ { s8 a[4]; s4 b[8]; };
#define RA(G, s, u, i) RD128(G.a[i], aad[((s) % 3) == 2][u], (((s) % 3) & 1) * SLOT + (i) * 2048)
#define RB(G, s, u, n) RDTR(G.b[n], bad[((s) % 3) == 2][((n) >> 1) & 1], (((s) % 3) & 1) * SLOT + ((n) >> 2) * 8192 + (u) * 4096 + ((n) & 1) * 512)
#define FB(G, j) CAT(G.b[2 * (j)], G.b[2 * (j) + 1])

#define KD 8
#define KSL 2
#define PD5 15
#define OSL 4
#define MD1 40
#define MD2 60
#define TKP 9

__global__ __launch_bounds__(512, 2) NOPK void fused(const u16* __restrict__ x, const u16* __restrict__ wqkv, const u16* __restrict__ wo, const u16* __restrict__ wr,
                                                const u16* __restrict__ wg, const u16* __restrict__ wu, const u16* __restrict__ wd, char* ws,
                                                u16* __restrict__ out) {
  extern __shared__ __attribute__((aligned(16))) char lds[];
  const int t0 = threadIdx.x, w = __builtin_amdgcn_readfirstlane(t0 >> 6), l = t0 & 63;
  STMP(0);
  const int gb = blockIdx.x, xcd = gb & 7, j = gb >> 3, xq = xcd >> 1;
  const int np = l & 15, g = l >> 4, rr = l >> 3, sl = l & 7;
  const unsigned ldsb = (unsigned)(size_t)(LAS char*)lds;
  const __amdgpu_buffer_rsrc_t wrs = __builtin_amdgcn_make_buffer_rsrc(ws, 0, WS_END, 0x00020000);
  const int mb2 = (xcd & 1) * 8 + (xcd >> 2) * 4 + (j & 3), nb = ((xcd >> 1) & 1) * 8 + (j >> 2), hx = (mb2 & 3) * 8;
  const unsigned msg = WS_MSG + xcd * 256 + ((l & 8) ? 0 : 64 + sl * 8);
#define MSGOK(f) all64((l & 8) ? ((f)[0] == tlo && (f)[1] == thi) : (f)[1] == tlo)
#define WO_SETUP() const u4 rsO = mkrs((const char*)wo + nb * 128, 1024 * 2048 - nb * 128), rsX = mkrs((const char*)x + (size_t)mb2 * 128 * 2048 + nb * 128, 128 * 2048 - nb * 128); \
  unsigned vW = rr * 2048 + ((sl ^ (((rr >> 1) & 3) << 1)) * 16), vX = rr * 2048 + ((sl ^ rr) * 16); \
  const unsigned w6m = RFL(ldsb + L_WO + w * 6144), w6s = RFL(w * 6 * 16384), w2m = RFL(ldsb + w * 2048), w2s = RFL(w * 2 * 16384), hxs = RFL(hx * 65536); \
  unsigned sW; PIN(vW); PIN(vX)
#define WN(i) ((i) < 6 ? 16 + (i) : (i) < 12 ? 58 + (i) : (i) < 14 ? (i) - 12 : (i) < 16 ? 98 + (i) : (i) - 16)
#define WSET(i) do { if ((i) < 16) asm volatile("s_add_u32 m0, %1, %2\n s_add_u32 %0, %3, %4\n s_xor_b32 %0, %0, %5" : "=&s"(sW) : "s"((i) < 12 ? w6m : w2m), "i"(WN(i) * 1024 + ((i) < 12 ? 0 : L_WO)), "s"((i) < 12 ? w6s : w2s), "i"(WN(i) * 16384), "s"(hxs) : "scc", "memory"); \
    else asm volatile("s_add_u32 m0, %1, %2\n s_add_u32 %0, %3, %4" : "=&s"(sW) : "s"(w2m), "i"(L_XT + WN(i) * 1024), "s"(w2s), "i"(WN(i) * 16384) : "scc", "memory"); } while (0)
#define WLD(i) do { if ((i) < 16) BLDS(vW, rsO, sW, 0); else BLDS(vX, rsX, sW, 0); } while (0)
#define WSL(i) do { WSET(i); WLD(i); } while (0)
#define WO_ALL() do { WSL(12); WSL(13); WSL(16); WSL(17); WSL(0); WSL(1); WSL(2); WSL(3); WSL(4); WSL(5); WSL(6); WSL(7); WSL(8); WSL(9); WSL(10); WSL(11); WSL(14); WSL(15); } while (0)
  u2 st2[2];
  const unsigned sto = WS_MS + (mb2 * 128 + (w & 3) * 32 + np) * 8;

  if (j < 16) {
    const int rg = (xcd & 1) * 4 + (j & 3), hp = xq * 4 + (j >> 2);
    if (w == 7) {
      const u4 rsW = mkrs(ws, WS_END);
      int nbar = 16;
#define TICK() do { if (nbar) { BAR(); --nbar; } } while (0)
      TICK();
      u2 ep; unsigned z0 = 0; PIN(z0);
      ALD64(ep, z0);
      { const u4 pz = (u4){POISON, POISON, POISON, POISON};
        const unsigned kb = WS_KVP + (hp * 16 + rg) * KVS + l * 16;
        ST128(pz, kb); ST128(pz, kb + 1024); ST128(pz, kb + 8 * KVS); ST128(pz, kb + 8 * KVS + 1024);
        ST128(pz, l < 16 ? WS_KVP + (hp * 16 + rg + (l & 8)) * KVS + 2048 + sl * 16 : WS_OFF); }
      SBAR();
      TICK(); TICK(); TICK();
      WAITV(0); PIN(ep);
      const u64 tag = (((u64)RFL(ep[1]) << 32) | RFL(ep[0])) + TAG_STEP;
      const unsigned tlo = (unsigned)tag, thi = (unsigned)(tag >> 32);
      ST64(((u2){tlo, thi}), l == 0 ? WS_FLAG + gb * 8 : WS_OFF);
      STMP(8);
#pragma nounroll
      while (nbar > 16 - TKP) TICK();
      u2 f;
#pragma nounroll
      for (unsigned it = 0; it < SPIN_CAP; ++it) {
        ALD64(f, msg);
        if (nbar > 2) TICK();
        WAITV(0); PIN(f);
        if (MSGOK(f)) break;
      }
      STMP(9);
      u4 ms[2], cs[2];
      { const unsigned a0 = WS_MS + rg * 2048 + l * 32, a1 = a0 + 16, a2 = WS_CS + (g * 3072 + 1024 + hp * 64 + np * 4) * 4, a3 = a2 + 4096;
        ALD128(ms[0], a0); ALD128(ms[1], a1); ALD128(cs[0], a2); ALD128(cs[1], a3); }
      TICK(); TICK();
      WAITV(0); PIN(ms[0]); PIN(ms[1]); PIN(cs[0]); PIN(cs[1]);
      u4 pp, rs;
#pragma unroll
      for (int e = 0; e < 4; ++e) {
        const unsigned m = ms[e >> 1][(e & 1) * 2] ^ 0x80000000u;
        pp[e] = (m & 0xFFFF0000u) | (m >> 16);
        rs[e] = F2U(U2F(ms[e >> 1][(e & 1) * 2 + 1]) * 1.44269504f);
      }
      PIN(pp); PIN(rs);
#pragma nounroll
      while (nbar) TICK();
      *(u4*)(lds + L_ROWP + l * 16) = pp; *(u4*)(lds + L_ROWS + l * 16) = rs;
      *(u4*)(lds + L_COL + g * 512 + np * 16) = cs[0]; *(u4*)(lds + L_COL + g * 512 + 256 + np * 16) = cs[1];
      LBAR();
      if (l < 8) ((unsigned*)(lds + L_TAG + 16))[l] = f[0];
      if (l == 8) *(u2*)(lds + L_TAG) = f;
      if (l < 8) ((unsigned*)(lds + L_CNT))[l] = 0u;
      LBAR(); BAR();
    } else if (w >= 4) {
      const bool isB = w == 6;
      unsigned v[4];
#pragma unroll
      for (int i = 0; i < 4; ++i) { const int row = 8 * i + rr;
        v[i] = (isB ? row * 6144 + ((sl ^ ((((row >> 1) & 1) | (((row >> 3) & 1) << 1)) << 1)) * 16) : row * 2048 + ((sl ^ ((row >> 1) & 7)) * 16)) - i * 1024; }
      const u4 rs = isB ? mkrs((const char*)wqkv + 2048 + hp * 128, 1024 * 6144 - 2048 - hp * 128) : mkrs((const char*)x + (size_t)(rg * 256 + (w - 4) * 128) * 2048, 128 * 2048);
      const unsigned mb_ = RFL(ldsb + (w - 4) * 16384), step = isB ? 64 * 6144 : 128;
      const unsigned d1 = isB ? 32 * 6144 : 65536, d2 = isB ? 2048 : 131072, d3 = d1 + d2;
      unsigned so = 0;
#define G4(s, q, dq) do { unsigned t_; asm volatile("s_add_u32 m0, %1, %2\n s_add_u32 %0, %3, %4" : "=s"(t_) : "s"(mb_), "i"(((s) % 3) * SLOT + (q) * 4096), "s"(so), "s"(dq) : "scc", "memory"); \
      BLDS(v[0], rs, t_, 0); BLDS(v[1], rs, t_, 1024); BLDS(v[2], rs, t_, 2048); BLDS(v[3], rs, t_, 3072); } while (0)
#define PA(s) do { G4(s, 0, 0u); G4(s, 1, d1); } while (0)
#define PB(s) do { G4(s, 2, d2); G4(s, 3, d3); asm volatile("s_add_u32 %0, %0, %1" : "+s"(so) : "s"(step) : "scc"); } while (0)
      PA(0); PB(0); PA(1); WAITV(8); BAR(); PB(1); PA(2); WAITV(8); BAR(); PB(2); PA(0); PB(0);
#define LS(s) do { if ((s) < 14) WAITV(16); else WAITV(0); BAR(); if ((s) + 3 < 16) { PA(s); PB(s); } } while (0)
      LS(1); LS(2); LS(3); LS(4); LS(5); LS(6); LS(7); LS(8); LS(9); LS(10); LS(11); LS(12); LS(13); LS(14);
      BAR(); BAR(); BAR();
    } else {
      const int wm = w >> 1, wn = w & 1;
      unsigned aad[2][2], bad[2][2];
      { const int q = np >> 2, p = l & 3, f = ((q >> 1) & 1) | ((g & 1) << 1);
#pragma unroll
        for (int u = 0; u < 2; ++u) { aad[0][u] = ldsb + (wm * 128 + np) * 128 + (((4 * u + g) ^ (np >> 1)) * 16); aad[1][u] = aad[0][u] + 2 * SLOT;
          bad[0][u] = ldsb + L_B + (8 * g + q) * 128 + (((2 * wn + u) ^ f) * 32) + p * 8; bad[1][u] = bad[0][u] + 2 * SLOT; } }
      f4 c[8][4];
#pragma unroll
      for (int i = 0; i < 8; ++i)
#pragma unroll
        for (int jj = 0; jj < 4; ++jj) c[i][jj] = (f4)(0.f);
#define RW(G, s, u, n) do { if ((n) < 8) RB(G, s, u, (n) & 7); else RA(G, s, u, ((n) - 8) & 7); SBAR(); } while (0)
#define SNK(n) ((((n) >> 2) & 1) ? 3 - ((n) & 3) : (n) & 3)
#define MW(G, n) do { c[(n) >> 2][SNK(n)] = MFMA16(G.a[(n) >> 2], FB(G, SNK(n)), c[(n) >> 2][SNK(n)]); SBAR(); } while (0)
#define MW2(Fq, G, sn, un, n) do { MW(Fq, 2 * (n)); if ((n) < 12) RW(G, sn, un, (n) + ((n) + 2) / 3); MW(Fq, 2 * (n) + 1); if ((n) < 12 && (n) % 3 == 0) RW(G, sn, un, (n) + (n) / 3 + 1); } while (0)
#define HALFW(Fq, G, sn, un) do { MW2(Fq, G, sn, un, 0); MW2(Fq, G, sn, un, 1); MW2(Fq, G, sn, un, 2); MW2(Fq, G, sn, un, 3); MW2(Fq, G, sn, un, 4); MW2(Fq, G, sn, un, 5); MW2(Fq, G, sn, un, 6); MW2(Fq, G, sn, un, 7); \
    MW2(Fq, G, sn, un, 8); MW2(Fq, G, sn, un, 9); MW2(Fq, G, sn, un, 10); MW2(Fq, G, sn, un, 11); MW2(Fq, G, sn, un, 12); MW2(Fq, G, sn, un, 13); MW2(Fq, G, sn, un, 14); MW2(Fq, G, sn, un, 15); WAITL(0); SBAR(); } while (0)
#define STAGEW(s) do { HALFW(f0, f1, s, 1); BAR(); SBAR(); HALFW(f1, f0, (s) + 1, 0); } while (0)
      FW f0, f1;
      BAR();
      STMP(1); STMC(40);
      RW(f0, 0, 0, 0); RW(f0, 0, 0, 1); RW(f0, 0, 0, 2); RW(f0, 0, 0, 3); RW(f0, 0, 0, 4); RW(f0, 0, 0, 5); RW(f0, 0, 0, 6); RW(f0, 0, 0, 7);
      RW(f0, 0, 0, 8); RW(f0, 0, 0, 9); RW(f0, 0, 0, 10); RW(f0, 0, 0, 11); RW(f0, 0, 0, 12); RW(f0, 0, 0, 13); RW(f0, 0, 0, 14); RW(f0, 0, 0, 15);
      WAITL(0); SBAR();
      STAGEW(0); STAGEW(1); STAGEW(2); STAGEW(3); STAGEW(4); STAGEW(5); STAGEW(6); STAGEW(7);
      STAGEW(8); STAGEW(9); STAGEW(10); STAGEW(11); STAGEW(12); STAGEW(13); STAGEW(14);
      HALFW(f0, f1, 15, 1); BAR(); SBAR();
      unsigned pp[8], cc[4]; f4 rsk[8];
      { const unsigned tA = ldsb + L_ROWP + (wm * 128 + np) * 4, tR = ldsb + L_ROWS + (wm * 128 + 4 * g) * 4, tC = ldsb + L_COL + g * 512 + (wn * 32 + np) * 4;
#define TB(n) do { if ((n) < 4) RD32(cc[(n) & 3], tC, ((n) >> 1) * 256 + ((n) & 1) * 64); else if ((n) < 12) RD32(pp[((n) - 4) & 7], tA, (((n) - 4) & 7) * 64); else if ((n) < 20) RD128(rsk[((n) - 12) & 7], tR, (((n) - 12) & 7) * 64); SBAR(); } while (0)
#define MT(n) do { MW(f1, n); TB(n); } while (0)
        MT(0); MT(1); MT(2); MT(3); MT(4); MT(5); MT(6); MT(7); MT(8); MT(9); MT(10); MT(11); MT(12); MT(13); MT(14); MT(15);
        MT(16); MT(17); MT(18); MT(19); MT(20); MT(21); MT(22); MT(23); MT(24); MT(25); MT(26); MT(27); MT(28); MT(29); MT(30); MT(31);
        WAITL(0);
#pragma unroll
        for (int i = 0; i < 8; ++i) { PIN(pp[i]); PIN(rsk[i]); }
        PIN(cc[0]); PIN(cc[1]); PIN(cc[2]); PIN(cc[3]); }
      STMP(2); STMC(41);
      f4 kv[2][2], zz[2];
      kv[0][0] = kv[0][1] = kv[1][0] = kv[1][1] = zz[0] = zz[1] = (f4)(0.f);
      const u4 ones = (u4){0x3F803F80u, 0x3F803F80u, 0x3F803F80u, 0x3F803F80u};
#define MEAN(i) do { _Pragma("unroll") for (int jj = 0; jj < 4; ++jj) c[i][jj] = MFMAH(pp[i], cc[jj], c[i][jj]); } while (0)
      MEAN(0); MEAN(1);
#pragma unroll
      for (int ip = 0; ip < 4; ++ip) {
        if (ip < 3) { MEAN(2 * ip + 2); MEAN(2 * ip + 3); }
        u4 kf[2], vf[2];
#pragma unroll
        for (int t = 0; t < 2; ++t)
#pragma unroll
          for (int e = 0; e < 4; ++e) {
            const int i = 2 * ip + (e >> 1), e0 = (e & 1) * 2;
            kf[t][e] = pk2(phi1(c[i][t][e0] * rsk[i][e0]), phi1(c[i][t][e0 + 1] * rsk[i][e0 + 1]));
            vf[t][e] = pk2(c[i][2 + t][e0] * rsk[i][e0], c[i][2 + t][e0 + 1] * rsk[i][e0 + 1]);
          }
#pragma unroll
        for (int dt = 0; dt < 2; ++dt) {
          kv[dt][0] = MFMA16(BC8(kf[dt]), BC8(vf[0]), kv[dt][0]); kv[dt][1] = MFMA16(BC8(kf[dt]), BC8(vf[1]), kv[dt][1]);
          zz[dt] = MFMA16(BC8(kf[dt]), BC8(ones), zz[dt]);
        }
      }
      STMP(19);
      const f4 zs = (np & 1) ? zz[1] : zz[0];
      char* ex = lds + L_EX + l * 16;
      *(f4*)(ex + w * 4096) = wm ? kv[0][0] : kv[0][1]; *(f4*)(ex + w * 4096 + 1024) = wm ? kv[1][0] : kv[1][1]; *(f4*)(ex + w * 4096 + 2048) = zs;
      LBAR();
      const f4 o0 = ((wm ? kv[0][1] : kv[0][0]) + *(const f4*)(ex + (w ^ 2) * 4096)) * 0.693147181f;
      const f4 o1 = ((wm ? kv[1][1] : kv[1][0]) + *(const f4*)(ex + (w ^ 2) * 4096 + 1024)) * 0.693147181f;
      const f4 zt = zs + *(const f4*)(ex + (w ^ 2) * 4096 + 2048);
      const unsigned kb = WS_KVP + ((hp * 2 + wn) * 8 + rg) * KVS;
      ST128(san4((u4){pk2(o0[0], o0[1]), pk2(o0[2], o0[3]), pk2(o1[0], o1[1]), pk2(o1[2], o1[3])}), kb + wm * 1024 + l * 16);
      ST128(san4(__builtin_bit_cast(u4, zt)), wm == 0 && np < 2 ? kb + 2048 + np * 64 + g * 16 : WS_OFF);
      SBAR();
      STMP(3);
      BAR();
    }
    WO_SETUP();
    st2[0] = LD64(sto); st2[1] = LD64(sto + 128);
    SBAR();
    WO_ALL();
    WAITV(0); BAR();
    STMP(20);
  } else if (j < 24) {
    const int rg = (xcd & 1) * 4 + (j & 3), qg = xq * 2 + ((j >> 2) & 1);
    const int wm = w >> 1, wn = w & 1;
    const u4 rsW = mkrs(ws, WS_END);
    const u4 rsA = mkrs((const char*)x + (size_t)rg * 256 * 2048, 256 * 2048), rsB = mkrs((const char*)wqkv + qg * 256, 1024 * 6144 - qg * 256);
    const int row0 = rg * 256 + qg * 32 + w * 4;
    u4 xr[8], wv[2]; u2 ep;
    { const unsigned xo = (qg * 32 + w * 4) * 2048 + l * 16, xo2 = xo + 4096; unsigned z0 = 0; PIN(z0);
#pragma unroll
      for (int i = 0; i < 8; ++i) asm volatile("buffer_load_dwordx4 %0, %1, %2, 0 offen offset:%3" : "=v"(xr[i]) : "v"(i < 4 ? xo : xo2), "s"(rsA), "i"((i & 3) * 1024) : "memory");
      const unsigned l16 = l * 16;
      asm volatile("global_load_dwordx4 %0, %2, %3\n global_load_dwordx4 %1, %2, %3 offset:1024" : "=&v"(wv[0]), "=&v"(wv[1]) : "v"(l16), "s"(uptr((const char*)wr)) : "memory");
      ALD64(ep, z0); }
    unsigned vA[4], vB[2];
#pragma unroll
    for (int i = 0; i < 4; ++i) { const int row = 32 * w + 8 * i + rr; vA[i] = row * 2048 + ((sl ^ ((row >> 1) & 7)) * 16) - i * 1024; }
#pragma unroll
    for (int i = 0; i < 2; ++i) { const int row = ((w & 3) * 2 + i) * 8 + rr;
      vB[i] = row * 6144 + (w >> 2) * 128 + ((sl ^ ((((row >> 1) & 1) | (((row >> 3) & 1) << 1)) << 1)) * 16) - i * 1024; }
    const unsigned mA = RFL(ldsb + w * 4096), mB = RFL(ldsb + L_B + w * 2048);
    unsigned sA = 0, sB = 0;
#define M0A(s) asm volatile("s_add_u32 m0, %0, %1" ::"s"(mA), "i"(((s) % 3) * SLOT) : "scc", "memory")
#define M0B(s) asm volatile("s_add_u32 m0, %0, %1" ::"s"(mB), "i"(((s) % 3) * SLOT) : "scc", "memory")
#define LA(i) BLDS(vA[i], rsA, sA, (i) * 1024)
#define LB(i) BLDS(vB[i], rsB, sB, (i) * 1024)
#define NEXTA() asm volatile("s_add_u32 %0, %0, 0x80" : "+s"(sA) :: "scc")
#define NEXTB() asm volatile("s_add_u32 %0, %0, 0x60000" : "+s"(sB) :: "scc")
#define NOP1() asm volatile("s_nop 0")
#define PRE(s) do { M0B(s); NOP1(); LB(0); LB(1); NEXTB(); M0A(s); NOP1(); LA(0); LA(1); LA(2); LA(3); NEXTA(); } while (0)
    PRE(0);
    WAITV(6);
#pragma unroll
    for (int i = 0; i < 8; ++i) PIN(xr[i]);
    PIN(wv[0]); PIN(wv[1]); PIN(ep);
    PRE(1); PRE(2);
    STMP(17);
    const u64 tag = (((u64)RFL(ep[1]) << 32) | RFL(ep[0])) + TAG_STEP;
    const unsigned tlo = (unsigned)tag, thi = (unsigned)(tag >> 32);
    const unsigned ofl = WS_OFL + ((rg * 4 + wm) * 16 + qg * 2 + wn) * 128 + l * 16;
    {
      float a[13];
#pragma unroll
      for (int i = 0; i < 13; ++i) a[i] = 0.f;
      unsigned one2 = 0x3F803F80u; PIN(one2);
#pragma unroll
      for (int jj = 0; jj < 8; ++jj) {
        const unsigned wv_ = wv[jj >> 2][jj & 3];
#pragma unroll
        for (int i = 0; i < 4; ++i) {
          const unsigned xv = xr[2 * i + (jj >> 2)][jj & 3];
          a[3 * i] = DOT2(xv, one2, a[3 * i]); a[3 * i + 1] = DOT2(xv, xv, a[3 * i + 1]); a[3 * i + 2] = DOT2(xv, wv_, a[3 * i + 2]);
        }
        a[12] = DOT2(wv_, one2, a[12]);
      }
#define RSTEP(n, ctrl) _Pragma("unroll") for (int i = 0; i < n; ++i) a[i] += DPPF(a[i], ctrl, 0xf)
      RSTEP(13, 0xB1); RSTEP(13, 0x4E);
#pragma unroll
      for (int i = 0; i < 3; ++i) { const float u0 = (l & 1) ? a[3 + i] : a[i], u1 = (l & 1) ? a[9 + i] : a[6 + i]; a[i] = (l & 2) ? u1 : u0; }
      a[3] = a[12];
      RSTEP(4, 0x124); RSTEP(4, 0x128);
#pragma unroll
      for (int i = 0; i < 4; ++i) {
        auto s16 = __builtin_amdgcn_permlane16_swap(F2U(a[i]), F2U(a[i]), false, false);
        a[i] = U2F(s16[0]) + U2F(s16[1]);
        auto s32 = __builtin_amdgcn_permlane32_swap(F2U(a[i]), F2U(a[i]), false, false);
        a[i] = U2F(s32[0]) + U2F(s32[1]);
      }
      const float mf = a[0] * (1.f / D), m = rbf(mf);
      const float var = rbf(fmaxf(a[1] * (1.f / D) - mf * mf, 0.f));
      const float rs = __builtin_amdgcn_rcpf(rbf(__builtin_amdgcn_sqrtf(rbf(var + 1e-5f))));
      const float dsc = rs * (a[2] - m * a[3]);
      const u2 o = l < 4 ? (u2){F2U(m), F2U(rs)} : (u2){(pk2(dsc, 0.f) << 16) | (tlo & 0xFFFFu), thi};
      ST64(o, l < 8 ? (l < 4 ? WS_MS : WS_R) + (row0 + (l & 3)) * 8 : WS_OFF);
      ST128(((u4){POISON, POISON, POISON, POISON}), l < 8 ? ofl : WS_OFF);
    }
    SBAR();
    WO_SETUP();
    unsigned aad[2][2], bad[2][2];
    { const int q = np >> 2, p = l & 3, f = ((q >> 1) & 1) | ((g & 1) << 1);
#pragma unroll
      for (int u = 0; u < 2; ++u) { aad[0][u] = ldsb + (wm * 64 + np) * 128 + (((4 * u + g) ^ (np >> 1)) * 16); aad[1][u] = aad[0][u] + 2 * SLOT;
        bad[0][u] = ldsb + L_B + (8 * g + q) * 128 + (((2 * wn + u) ^ f) * 32) + p * 8; bad[1][u] = bad[0][u] + 2 * SLOT; } }
    f4 c[4][4];
#pragma unroll
    for (int i = 0; i < 4; ++i)
#pragma unroll
      for (int jj = 0; jj < 4; ++jj) c[i][jj] = (f4)(0.f);
#define RQ(G, s, u, n) do { if ((s) < 16) { if ((n) < 4) RA(G, s, u, (n) & 3); else RB(G, s, u, ((n) - 4) & 7); } } while (0)
#define MQ(G, n) do { c[(n) >> 2][SNK(n)] = MFMA16(FB(G, SNK(n)), G.a[(n) >> 2], c[(n) >> 2][SNK(n)]); SBAR(); } while (0)
#define HALFQ(Fq, G, sn, un, S0, X0, S1, X1, S2, X2, E) do { \
    MQ(Fq, 0); RQ(G, sn, un, 0); S0; SBAR(); MQ(Fq, 1); RQ(G, sn, un, 4); X0; SBAR(); MQ(Fq, 2); RQ(G, sn, un, 5); SBAR(); MQ(Fq, 3); RQ(G, sn, un, 1); SBAR(); \
    MQ(Fq, 4); RQ(G, sn, un, 6); SBAR(); MQ(Fq, 5); RQ(G, sn, un, 7); S1; SBAR(); MQ(Fq, 6); RQ(G, sn, un, 2); X1; SBAR(); MQ(Fq, 7); RQ(G, sn, un, 8); SBAR(); \
    MQ(Fq, 8); RQ(G, sn, un, 9); SBAR(); MQ(Fq, 9); RQ(G, sn, un, 3); SBAR(); MQ(Fq, 10); RQ(G, sn, un, 10); S2; SBAR(); MQ(Fq, 11); RQ(G, sn, un, 11); X2; SBAR(); \
    MQ(Fq, 12); E; SBAR(); MQ(Fq, 13); MQ(Fq, 14); MQ(Fq, 15); WAITL(0); SBAR(); } while (0)
#define Q0(s, X, Y) do { if ((s) >= 14) { Y; } else if ((s) >= 1) { X; } } while (0)
#define Q1(s, X, Y) do { if ((s) >= 13) { Y; } else { X; } } while (0)
#define WI0(s, i) (((s) >= 14 ? (s) - 14 : 0) * 6 + 3 + (i))
#define WI1(s, i) (((s) >= 13 ? (s) - 13 : 0) * 6 + (i))
#define STAGEQ(s, NV, E0, E1) do { \
    HALFQ(f0, f1, s, 1, Q0(s, , WSET(WI0(s, 0))), Q0(s, LA(1), WLD(WI0(s, 0))), Q0(s, , WSET(WI0(s, 1))), Q0(s, LA(2), WLD(WI0(s, 1))), Q0(s, , WSET(WI0(s, 2))), Q0(s, LA(3), WLD(WI0(s, 2))), Q0(s, NEXTA(), ); E0); \
    if ((s) < 15) WAITV(NV); \
    BAR(); SBAR(); \
    HALFQ(f1, f0, (s) + 1, 0, Q1(s, M0B(s), WSET(WI1(s, 0))), Q1(s, LB(0), WLD(WI1(s, 0))), Q1(s, , WSET(WI1(s, 1))), Q1(s, LB(1), WLD(WI1(s, 1))), Q1(s, M0A(s), WSET(WI1(s, 2))), Q1(s, LA(0), WLD(WI1(s, 2))), Q1(s, NEXTB(), ); E1); } while (0)
    FQ f0, f1;
    u2 f, ms[4]; unsigned cc[4];
    unsigned flo = t0 == 0 ? WS_FLAG + gb * 8 : WS_OFF, mso = WS_MS + (rg * 256 + wm * 64 + np) * 8, cso = WS_CS + (g * 3072 + qg * 128 + wn * 32 + np) * 4, msg_ = msg, sto_ = sto;
    u2 tgv = (u2){tlo, thi};
    PIN(flo); PIN(mso); PIN(cso); PIN(msg_); PIN(sto_); PIN(tgv);
#define QFLAG() asm volatile("buffer_store_dwordx2 %0, %1, %2, 0 offen sc1" :: "v"(tgv), "v"(flo), "s"(rsW) : "memory")
#define QCHK() do { PIN(f); if (__builtin_expect(!MSGOK(f), 0)) { _Pragma("nounroll") for (unsigned it = 0; it < SPIN_CAP; ++it) { ALD64(f, msg_); WAITV(0); PIN(f); if (MSGOK(f)) break; __builtin_amdgcn_s_sleep(4); } } \
    _Pragma("unroll") for (int i = 0; i < 4; ++i) asm volatile("buffer_load_dwordx2 %0, %1, %2, 0 offen offset:%3 sc1" : "=v"(ms[i]) : "v"(mso), "s"(rsW), "i"(i * 128) : "memory"); \
    _Pragma("unroll") for (int i = 0; i < 4; ++i) asm volatile("buffer_load_dword %0, %1, %2, 0 offen offset:%3 sc1" : "=v"(cc[i]) : "v"(cso), "s"(rsW), "i"((i >> 1) * 256 + (i & 1) * 64) : "memory"); \
    _Pragma("unroll") for (int i = 0; i < 2; ++i) asm volatile("buffer_load_dwordx2 %0, %1, %2, 0 offen offset:%3 sc1" : "=v"(st2[i]) : "v"(sto_), "s"(rsW), "i"(i * 128) : "memory"); } while (0)
    WAITV(14); BAR();
    STMP(1); STMC(40);
    RQ(f0, 0, 0, 0); RQ(f0, 0, 0, 1); RQ(f0, 0, 0, 2); RQ(f0, 0, 0, 3); RQ(f0, 0, 0, 4); RQ(f0, 0, 0, 5); RQ(f0, 0, 0, 6); RQ(f0, 0, 0, 7); RQ(f0, 0, 0, 8); RQ(f0, 0, 0, 9); RQ(f0, 0, 0, 10); RQ(f0, 0, 0, 11);
    WAITL(0); SBAR();
    STAGEQ(0, 8, , ); STAGEQ(1, 8, , ); STAGEQ(2, 6, , QFLAG()); STAGEQ(3, 7, , ); STAGEQ(4, 6, , ); STAGEQ(5, 6, , ); STAGEQ(6, 6, , ); STAGEQ(7, 6, , );
    STAGEQ(8, 6, , ALD64(f, msg_)); STAGEQ(9, 7, , ); STAGEQ(10, 6, , QCHK()); STAGEQ(11, 16, , ); STAGEQ(12, 6, , ); STAGEQ(13, 6, , ); STAGEQ(14, 6, , ); STAGEQ(15, 0, , );
    WSL(15); WSL(16); WSL(17);
#pragma unroll
    for (int i = 0; i < 4; ++i) { PIN(ms[i]); PIN(cc[i]); }
    PIN(st2[0]); PIN(st2[1]);
    STMP(2); STMC(41);
    u4 pf[4][2];
#pragma unroll
    for (int i = 0; i < 4; ++i) {
      const unsigned m = ms[i][0] ^ 0x80000000u, pp = (m & 0xFFFF0000u) | (m >> 16);
#pragma unroll
      for (int jj = 0; jj < 4; ++jj) c[i][jj] = MFMAH(cc[jj], pp, c[i][jj]);
    }
#pragma unroll
    for (int i = 0; i < 4; ++i) {
      const float rs = U2F(ms[i][1]) * 1.44269504f;
#pragma unroll
      for (int hh = 0; hh < 2; ++hh) {
#pragma unroll
        for (int e = 0; e < 4; ++e) pf[i][hh][e] = pk2(phi1(c[i][2 * hh + (e >> 1)][(e & 1) * 2] * rs), phi1(c[i][2 * hh + (e >> 1)][(e & 1) * 2 + 1] * rs));
        PIN(pf[i][hh]);
      }
    }
    STMP(19);
    if (KD) PACE(KD);
    {
      const unsigned sb = WS_KVP + (qg * 4 + (w & 3)) * 8 * KVS + (w >> 2) * 1024 + l * 16;
      const unsigned sz = w < 4 ? WS_KVP + ((qg * 4 + w) * 8 + (np >> 1)) * KVS + 2048 + (np & 1) * 64 + g * 16 : WS_OFF;
      u4 prm[2];
#pragma unroll
      for (int e = 0; e < 4; ++e) {
        const unsigned o = (np >> 2) == g ? ((np & 1) ? 0x3F800000u : 0x00003F80u) : 0u;
        prm[0][e] = e == ((np >> 1) & 1) ? o : 0u; prm[1][e] = e == 2 + ((np >> 1) & 1) ? o : 0u;
      }
      u4 v[8], zv; f4 kg[2][2];
      unsigned pend = 7;
#pragma nounroll
      for (unsigned it = 0; it < SPIN_CAP; ++it) {
        CLOB();
#pragma unroll
        for (int gq = 0; gq < 2; ++gq)
          if (pend & (1u << gq)) {
#pragma unroll
            for (int p = 4 * gq; p < 4 * gq + 4; ++p) v[p] = __builtin_amdgcn_raw_buffer_load_b128(wrs, sb, p * KVS, 16);
          }
        if (pend & 4u) zv = LD128(sz);
        SBAR();
#pragma unroll
        for (int gq = 0; gq < 2; ++gq)
          if (pend & (1u << gq)) {
            unsigned mx = 0; f4 t0_ = (f4)(0.f), t1_ = (f4)(0.f);
#pragma unroll
            for (int p = 4 * gq; p < 4 * gq + 4; ++p) { mx = mx4(mx, v[p]); t0_ = MFMA16(BC8(prm[0]), BC8(v[p]), t0_); t1_ = MFMA16(BC8(prm[1]), BC8(v[p]), t1_); }
            kg[gq][0] = t0_; kg[gq][1] = t1_;
            if (clean(mx)) pend &= ~(1u << gq);
          }
        if ((pend & 4u) && clean(mx4(0u, zv))) pend &= ~4u;
        if (!pend) break;
        if (KSL) __builtin_amdgcn_s_sleep(KSL);
      }
      STMP(4);
      const f4 k0 = kg[0][0] + kg[1][0], k1 = kg[0][1] + kg[1][1];
      *(u4*)(lds + L_KX + ((w & 3) * 2 + (w >> 2)) * 1024 + l * 16) = (u4){pk2(k0[0], k0[1]), pk2(k0[2], k0[3]), pk2(k1[0], k1[1]), pk2(k1[2], k1[3])};
      if (w < 4) {
        f4 z = __builtin_bit_cast(f4, zv);
#pragma unroll
        for (int e = 0; e < 4; ++e) { z[e] += DPPF(z[e], 0x122, 0xf); z[e] += DPPF(z[e], 0x124, 0xf); z[e] += DPPF(z[e], 0x128, 0xf); }
        if (np < 2) *(u2*)(lds + L_ZX + w * 64 + g * 16 + np * 8) = (u2){pk2(z[0], z[1]), pk2(z[2], z[3])};
      } else if (w == 7) {
        if (l < 8) ((unsigned*)(lds + L_TAG + 16))[l] = f[0];
        if (l == 8) *(u2*)(lds + L_TAG) = f;
        if (l < 8) ((unsigned*)(lds + L_CNT))[l] = 0u;
      }
      LBAR();
    }
    STMP(22);
#pragma unroll
    for (int hh = 0; hh < 2; ++hh) {
      const u4 kA0 = *(const u4*)(lds + L_KX + (2 * hh + wn) * 2048 + l * 16), kA1 = *(const u4*)(lds + L_KX + (2 * hh + wn) * 2048 + 1024 + l * 16);
      const u4 zA = *(const u4*)(lds + L_ZX + (2 * hh + wn) * 64 + g * 16);
#pragma unroll
      for (int i = 0; i < 4; ++i) {
        const f4 dn = MFMA16(BC8(zA), BC8(pf[i][hh]), (f4)(0.f));
        const f4 x0 = MFMA16(BC8(kA0), BC8(pf[i][hh]), (f4)(0.f)), x1 = MFMA16(BC8(kA1), BC8(pf[i][hh]), (f4)(0.f));
        const float inv = __builtin_amdgcn_rcpf(rbf(rbf(dn[0]) + 1e-6f));
        ST128(((u4){pk2(x0[0] * inv, x0[1] * inv), pk2(x0[2] * inv, x0[3] * inv), pk2(x1[0] * inv, x1[1] * inv), pk2(x1[2] * inv, x1[3] * inv)}),
              WS_OF + ((rg * 16 + wm * 4 + i) * 32 + qg * 4 + 2 * hh + wn) * 1024 + l * 16);
      }
    }
    STMP(23);
    SBAR();
    WAITV(0);
    ST128(((u4){tlo, thi, tlo, thi}), l < 8 ? ofl : WS_OFF);
    SBAR();
    STMP(5);
  } else {
    const int hc = (j - 24) * 8 + xcd, m16 = (xcd & 1) * 8 + j - 24;
    const bool moe = hc < 56;
    u4 cw[12];
    { const u16* p = wqkv + (size_t)((m16 & 3) * 256 + w * 32 + rr) * 3072 + (xq * 4 + (m16 >> 2)) * 64 + sl * 8;
#pragma unroll
      for (int i = 0; i < 12; ++i) cw[i] = *(const u4*)(p + (size_t)(i & 3) * 8 * 3072 + ((i >> 2) + 1) % 3 * 1024); }
    u2 ep = LD64(WS_EP);
    SBAR();
    u4 gw[4][8], dw[2][8];
    const u16* wp = moe ? ((np >> 3) ? wu : wg) + (size_t)(128 * w + 8 * g) * F + 64 * hc + 8 * sl : wr + l * 8;
    const u16* dp = moe ? wd + (size_t)(64 * hc + 8 * g) * D + 128 * w + 8 * np : wr + l * 8;
    const unsigned fs = moe ? F : 0, ds = moe ? D : 0;
#define GL(b) do { _Pragma("unroll") for (int i_ = 0; i_ < 8; ++i_) gw[b][i_] = NTL((const u4*)(wp + (size_t)((32 * (b) + i_) * fs))); SBAR(); } while (0)
#define DL(b) do { _Pragma("unroll") for (int i_ = 0; i_ < 8; ++i_) dw[b][i_] = NTL((const u4*)(dp + (size_t)((32 * (b) + i_) * ds))); SBAR(); } while (0)
    GL(0);
    float* red = (float*)(lds + L_RED);
    {
      u4 sel8;
      const unsigned one = (np & 1) ? 0x3F800000u : 0x00003F80u;
#pragma unroll
      for (int e = 0; e < 4; ++e) sel8[e] = ((np >> 1) == e) ? one : 0u;
#pragma unroll
      for (int p = 0; p < 3; ++p) {
        f4 c = (f4)(0.f);
#pragma unroll
        for (int i = 0; i < 4; ++i) c = MFMA16(BC8(sel8), BC8(cw[p * 4 + i]), c);
#pragma unroll
        for (int e = 0; e < 4; ++e) c[e] += DPPF(c[e], 0x128, 0xf);
        if (g < 2 && np < 8) *(f4*)(red + (w * 3 + p) * 64 + 8 * np + 4 * g) = c;
      }
      if (t0 < 8) ((unsigned*)(lds + L_CNT))[l] = 0u;
    }
    PIN(ep);
    const u64 tag = (((u64)RFL(ep[1]) << 32) | RFL(ep[0])) + TAG_STEP;
    const unsigned tlo = (unsigned)tag, thi = (unsigned)(tag >> 32);
    const u2 tgv = (u2){tlo, thi};
    LBAR();
    if (w == 0) {
      const int lc = min(l, 47);
      f4 c = *(const f4*)(red + lc * 4);
#pragma unroll
      for (int i = 1; i < 8; ++i) c += *(const f4*)(red + i * 192 + lc * 4);
      u4 o;
#pragma unroll
      for (int e = 0; e < 4; ++e) { const float h_ = rbf(c[e]); o[e] = pk2(h_, c[e] - h_); }
      ST128(o, l < 48 ? WS_CS + ((m16 & 3) * 3072 + (g + 1) % 3 * 1024 + (xq * 4 + (m16 >> 2)) * 64 + np * 4) * 4 : WS_OFF);
    }
    SBAR();
    STMP(17);
    GL(1);
    if (w == 0) { WAITV(8); ST64(tgv, l == 0 ? WS_FLAG + gb * 8 : WS_OFF); STMP(8); }
    SBAR();
    if (hc == 56 && (w == 1 || w == 2)) {
      u4 rv[8];
      const unsigned ra = WS_R + ((w - 1) * 1024 + l * 2) * 8;
      PACE(MD1);
#pragma unroll
      for (int i = 0; i < 8; ++i) rv[i] = LD128(ra + i * 1024);
      SBAR();
#pragma nounroll
      for (unsigned it = 0; it < SPIN_CAP; ++it) {
        unsigned bad = 0;
#pragma unroll
        for (int i = 0; i < 8; ++i) bad |= ((((rv[i][0] ^ tlo) | (rv[i][2] ^ tlo)) & 0xFFFFu) | (rv[i][1] ^ thi) | (rv[i][3] ^ thi)) ? 1u << i : 0u;
        if (all64(bad == 0u)) break;
        __builtin_amdgcn_s_sleep(4);
        CLOB();
#pragma unroll
        for (int i = 0; i < 8; ++i) {
          const u4 v = LD128((bad >> i) & 1u ? ra + i * 1024 : WS_OFF);
          rv[i] = (bad >> i) & 1u ? v : rv[i];
        }
      }
      unsigned key[16];
#pragma unroll
      for (int i = 0; i < 16; ++i) {
        unsigned b = rv[i >> 1][(i & 1) * 2] >> 16;
        unsigned mono = (b & 0x8000u) ? (~b & 0xFFFFu) : (b | 0x8000u);
        key[i] = (mono << 16) | (unsigned)(0xFFFF - ((w - 1) * 1024 + (i >> 1) * 128 + l * 2 + (i & 1)));
      }
      unsigned* cand = (unsigned*)(lds + L_TOP);
#define CE(i, j) do { const unsigned hi_ = max(key[i], key[j]), lo_ = min(key[i], key[j]); key[i] = hi_; key[j] = lo_; } while (0)
#define SORT8(o) do { CE(o + 0, o + 2); CE(o + 1, o + 3); CE(o + 4, o + 6); CE(o + 5, o + 7); CE(o + 0, o + 4); CE(o + 1, o + 5); CE(o + 2, o + 6); CE(o + 3, o + 7); \
    CE(o + 0, o + 1); CE(o + 2, o + 3); CE(o + 4, o + 5); CE(o + 6, o + 7); CE(o + 2, o + 4); CE(o + 3, o + 5); CE(o + 1, o + 4); CE(o + 3, o + 6); CE(o + 1, o + 2); CE(o + 3, o + 4); CE(o + 5, o + 6); } while (0)
      SORT8(0); SORT8(8);
#pragma unroll
      for (int i = 0; i < 8; ++i) key[i] = max(key[i], key[15 - i]);
      CE(0, 4); CE(1, 5); CE(2, 6); CE(3, 7); CE(0, 2); CE(1, 3); CE(4, 6); CE(5, 7); CE(0, 1); CE(2, 3); CE(4, 5); CE(6, 7);
      unsigned cv = 0;
#pragma unroll
      for (int it = 0; it < 8; ++it) {
        const unsigned m = wave_max(key[0]);
        const bool win = key[0] == m;
#pragma unroll
        for (int i = 0; i < 7 - it; ++i) key[i] = win ? key[i + 1] : key[i];
        cv = sl == it ? m : cv;
      }
      LAS unsigned* cnt = (LAS unsigned*)(lds + L_CNT) + 1;
      if (w == 1) {
        if (l < 8) cand[l] = cv;
        lds_arrive(cnt);
      } else {
        lds_wait<1>(cnt, 1);
        u2 t; t[0] = max(cv, cand[7 - sl]); t[1] = tlo;
        ST64(t, WS_MSG + rr * 256 + 64 + sl * 8);
        STMP(29);
      }
    }
    if (hc == 57 && w == 1) {
      PACE(MD2);
      bool ok = false;
#pragma nounroll
      for (unsigned it = 0; it < SPIN_CAP; ++it) {
        if (!ok) {
          u4 f0 = LD128(WS_FLAG + l * 32), f1 = LD128(WS_FLAG + l * 32 + 16);
          ok = f0[0] == tlo && f0[1] == thi && f0[2] == tlo && f0[3] == thi && f1[0] == tlo && f1[1] == thi && f1[2] == tlo && f1[3] == thi;
        }
        if (all64(ok)) break;
        __builtin_amdgcn_s_sleep(8);
        CLOB();
      }
      CLOB();
      if (l < 8) ST64(tgv, WS_MSG + l * 256);
      if (l == 0) ST64(tgv, WS_EP);
      STMP(30);
    }
    GL(2);
    u4 gf[4][8];
#define GT(b) _Pragma("unroll") for (int c_ = 0; c_ < 8; ++c_) { TRFRAG(gf[b][c_], gw[b], c_); PIN(gf[b][c_]); }
    GT(0);
    GL(3);
    GT(1);
    if (w == 0) {
      if (!moe) PACE(100);
      u2 f;
#pragma nounroll
      for (unsigned it = 0; it < SPIN_CAP; ++it) {
        f = LD64(msg);
        if (MSGOK(f)) break;
        __builtin_amdgcn_s_sleep(8);
        CLOB();
      }
      CLOB();
      if (l < 8) ((unsigned*)(lds + L_TAG + 16))[l] = f[0];
      if (l == 8) *(u2*)(lds + L_TAG) = f;
    }
    LBAR();
    STMP(9);
    if (moe) {
      const int idx = (0xFFFF - (int)(((const unsigned*)(lds + L_TAG + 16))[sl] & 0xFFFF)) & 2047;
      const u16* xp = x + idx * D + 128 * w + 32 * (np >> 3) + 8 * g;
      const u4 r0 = *(const u4*)xp, r1 = *(const u4*)(xp + 64);
      const u2 msx = LD64(WS_MS + idx * 8);
      SBAR();
      DL(0);
      GT(2); GT(3);
      DL(1);
      f4 acc[4];
#pragma unroll
      for (int c2 = 0; c2 < 4; ++c2) acc[c2] = (f4)(0.f);
      const float m = U2F(msx[0]), rs = U2F(msx[1]);
#pragma unroll
      for (int b2 = 0; b2 < 2; ++b2) {
        u4 xl[2], xh[2];
#pragma unroll
        for (int e = 0; e < 4; ++e) {
          const unsigned r = b2 ? r1[e] : r0[e];
          const unsigned v = pk2((lo(r) - m) * rs, (hi(r) - m) * rs);
          const unsigned o = (unsigned)__builtin_amdgcn_update_dpp(0, (int)v, 0x128, 0xf, 0xf, false);
          xl[0][e] = np < 8 ? v : 0u; xh[0][e] = np < 8 ? 0u : o; xl[1][e] = np < 8 ? o : 0u; xh[1][e] = np < 8 ? 0u : v;
        }
#pragma unroll
        for (int it = 0; it < 2; ++it)
#pragma unroll
          for (int c = 0; c < 8; ++c) acc[c >> 1] = MFMA16(BC8((c & 1) ? xh[it] : xl[it]), BC8(gf[2 * b2 + it][c]), acc[c >> 1]);
      }
      STMP(12);
#pragma unroll
      for (int c2 = 0; c2 < 4; ++c2) *(f4*)(red + (w * 4 + c2) * 256 + l * 4) = acc[c2];
      LBAR();
      f2 sv = *(const f2*)(red + (w >> 1) * 256 + l * 4 + (w & 1) * 2);
#pragma unroll
      for (int i = 1; i < 8; ++i) sv += *(const f2*)(red + (i * 4 + (w >> 1)) * 256 + l * 4 + (w & 1) * 2);
      u16* hb = (u16*)(lds + L_H);
#pragma unroll
      for (int e = 0; e < 2; ++e) {
        const float gg = rbf(sv[e]), u1 = DPPF(gg, 0x128, 0xf);
        const float hv = rbf(gg * __builtin_amdgcn_rcpf(1.f + __builtin_amdgcn_exp2f(gg * -1.44269504f))) * u1;
        if (np < 8) hb[(4 * (g & 1) + 2 * (w & 1) + e) * 64 + 8 * np + 2 * (w >> 1) + (g >> 1)] = (u16)(pk2(hv, hv) & 0xFFFFu);
      }
      LBAR();
      f4 acd[4];
#pragma unroll
      for (int c = 0; c < 4; ++c) acd[c] = (f4)(0.f);
#pragma unroll
      for (int fb = 0; fb < 2; ++fb) {
        const u4 ha = *(const u4*)(hb + sl * 64 + 32 * fb + 8 * g);
        u4 hl, hh;
#pragma unroll
        for (int e = 0; e < 4; ++e) { hl[e] = np < 8 ? ha[e] : 0u; hh[e] = np < 8 ? 0u : ha[e]; }
#pragma unroll
        for (int c = 0; c < 4; ++c) {
          u4 d0, d1; TRFRAG(d0, dw[fb], c); TRFRAG(d1, dw[fb], c + 4);
          acd[c] = MFMA16(BC8(hh), BC8(d1), MFMA16(BC8(hl), BC8(d0), acd[c]));
        }
      }
#pragma unroll
      for (int e = 0; e < 4; ++e)
        ST128(tag4((f4){acd[0][e], acd[1][e], acd[2][e], acd[3][e]}, tlo), WS_YP + (hc * 8 + 4 * (g & 1) + e) * 4096 + w * 512 + (g >> 1) * 256 + np * 16);
      STMP(14);
      LBAR();
    }
    WO_SETUP();
    st2[0] = LD64(sto); st2[1] = LD64(sto + 128);
    SBAR();
    WO_ALL();
    WAITV(0); BAR();
    STMP(20);
  }

  const u2 tg = *(const u2*)(lds + L_TAG);
  LAS unsigned* cnt = (LAS unsigned*)(lds + L_CNT);
  if (w >= 4) {
    const unsigned mykey = ((const unsigned*)(lds + L_TAG + 16))[sl], tlo = RFL(tg[0]);
#pragma unroll 1
    for (int i = w - 4; i < 8; i += 4) {
      const unsigned kk = (unsigned)__builtin_amdgcn_readlane((int)mykey, i);
      const int ti = 0xFFFF - (int)(kk & 0xFFFF);
      if ((ti >> 7) != mb2) continue;
      const unsigned yb = WS_YP + (g * 14 * 8 + i) * 4096 + (nb >> 1) * 512 + (np >> 3) * 256 + ((nb & 1) * 8 + sl) * 16;
      f4 y;
#pragma nounroll
      for (unsigned it = 0; it < SPIN_CAP; ++it) {
        u4 yv[14];
#pragma unroll
        for (int k = 0; k < 14; ++k) yv[k] = LD128(yb + k * 32768);
        SBAR();
        unsigned bad = 0;
        y = untag4(yv[0], tlo, bad);
#pragma unroll
        for (int k = 1; k < 14; ++k) y += untag4(yv[k], tlo, bad);
        if (tagok(bad)) break;
        __builtin_amdgcn_s_sleep(16);
        CLOB();
      }
#pragma unroll
      for (int e = 0; e < 4; ++e) {
        auto a16 = __builtin_amdgcn_permlane16_swap(F2U(y[e]), F2U(y[e]), false, false);
        y[e] = U2F(a16[0]) + U2F(a16[1]);
        auto a32 = __builtin_amdgcn_permlane32_swap(F2U(y[e]), F2U(y[e]), false, false);
        y[e] = U2F(a32[0]) + U2F(a32[1]);
      }
      const unsigned mono = kk >> 16;
      const float rv = U2F(((mono & 0x8000u) ? (mono & 0x7FFFu) : (~mono & 0xFFFFu)) << 16);
      const float sg = rbf(__builtin_amdgcn_rcpf(1.f + __builtin_amdgcn_exp2f(rv * -1.44269504f)));
#pragma unroll
      for (int e = 0; e < 4; ++e) y[e] = rbf(rbf(y[e]) * sg);
      if (g == 0) *(f4*)(lds + L_YS + i * 256 + sl * 32 + (np >> 3) * 16) = y;
    }
    lds_arrive(cnt);
    STMP(32);
    STDUMP();
    return;
  }
  f4 oc[2][4];
  int sel[2] = {-1, -1};
  char* xt = lds + L_XT + (w * 32 + np) * 128 + (g & 1) * 8;
#define XT(r, ct) (xt + (r) * 2048 + (((2 * (ct) + (g >> 1)) ^ sl) * 16))
  {
    const u4 k0 = *(const u4*)(lds + L_TAG + 16), k1 = *(const u4*)(lds + L_TAG + 32);
#pragma unroll
    for (int r = 0; r < 2; ++r) {
      const float c1 = 1.f + U2F(st2[r][1]), c0 = -U2F(st2[r][0]) * U2F(st2[r][1]);
#pragma unroll
      for (int ct = 0; ct < 4; ++ct) {
        const u2 v = *(const u2*)XT(r, ct);
        oc[r][ct] = (f4){__builtin_fmaf(lo(v[0]), c1, c0), __builtin_fmaf(hi(v[0]), c1, c0), __builtin_fmaf(lo(v[1]), c1, c0), __builtin_fmaf(hi(v[1]), c1, c0)};
        PIN(oc[r][ct]);
      }
      const unsigned want = 0xFFFFu - (unsigned)(mb2 * 128 + w * 32 + r * 16 + np);
#pragma unroll
      for (int i = 0; i < 4; ++i) { if ((k0[i] & 0xFFFFu) == want) sel[r] = i; if ((k1[i] & 0xFFFFu) == want) sel[r] = 4 + i; }
      PIN(sel[r]);
    }
  }
  unsigned woad[2][4];
  { const int q = np >> 2, p = l & 3, row = 4 * g + q;
#pragma unroll
    for (int ct = 0; ct < 4; ++ct) { woad[0][ct] = ldsb + L_WO + row * 128 + ((ct ^ ((row >> 1) & 3)) * 32) + p * 8; woad[1][ct] = woad[0][ct] + 65536; } }
  s4 b[2][16];
#define K3RD(hc, i) RDTR(b[(hc) & 1][i], woad[(hc) >> 3][((i) >> 1) & 3], ((((hc) & 7) * 2 + ((i) >> 3)) * 4096 + ((i) & 1) * 2048))
  K3RD(0, 0); K3RD(0, 1); K3RD(0, 2); K3RD(0, 3); K3RD(0, 4); K3RD(0, 5); K3RD(0, 6); K3RD(0, 7);
  K3RD(0, 8); K3RD(0, 9); K3RD(0, 10); K3RD(0, 11); K3RD(0, 12); K3RD(0, 13); K3RD(0, 14); K3RD(0, 15);
  const unsigned flg = l < 16 ? WS_OFL + ((mb2 * 2 + (w >> 1)) * 16 + (np ^ (hx >> 1))) * 128 : WS_OFF;
  if (j >= 16 && j < 24) PACE(PD5);
  STMP(26);
  unsigned rdy = 0;
#define OPOLL(MASK, SL) _Pragma("nounroll") for (unsigned it = 0; it < SPIN_CAP && (rdy & (MASK)) != (MASK); ++it) { \
    u2 f_ = LD64(((rdy >> np) & 1u) ? WS_OFF : flg); \
    rdy |= (unsigned)__builtin_amdgcn_ballot_w64(f_[0] == tg[0] && f_[1] == tg[1]); \
    if ((SL) && rdy == 0) __builtin_amdgcn_s_sleep(SL); \
    CLOB(); }
  OPOLL(0x00FFu, OSL);
  CLOB();
  STMP(6);
#define OPF 18
#define OW0 6
#define OW 12
  u4 a[32]; u2 fl;
  const unsigned abase = WS_OF + (mb2 * 8 + w * 2) * 32768 + l * 16, abase1 = abase + 32768;
#define OLD(f) do { if ((f) < 64) { if ((f) == 32 && rdy != 0xFFFFu) { rdy |= (unsigned)__builtin_amdgcn_ballot_w64(fl[0] == tg[0] && fl[1] == tg[1]); OPOLL(0xFFFFu, 0); CLOB(); } \
    a[(f) & 31] = __builtin_amdgcn_raw_buffer_load_b128(wrs, (((f) & 1) ? abase1 : abase) + (((f) >> 1) & 3) * 1024, (((f) >> 3) * 4096) ^ (hx * 1024), 16); if ((f) == OPF) fl = LD64(((rdy >> np) & 1u) ? WS_OFF : flg); } } while (0)
#pragma unroll
  for (int f = 0; f < OW0; ++f) OLD(f);
#define OHI(c) ((OW0 + 2 * (c)) < (OW + (c)) ? (OW0 + 2 * (c)) : (OW + (c)))
#define K3R(i) (((i) ^ ((i) >> 1)) & 1)
#define K3C(hc, i) ((hc) * 4 + ((i) >> 2))
#define K3M(hc, i) do { if (!((i) & 1)) WAITL((hc) < 15 ? 14 : 14 - (i)); SBAR(); \
    oc[K3R(i)][((i) >> 1) & 3] = MFMA16(CAT(b[(hc) & 1][(i) & ~1], b[(hc) & 1][(i) | 1]), BC8(a[((hc) * 4 + ((i) >> 3) * 2 + K3R(i)) & 31]), oc[K3R(i)][((i) >> 1) & 3]); SBAR(); \
    if ((hc) < 15) { K3RD((hc) + 1, i); } \
    if (((i) & 3) == 3) { OLD(OHI(K3C(hc, i) + 1) - 1); } else if (((i) & 3) == 1 && OHI(K3C(hc, i) + 1) - OHI(K3C(hc, i)) == 2) { OLD(OHI(K3C(hc, i))); } \
    SBAR(); } while (0)
#define K3H(hc) do { K3M(hc, 0); K3M(hc, 1); K3M(hc, 2); K3M(hc, 3); K3M(hc, 4); K3M(hc, 5); K3M(hc, 6); K3M(hc, 7); \
    K3M(hc, 8); K3M(hc, 9); K3M(hc, 10); K3M(hc, 11); K3M(hc, 12); K3M(hc, 13); K3M(hc, 14); K3M(hc, 15); } while (0)
  K3H(0); STMP(27); K3H(1); K3H(2); K3H(3); K3H(4); K3H(5); K3H(6); K3H(7); STMP(31); K3H(8); K3H(9); K3H(10); K3H(11); K3H(12); K3H(13); K3H(14); K3H(15);
  STMP(7);
  if (!all64(sel[0] < 0 && sel[1] < 0)) {
    lds_wait<0>(cnt, 4);
#pragma unroll
    for (int r = 0; r < 2; ++r)
      if (sel[r] >= 0) {
#pragma unroll
        for (int ct = 0; ct < 4; ++ct) oc[r][ct] += *(const f4*)(lds + L_YS + sel[r] * 256 + ct * 64 + g * 16);
      }
  }
#pragma unroll
  for (int r = 0; r < 2; ++r)
#pragma unroll
    for (int ct = 0; ct < 4; ++ct) *(u2*)XT(r, ct) = (u2){pk2(oc[r][ct][0], oc[r][ct][1]), pk2(oc[r][ct][2], oc[r][ct][3])};
  {
    const __amdgpu_buffer_rsrc_t ors = __builtin_amdgcn_make_buffer_rsrc(out, 0, S * D * 2, 0x00020000);
#pragma unroll
    for (int i = 0; i < 4; ++i) {
      const u4 v = *(const u4*)(lds + L_XT + (w * 32 + 8 * i + rr) * 128 + ((sl ^ rr) * 16));
      __builtin_amdgcn_raw_buffer_store_b128(v, ors, (mb2 * 128 + w * 32 + 8 * i + rr) * 2048 + nb * 128 + sl * 16, 0, 16);
    }
  }
  STMP(16);
  STDUMP();
}

extern "C" void kernel_launch(void* const* d_in, const int* in_sizes, int n_in, void* d_out, int out_size,
                              void* d_ws, size_t ws_size, hipStream_t stream) {
  const u16* x = (const u16*)d_in[0];
  const u16* wqkv = (const u16*)d_in[1];
  const u16* wo = (const u16*)d_in[2];
  const u16* wr = (const u16*)d_in[4];
  const u16* wg = (const u16*)d_in[5];
  const u16* wu = (const u16*)d_in[6];
  const u16* wd = (const u16*)d_in[7];
  (void)hipFuncSetAttribute((const void*)fused, hipFuncAttributeMaxDynamicSharedMemorySize, K_LDS);
  fused<<<256, 512, K_LDS, stream>>>(x, wqkv, wo, wr, wg, wu, wd, (char*)d_ws, (u16*)d_out);
}
```

```cpp
#include <hip/hip_runtime.h>
#include <hip/hip_bf16.h>
#include <stdint.h>

typedef unsigned short u16;
typedef unsigned long long u64;
typedef short s4 __attribute__((ext_vector_type(4)));
typedef short s8 __attribute__((ext_vector_type(8)));
typedef float f4 __attribute__((ext_vector_type(4)));
typedef float f2 __attribute__((ext_vector_type(2)));
typedef unsigned u4 __attribute__((ext_vector_type(4)));
typedef unsigned u2 __attribute__((ext_vector_type(2)));
typedef __bf16 bf2 __attribute__((ext_vector_type(2)));
#define LAS __attribute__((address_space(3)))
#define DEV __device__ __forceinline__

DEV float U2F(unsigned u) { return __builtin_bit_cast(float, u); }
DEV unsigned F2U(float f) { return __builtin_bit_cast(unsigned, f); }
DEV float I2F(int u) { return __builtin_bit_cast(float, u); }
DEV int F2I(float f) { return __builtin_bit_cast(int, f); }

constexpr int S = 2048, D = 1024, F = 3584;

constexpr unsigned WS_EP = 0;
constexpr unsigned WS_FLAG = 4096;
constexpr unsigned WS_MSG = 6144;
constexpr unsigned WS_MS = 32768;
constexpr unsigned WS_CS = 256u << 10;
constexpr unsigned WS_GP = 512u << 10;
constexpr unsigned WS_H = 640u << 10;
constexpr unsigned WS_YP = 1280u << 10;
constexpr unsigned WS_KVP = 3328u << 10;
constexpr unsigned WS_OF = 6u << 20;
constexpr unsigned WS_OFL = 10u << 20;
constexpr unsigned WS_END = (10u << 20) + (128u << 10);
constexpr unsigned WS_OFF = 0x7FFF0000u;
constexpr unsigned KVS = 2304;
constexpr unsigned POISON = 0xFFFFFFFFu;

DEV unsigned pk2(float a, float b) { bf2 v; v[0] = (__bf16)a; v[1] = (__bf16)b; return __builtin_bit_cast(unsigned, v); }
DEV float lo(unsigned u) { return U2F(u << 16); }
DEV float hi(unsigned u) { return U2F(u & 0xFFFF0000u); }
DEV float rbf(float a) { return lo(pk2(a, a)); }
DEV float add1(float a, float b) { float r; asm("v_add_f32 %0, %1, %2" : "=v"(r) : "v"(a), "v"(b)); return r; }
DEV float mul1(float a, float b) { float r; asm("v_mul_f32 %0, %1, %2" : "=v"(r) : "v"(a), "v"(b)); return r; }
DEV unsigned san(unsigned u) { return min(u, 0xFFFFFFFEu); }
DEV u4 san4(u4 v) { v[0] = san(v[0]); v[1] = san(v[1]); v[2] = san(v[2]); v[3] = san(v[3]); return v; }
DEV unsigned mx3(unsigned a, unsigned b, unsigned c) { return max(max(a, b), c); }
DEV unsigned mx4(unsigned m, u4 v) { return mx3(mx3(m, v[0], v[1]), v[2], v[3]); }
DEV u4 tag4(f4 v, unsigned t) { u4 r; _Pragma("unroll") for (int e = 0; e < 4; ++e) r[e] = (F2U(v[e]) & 0xFFFFFF00u) | ((t >> (8 * e)) & 0xFFu); return r; }
DEV f4 untag4(u4 v, unsigned t, unsigned& bad) {
  f4 r; _Pragma("unroll") for (int e = 0; e < 4; ++e) { const unsigned u = v[e] ^ ((t >> (8 * e)) & 0xFFu); bad |= u; r[e] = U2F(u); } return r; }
DEV bool tagok(unsigned bad) { return __builtin_amdgcn_ballot_w64((bad & 0xFFu) != 0u) == 0ull; }
DEV bool clean(unsigned m) { return __builtin_amdgcn_ballot_w64(m == POISON) == 0ull; }
DEV bool all64(bool c) { return __builtin_amdgcn_ballot_w64(!c) == 0ull; }
DEV float phi1(float a) { return __builtin_fmaf(fmaxf(a, 1.44269504e-6f), 0.693147181f, __builtin_amdgcn_fmed3f(__builtin_amdgcn_exp2f(a), 0.f, 1.f)); }
DEV unsigned phi2(f2 a) {
  const f2 e = (f2){__builtin_amdgcn_fmed3f(__builtin_amdgcn_exp2f(a[0]), 0.f, 1.f), __builtin_amdgcn_fmed3f(__builtin_amdgcn_exp2f(a[1]), 0.f, 1.f)};
  const f2 r = __builtin_elementwise_fma((f2){fmaxf(a[0], 1.44269504e-6f), fmaxf(a[1], 1.44269504e-6f)}, (f2)(0.693147181f), e);
  return pk2(r[0], r[1]);
}
#define RD128(dst, addr, OFF) asm volatile("ds_read_b128 %0, %1 offset:%2" : "=v"(dst) : "v"(addr), "i"(OFF) : "memory")
#define RD64(dst, addr, OFF) asm volatile("ds_read_b64 %0, %1 offset:%2" : "=v"(dst) : "v"(addr), "i"(OFF) : "memory")
#define RD32(dst, addr, OFF) asm volatile("ds_read_b32 %0, %1 offset:%2" : "=v"(dst) : "v"(addr), "i"(OFF) : "memory")
#define RDTR(dst, addr, OFF) asm volatile("ds_read_b64_tr_b16 %0, %1 offset:%2" : "=v"(dst) : "v"(addr), "i"(OFF) : "memory")
#define SBAR() __builtin_amdgcn_sched_barrier(0)
#define CAT(x, y) __builtin_shufflevector(x, y, 0, 1, 2, 3, 4, 5, 6, 7)
#define WAITV(n) asm volatile("s_waitcnt vmcnt(%0)" ::"i"(n) : "memory")
#define WAITL(n) asm volatile("s_waitcnt lgkmcnt(%0)" ::"i"(n) : "memory")
#define CLOB() asm volatile("" ::: "memory")
#define BAR() asm volatile("s_barrier" ::: "memory")
#define LBAR() asm volatile("s_waitcnt lgkmcnt(0)\n s_barrier" ::: "memory")
#define PIN(v) asm volatile("" : "+v"(v))
#define PACE(n) do { SBAR(); __builtin_amdgcn_s_sleep(n); SBAR(); } while (0)
#define RFL(v) ((unsigned)__builtin_amdgcn_readfirstlane(v))
DEV const char* uptr(const char* p) {
  u64 v = (u64)p;
  return (const char*)(((u64)RFL((unsigned)(v >> 32)) << 32) | RFL((unsigned)v));
}
DEV u4 mkrs(const char* p, unsigned bytes) {
  const u64 v = (u64)uptr(p);
  u4 r; r[0] = (unsigned)v; r[1] = (unsigned)(v >> 32) & 0xFFFFu; r[2] = RFL(bytes); r[3] = 0x00020000u;
  return r;
}
#define BLDS(voff, rs, so, OFF) asm volatile("buffer_load_dwordx4 %0, %1, %2 offen offset:%3 lds" ::"v"(voff), "s"(rs), "s"(so), "i"(OFF) : "memory")
#define MFMA16(a, b, c) __builtin_amdgcn_mfma_f32_16x16x32_bf16(a, b, c, 0, 0, 0)
#define MFMAH(a, b, c) __builtin_amdgcn_mfma_f32_16x16x16bf16_1k(__builtin_bit_cast(s4, (u2){(a), 0u}), __builtin_bit_cast(s4, (u2){(b), 0u}), c, 0, 0, 0)
#define DOT2(a, b, c) __builtin_amdgcn_fdot2_f32_bf16(__builtin_bit_cast(bf2, (unsigned)(a)), __builtin_bit_cast(bf2, (unsigned)(b)), c, false)
#define BC8(v) __builtin_bit_cast(s8, v)
#define LD128(off) __builtin_amdgcn_raw_buffer_load_b128(wrs, (off), 0, 16)
#define LD64(off) __builtin_amdgcn_raw_buffer_load_b64(wrs, (off), 0, 16)
#define LD32(off) __builtin_amdgcn_raw_buffer_load_b32(wrs, (off), 0, 16)
#define ST128(v, off) __builtin_amdgcn_raw_buffer_store_b128((v), wrs, (off), 0, 16)
#define ST64(v, off) __builtin_amdgcn_raw_buffer_store_b64((v), wrs, (off), 0, 16)
#define ST32(v, off) __builtin_amdgcn_raw_buffer_store_b32((v), wrs, (off), 0, 16)
#define ALD128(dst, off) asm volatile("buffer_load_dwordx4 %0, %1, %2, 0 offen sc1" : "=v"(dst) : "v"(off), "s"(rsW) : "memory")
#define ALD64(dst, off) asm volatile("buffer_load_dwordx2 %0, %1, %2, 0 offen sc1" : "=v"(dst) : "v"(off), "s"(rsW) : "memory")
#define ALD32(dst, off) asm volatile("buffer_load_dword %0, %1, %2, 0 offen sc1" : "=v"(dst) : "v"(off), "s"(rsW) : "memory")
constexpr unsigned SPIN_CAP = 1u << 20;

DEV void lds_arrive(LAS unsigned* c) {
  asm volatile("s_mov_b64 exec, 1\n ds_add_u32 %0, %1\n s_mov_b64 exec, -1" ::"v"((unsigned)(size_t)c), "v"(1u) : "memory");
}
template <int SL> DEV void lds_wait(LAS unsigned* c, unsigned target) {
#pragma nounroll
  while (RFL(__hip_atomic_load(c, __ATOMIC_RELAXED, __HIP_MEMORY_SCOPE_WORKGROUP)) < target) { if (SL) __builtin_amdgcn_s_sleep(SL); }
  CLOB();
}

#define STMP(idx)
#define STMC(idx)
#define STDUMP()
#define DPPF(v, ctrl, rm) I2F(__builtin_amdgcn_update_dpp(0, F2I(v), ctrl, rm, 0xf, false))
DEV unsigned wave_max(unsigned v) {
  v = max(v, (unsigned)__builtin_amdgcn_update_dpp(0, (int)v, 0x111, 0xf, 0xf, false));
  v = max(v, (unsigned)__builtin_amdgcn_update_dpp(0, (int)v, 0x112, 0xf, 0xf, false));
  v = max(v, (unsigned)__builtin_amdgcn_update_dpp(0, (int)v, 0x114, 0xf, 0xf, false));
  v = max(v, (unsigned)__builtin_amdgcn_update_dpp(0, (int)v, 0x118, 0xf, 0xf, false));
  v = max(v, (unsigned)__builtin_amdgcn_update_dpp(0, (int)v, 0x142, 0xa, 0xf, false));
  v = max(v, (unsigned)__builtin_amdgcn_update_dpp(0, (int)v, 0x143, 0xc, 0xf, false));
  return (unsigned)__builtin_amdgcn_readlane((int)v, 63);
}

constexpr int SLOT = 49152, L_B = 32768;
constexpr int L_EX = SLOT;
constexpr int L_ROWM = 159744;
constexpr int L_ROWS = L_ROWM + 1024;
constexpr int L_COL = L_ROWS + 1024;
constexpr int L_TAG = L_COL + 512;
constexpr int L_CNT = L_TAG + 64;
constexpr int L_TOP = L_CNT + 64;
constexpr int L_XT = 0;
constexpr int L_KX = 16384;
constexpr int L_ZX = L_KX + 8192;
constexpr int L_YS = L_ZX + 256;
constexpr int L_WO = 28672;
constexpr int L_RED = 65536;
constexpr int L_H = L_RED + 45056;
constexpr int K_LDS = 163840;

#define NTL(p) __builtin_nontemporal_load(p)
#define TRFRAG(f, W, c) do { _Pragma("unroll") for (int q_ = 0; q_ < 4; ++q_) { unsigned e_ = W[2 * q_][(c) >> 1], o_ = W[2 * q_ + 1][(c) >> 1]; \
    f[q_] = ((c) & 1) ? __builtin_amdgcn_perm(o_, e_, 0x07060302u) : __builtin_amdgcn_perm(o_, e_, 0x05040100u); } } while (0)
constexpr u64 TAG_STEP = 0x9E3779B97F4A7C15ull;

struct FW { s8 a[8]; s4 b[8]; };
#define RA(G, s, u, i) RD128(G.a[i], aad[((s) % 3) == 2][u], (((s) % 3) & 1) * SLOT + (i) * 2048)
#define RB(G, s, u, n) RDTR(G.b[n], bad[((s) % 3) == 2][((n) >> 1) & 1], (((s) % 3) & 1) * SLOT + ((n) >> 2) * 8192 + (u) * 4096 + ((n) & 1) * 512)
#define FB(G, j) CAT(G.b[2 * (j)], G.b[2 * (j) + 1])
#define RW(G, s, u, n) do { if ((n) < 8) RB(G, s, u, (n) & 7); else RA(G, s, u, ((n) - 8) & 7); SBAR(); } while (0)
#define SNK(n) ((((n) >> 2) & 1) ? 3 - ((n) & 3) : (n) & 3)
#define MW(G, n) do { if (TRN) c[(n) >> 2][SNK(n)] = MFMA16(FB(G, SNK(n)), G.a[(n) >> 2], c[(n) >> 2][SNK(n)]); \
    else c[(n) >> 2][SNK(n)] = MFMA16(G.a[(n) >> 2], FB(G, SNK(n)), c[(n) >> 2][SNK(n)]); SBAR(); } while (0)
#define MFA(c_, a_, b_) asm volatile("v_mfma_f32_16x16x32_bf16 %0, %1, %2, %0" : "+v"(c_) : "v"(a_), "v"(b_))
#define MA(G, n) do { if (TRN) MFA(c[(n) >> 2][SNK(n)], FB(G, SNK(n)), G.a[(n) >> 2]); else MFA(c[(n) >> 2][SNK(n)], G.a[(n) >> 2], FB(G, SNK(n))); SBAR(); } while (0)
#define MW2(Fq, G, sn, un, n) do { MA(Fq, 2 * (n)); if ((n) < 12) RW(G, sn, un, (n) + ((n) + 2) / 3); MA(Fq, 2 * (n) + 1); if ((n) < 12 && (n) % 3 == 0) RW(G, sn, un, (n) + (n) / 3 + 1); } while (0)
#define HALFW(Fq, G, sn, un) do { MW2(Fq, G, sn, un, 0); MW2(Fq, G, sn, un, 1); MW2(Fq, G, sn, un, 2); MW2(Fq, G, sn, un, 3); MW2(Fq, G, sn, un, 4); MW2(Fq, G, sn, un, 5); MW2(Fq, G, sn, un, 6); MW2(Fq, G, sn, un, 7); \
    MW2(Fq, G, sn, un, 8); MW2(Fq, G, sn, un, 9); MW2(Fq, G, sn, un, 10); MW2(Fq, G, sn, un, 11); MW2(Fq, G, sn, un, 12); MW2(Fq, G, sn, un, 13); MW2(Fq, G, sn, un, 14); MW2(Fq, G, sn, un, 15); WAITL(0); SBAR(); } while (0)
#define STAGEW(s) do { HALFW(f0, f1, s, 1); BAR(); SBAR(); HALFW(f1, f0, (s) + 1, 0); } while (0)
#define KLOOP() \
      unsigned aad[2][2], bad[2][2]; \
      { LANE(); const int q = np >> 2, p = l & 3, f = ((q >> 1) & 1) | ((g & 1) << 1); \
        _Pragma("unroll") for (int u = 0; u < 2; ++u) { aad[0][u] = ldsb + (wm * 128 + np) * 128 + (((4 * u + g) ^ (np >> 1)) * 16); aad[1][u] = aad[0][u] + 2 * SLOT; \
          bad[0][u] = ldsb + L_B + (8 * g + q) * 128 + (((2 * wn + u) ^ f) * 32) + p * 8; bad[1][u] = bad[0][u] + 2 * SLOT; \
          PIN(aad[0][u]); PIN(aad[1][u]); PIN(bad[0][u]); PIN(bad[1][u]); } } \
      f4 c[8][4]; \
      _Pragma("unroll") for (int i = 0; i < 8; ++i) _Pragma("unroll") for (int jj = 0; jj < 4; ++jj) { c[i][jj] = (f4)(0.f); PIN(c[i][jj]); } \
      FW f0, f1; \
      BAR(); \
      STMP(1); STMC(40); \
      RW(f0, 0, 0, 0); RW(f0, 0, 0, 1); RW(f0, 0, 0, 8); RW(f0, 0, 0, 2); RW(f0, 0, 0, 3); RW(f0, 0, 0, 4); RW(f0, 0, 0, 5); RW(f0, 0, 0, 6); RW(f0, 0, 0, 7); \
      RW(f0, 0, 0, 9); RW(f0, 0, 0, 10); RW(f0, 0, 0, 11); RW(f0, 0, 0, 12); RW(f0, 0, 0, 13); RW(f0, 0, 0, 14); RW(f0, 0, 0, 15); \
      WAITL(0); SBAR(); \
      STAGEW(0); STAGEW(1); STAGEW(2); STAGEW(3); STAGEW(4); STAGEW(5); STAGEW(6); STAGEW(7); \
      STAGEW(8); STAGEW(9); STAGEW(10); STAGEW(11); STAGEW(12); STAGEW(13); STAGEW(14); \
      unsigned tR, tC; \
      { LANE(); tR = ldsb + L_ROWM + (wm * 128 + (TRN ? np : 4 * g)) * 4; tC = ldsb + L_COL + (wn * 32 + (TRN ? 4 * g : np)) * 4; PIN(tR); PIN(tC); SBAR(); } \
      HALFW(f0, f1, 15, 1); BAR(); SBAR()
#define MT(n) do { MW(f1, n); TB(n); } while (0)
#define KLAST() do { MT(0); MT(1); MT(2); MT(3); MT(4); MT(5); MT(6); MT(7); MT(8); MT(9); MT(10); MT(11); MT(12); MT(13); MT(14); MT(15); \
      MT(16); MT(17); MT(18); MT(19); MT(20); MT(21); MT(22); MT(23); MT(24); MT(25); MT(26); MT(27); MT(28); MT(29); MT(30); MT(31); WAITL(0); } while (0)
#define H2(v, e0) ((f2){(v)[e0], (v)[(e0) + 1]})

#define WQ0 56
#define KD 20
#define KSL 2
#define PD5 8
#define OSL 4
#define MD1 4
#define MD2 30
#define TKP 11
#define CL0 4

__global__ __launch_bounds__(512, 2) void fused(const u16* __restrict__ x, const u16* __restrict__ wqkv, const u16* __restrict__ wo, const u16* __restrict__ wr,
                                                const u16* __restrict__ wg, const u16* __restrict__ wu, const u16* __restrict__ wd, char* ws,
                                                u16* __restrict__ out) {
  extern __shared__ __attribute__((aligned(16))) char lds[];
  const int w = __builtin_amdgcn_readfirstlane(threadIdx.x >> 6);
#define LANE() int l; asm volatile("v_mbcnt_lo_u32_b32 %0, -1, 0\n v_mbcnt_hi_u32_b32 %0, -1, %0" : "=v"(l)); \
  const int np = l & 15, g = l >> 4, rr = l >> 3, sl = l & 7; (void)np; (void)g; (void)rr; (void)sl
  STMP(0);
  const int gb = blockIdx.x, xcd = gb & 7, j = gb >> 3, xq = xcd >> 1;
  const unsigned ldsb = (unsigned)(size_t)(LAS char*)lds;
  const bool isQ = j >= 16;
  const int rg = (xcd & 1) * 4 + (j & 3), hp = xq * 4 + (j >> 2), qg = xq * 2 + ((j >> 2) & 1);
  const int hd0 = isQ ? qg * 4 : 32 + hp * 2, hd1 = isQ ? 2 : 32;
  const bool isB = w == 6;
  unsigned mb_, step, d1, d2, d3, so, lim; u4 rs;
#define VSET() unsigned v[4]; { const unsigned pitch = isB ? 6144 : 2048, sm = isB ? 2 : 7; \
    _Pragma("unroll") for (int i = 0; i < 4; ++i) v[i] = (8 * i + rr) * pitch + ((sl ^ ((isB ? rr : rr >> 1) & sm) ^ ((i & 1) << 2)) * 16) - i * 1024; }
#define QROT 2
#define G4(s, q, dq) do { unsigned t_; asm volatile("s_add_u32 m0, %1, %2\n s_add_u32 %0, %3, %4" : "=s"(t_) : "s"(mb_), "i"(((s) % 3) * SLOT + (q) * 4096), "s"(so), "s"(dq) : "scc", "memory"); \
        BLDS(v[0], rs, t_, 0); BLDS(v[1], rs, t_, 1024); BLDS(v[2], rs, t_, 2048); BLDS(v[3], rs, t_, 3072); } while (0)
#define PA(s) do { G4(s, 0, 0u); G4(s, 1, d1); } while (0)
#define PB(s) do { G4(s, 2, d2); G4(s, 3, d3); asm volatile("s_add_u32 %0, %0, %1\n s_cmp_eq_u32 %0, %2\n s_cselect_b32 %0, 0, %0" : "+s"(so) : "s"(step), "s"(lim) : "scc"); } while (0)
  if (__builtin_expect(j < 24 && w >= 4 && w < 7, 1)) {
    LANE();
    rs = isB ? mkrs((const char*)wqkv + hd0 * 64, 1024 * 6144 - hd0 * 64) : mkrs((const char*)x + (size_t)(rg * 256 + (w - 4) * 128) * 2048, 128 * 2048);
    mb_ = RFL(ldsb + (w - 4) * 16384); step = isB ? 64 * 6144 : 128;
    d1 = isB ? 32 * 6144 : 65536; d2 = isB ? hd1 * 64 : 131072; d3 = d1 + d2;
    so = isQ ? QROT * step : 0; lim = 16 * step;
    VSET();
    PA(0); PB(0); PA(1);
  }
#define CPF 1
  if (CPF && j < 24 && w < 4) {
    LANE();
    u64 pc; unsigned len;
    asm volatile("s_getpc_b64 %0\n.Lcp0:\n s_mov_b32 %1, .Lcp1-.Lcp0\n s_max_i32 %1, %1, .Lcp2-.Lcp0\n s_max_i32 %1, %1, .Lcp3-.Lcp0\n s_max_i32 %1, %1, .Lcp4-.Lcp0\n s_max_i32 %1, %1, .Lcp5-.Lcp0\n s_max_i32 %1, %1, .Lcp6-.Lcp0\n s_max_i32 %1, %1, 0" : "=&s"(pc), "=&s"(len) :: "scc");
    const u4 rsC = (u4){(unsigned)pc & ~127u, (unsigned)(pc >> 32) & 0xFFFFu, len, 0x00020000u};
    const unsigned o = (j * 4 + w) * 1024 + l * 16;
    asm volatile("s_mov_b32 m0, %2\n s_nop 0\n buffer_load_dwordx4 %0, %1, 0 offen lds" :: "v"(o), "s"(rsC), "s"(RFL(ldsb + 3 * SLOT + w * 1024)) : "memory");
  }
#define CPL(n) asm volatile(".Lcp" #n ":")
  const __amdgpu_buffer_rsrc_t wrs = __builtin_amdgcn_make_buffer_rsrc(ws, 0, WS_END, 0x00020000);
  const int mb2 = (xcd & 1) * 8 + (xcd >> 2) * 4 + (j >> 3), nb = ((xcd >> 1) & 1) * 8 + (j & 7), hx = ((mb2 + (mb2 >> 2)) & 3) * 8;
#define msg (WS_MSG + xcd * 256 + ((l & 8) ? 0 : 64 + sl * 8))
#define MSGOK(f) all64((((l & 8) ? ((f)[0] ^ tlo) | ((f)[1] ^ thi) : (f)[1] ^ tlo)) == 0u)
#define KEYOK(f) all64((l & 8) || (f)[1] == tlo)
#define GBAD(v) (((v)[1] ^ tlo) | ((v)[3] ^ thi))
#define WO_SETUP() const u4 rsO = mkrs((const char*)wo + nb * 128, 1024 * 2048 - nb * 128), rsX = mkrs((const char*)x + (size_t)mb2 * 128 * 2048 + nb * 128, 128 * 2048 - nb * 128); \
  const unsigned vW = rr * 2048 + ((sl ^ (((rr >> 1) & 3) << 1)) * 16), vX = rr * 2048 + ((sl ^ rr) * 16)
#define DMA1(v, rs, m, so) asm volatile("s_mov_b32 m0, %2\n s_nop 0\n buffer_load_dwordx4 %0, %1, %3 offen lds" ::"v"(v), "s"(rs), "s"(RFL(m)), "s"(RFL(so)) : "memory")
#define WOL(n0, n1, st) _Pragma("nounroll") for (int n_ = (n0); n_ < (n1); n_ += (st)) DMA1(vW, rsO, ldsb + L_WO + n_ * 1024, (n_ * 16384) ^ (hx * 65536))
#define XTL(n0, st) _Pragma("nounroll") for (int n_ = (n0); n_ < 16; n_ += (st)) DMA1(vX, rsX, ldsb + L_XT + n_ * 1024, n_ * 16384)
  const int ow = isQ && j < 24 ? w ^ 4 : w;
  f4 oc[2][4];
  int sel[2];
  u4 st2[2];
  unsigned tpoll = 0;
#define TPOLL(d) tpoll = (unsigned)__builtin_amdgcn_s_memrealtime() + (d)
#define PDK 280
#define PDQ 90
#define XT(r, ct) (xt + (r) * 2048 + (((2 * (ct) + (g >> 1)) ^ sl) * 16))
#define OINIT() do { const char* xt = lds + L_XT + (ow * 32 + np) * 128 + (g & 1) * 8; \
    { const u2 t_ = *(const u2*)(lds + L_TAG); if (!all64(((st2[0][1] ^ t_[0]) | (st2[0][3] ^ t_[1]) | (st2[1][1] ^ t_[0]) | (st2[1][3] ^ t_[1])) == 0u)) { CLOB(); ST2(); } } \
    const u4 k0 = *(const u4*)(lds + L_TAG + 16), k1 = *(const u4*)(lds + L_TAG + 32); \
    _Pragma("unroll") for (int r = 0; r < 2; ++r) { \
      const float c1 = 1.f + U2F(st2[r][2]), c0 = -hi(st2[r][0]) * U2F(st2[r][2]); \
      _Pragma("unroll") for (int ct = 0; ct < 4; ++ct) { const u2 v = *(const u2*)XT(r, ct); \
        oc[r][ct] = (f4){__builtin_fmaf(lo(v[0]), c1, c0), __builtin_fmaf(hi(v[0]), c1, c0), __builtin_fmaf(lo(v[1]), c1, c0), __builtin_fmaf(hi(v[1]), c1, c0)}; PIN(oc[r][ct]); } \
      const unsigned want = 0xFFFFu - (unsigned)(mb2 * 128 + ow * 32 + r * 16 + np); sel[r] = -1; \
      _Pragma("unroll") for (int i = 0; i < 4; ++i) { if ((k0[i] & 0xFFFFu) == want) sel[r] = i; if ((k1[i] & 0xFFFFu) == want) sel[r] = 4 + i; } \
      PIN(sel[r]); } } while (0)
#define XOWN() do { WO_SETUP(); (void)vW; (void)rsO; _Pragma("unroll") for (int n_ = 0; n_ < 4; ++n_) DMA1(vX, rsX, ldsb + L_XT + (ow * 4 + n_) * 1024, (ow * 4 + n_) * 16384); ST2(); } while (0)
#define ST2() do { const unsigned sto = WS_MS + (mb2 * 128 + (w & 3) * 32 + np) * 16; st2[0] = LD128(sto); st2[1] = LD128(sto + 256); } while (0)

  if (j < 24) {
    if (w >= 4) {
      LANE();
      unsigned tlo = 0, thi = 0;
      WO_SETUP();
      u2 f = (u2){0u, 0u};
      if (w == 7) {
        const u4 rsW = mkrs(ws, WS_END);
        int nbar = 16;
#define PFB 0
#define PFD 4
        const u4 rsP = mkrs((const char*)wqkv + 2048 + xq * 512, isQ && PFB ? 1024 * 6144 - 2048 - xq * 512 : 0);
        unsigned vP = ((j & 7) * 8 + rr) * 6144 + (sl >> 2) * 2048 + (sl & 3) * 128 + PFD * 64 * 6144;
#define PF() do { if (PFB) { asm volatile("s_mov_b32 m0, %2\n s_nop 0\n buffer_load_dword %0, %1, 0 offen lds" :: "v"(vP), "s"(rsP), "s"(RFL(ldsb + L_ROWM + 3072)) : "memory"); \
          if (PFB == 2) asm volatile("buffer_load_dword %0, %1, 0 offen offset:64 lds" :: "v"(vP), "s"(rsP) : "memory"); vP += 0x60000; } } while (0)
#define TICK() do { if (nbar) { BAR(); --nbar; } } while (0)
        TICK(); PF();
        u2 ep; unsigned z0 = 0; PIN(z0);
        ALD64(ep, z0);
        { const u4 pz = (u4){POISON, POISON, POISON, POISON};
          if (isQ) {
            const unsigned fb = WS_OFL + ((rg * 4 + (l >> 5)) * 32 + qg * 4) * 128 + (l & 31) * 16;
            ST128(pz, fb); ST128(pz, fb + 8192);
          } else {
            const unsigned kb = WS_KVP + (hp * 16 + rg) * KVS + l * 16;
            ST128(pz, kb); ST128(pz, kb + 1024); ST128(pz, kb + 8 * KVS); ST128(pz, kb + 8 * KVS + 1024);
            ST128(pz, l < 16 ? WS_KVP + (hp * 16 + rg + (l & 8)) * KVS + 2048 + sl * 16 : WS_OFF);
          } }
        SBAR();
        TICK(); PF(); TICK(); PF(); TICK(); PF();
        WAITV(3 * PFB); PIN(ep);
        const u64 tag = (((u64)RFL(ep[1]) << 32) | RFL(ep[0])) + TAG_STEP;
        tlo = (unsigned)tag; thi = (unsigned)(tag >> 32);
        ST64(((u2){tlo, thi}), l == 0 ? WS_FLAG + gb * 8 : WS_OFF);
        STMP(8);
#pragma nounroll
        while (nbar > 16 - TKP) { TICK(); PF(); }
        PF();
        u4 ms[4], cs[8];
        const unsigned a0 = WS_MS + (rg * 256 + l) * 16, a2 = WS_CS + ((g * 96 + hd0) * 16 + np) * 16, a3 = a2 + hd1 * 256, a4 = a2 + 4 * 96 * 256, a5 = a3 + 4 * 96 * 256, a6 = msg;
#pragma nounroll
        for (unsigned it = 0; it < SPIN_CAP; ++it) {
#pragma unroll
          for (int i = 0; i < 4; ++i) asm volatile("buffer_load_dwordx4 %0, %1, %2, 0 offen offset:%3 sc1" : "=v"(ms[i]) : "v"(a0), "s"(rsW), "i"(i * 1024) : "memory");
#pragma unroll
          for (int i = 0; i < 8; ++i) asm volatile("buffer_load_dwordx4 %0, %1, %2, 0 offen offset:%3 sc1" : "=v"(cs[i]) : "v"(i < 2 ? a2 : i < 4 ? a3 : i < 6 ? a4 : a5), "s"(rsW), "i"((i & 1) * 256) : "memory");
          ALD64(f, a6);
          if (nbar > 1) TICK();
          if (nbar > 1) TICK();
          WAITV(0);
          unsigned bad = 0;
#pragma unroll
          for (int i = 0; i < 4; ++i) { PIN(ms[i]); PIN(cs[i]); PIN(cs[4 + i]); bad |= GBAD(ms[i]) | GBAD(cs[i]) | GBAD(cs[4 + i]); }
          PIN(f);
          if (all64(bad == 0u)) break;
        }
        STMP(9);
        float ct[2];
#pragma unroll
        for (int e = 0; e < 2; ++e) {
          float t[4];
#pragma unroll
          for (int i = 0; i < 4; ++i) t[i] = add1(U2F(cs[i][2 * e]), U2F(cs[4 + i][2 * e]));
#pragma unroll
          for (int i = 0; i < 2; ++i) { auto r = __builtin_amdgcn_permlane32_swap(F2U(t[i]), F2U(t[i + 2]), false, false); t[i] = add1(U2F(r[0]), U2F(r[1])); }
          auto r = __builtin_amdgcn_permlane16_swap(F2U(t[0]), F2U(t[1]), false, false);
          ct[e] = add1(U2F(r[0]), U2F(r[1])); PIN(ct[e]);
        }
#pragma unroll
        for (int i = 0; i < 4; ++i) { ms[i][0] = (ms[i][0] & 0xFFFF0000u) ^ 0x80000000u; ms[i][1] = F2U(mul1(U2F(ms[i][2]), 1.44269504f)); PIN(ms[i]); }
#pragma nounroll
        while (nbar) TICK();
#pragma unroll
        for (int i = 0; i < 4; ++i) { ((unsigned*)(lds + L_ROWM))[i * 64 + l] = ms[i][0]; ((unsigned*)(lds + L_ROWS))[i * 64 + l] = ms[i][1]; }
        ((float*)(lds + L_COL))[g * 32 + np] = ct[0]; ((float*)(lds + L_COL))[g * 32 + 16 + np] = ct[1];
        if (l < 8) ((unsigned*)(lds + L_CNT))[l] = 0u;
        if (l == 8) *(u2*)(lds + L_TAG) = (u2){tlo, thi};
      } else {
        VSET();
        WAITV(8); BAR(); PB(1); PA(2); WAITV(8); BAR(); STMP(44); PB(2); PA(0); PB(0);
#define LS(s) do { if ((s) < 14) WAITV(16); else WAITV(0); BAR(); STMP(44 + (s)); if ((s) + 3 < 16) { PA(s); PB(s); } } while (0)
        LS(1); LS(2); LS(3); LS(4); LS(5); LS(6); LS(7); LS(8); LS(9); LS(10); LS(11); LS(12); LS(13); LS(14);
      }
#define KEYS() do { if (w == 7) { _Pragma("nounroll") for (unsigned it = 0; it < SPIN_CAP && !MSGOK(f); ++it) { f = LD64(msg); CLOB(); } \
        if (l < 8) ((unsigned*)(lds + L_TAG + 16))[l] = f[0]; \
        lds_arrive((LAS unsigned*)(lds + L_CNT) + 3); } } while (0)
      if (isQ) {
        if (w < 7) { WOL(20 + w - 4, WQ0, 3); }
        LBAR(); STMP(59);
        KEYS();
        WOL(WQ0 + w - 4, 128, 4); WOL(w - 4, 20, 4); XOWN();
        STMP(21);
        const unsigned sb = WS_KVP + (qg * 4 + (w & 1)) * 8 * KVS + ((w >> 1) & 1) * 1024 + l * 16;
        const unsigned sz = (w & 2) ? WS_OFF : WS_KVP + ((qg * 4 + (w & 1)) * 8 + (np >> 1)) * KVS + 2048 + (np & 1) * 64 + g * 16;
        u4 prm[2];
#pragma unroll
        for (int e = 0; e < 4; ++e) {
          const unsigned o = (np >> 2) == g ? ((np & 1) ? 0x3F800000u : 0x00003F80u) : 0u;
          prm[0][e] = e == ((np >> 1) & 1) ? o : 0u; prm[1][e] = e == 2 + ((np >> 1) & 1) ? o : 0u;
        }
        PIN(prm[0]); PIN(prm[1]);
        __builtin_amdgcn_s_setprio(2);
        if (KD) PACE(KD);
        lds_wait<1>((LAS unsigned*)(lds + L_CNT) + 3, 1);
        u4 v[16], zv[2]; f4 kg[4][2];
#define KVLD(gq) do { _Pragma("unroll") for (int p = 4 * (gq); p < 4 * (gq) + 4; ++p) v[p] = __builtin_amdgcn_raw_buffer_load_b128(wrs, sb, ((p & 7) + (p >> 3) * 16) * KVS, 16); } while (0)
#define ZLD(hh) zv[hh] = __builtin_amdgcn_raw_buffer_load_b128(wrs, sz, (hh) * 16 * KVS, 16)
        ZLD(0); KVLD(0); KVLD(1); SBAR(); ZLD(1); KVLD(2); KVLD(3); SBAR();
#pragma unroll
        for (int hh = 0; hh < 2; ++hh) {
          unsigned pend = 7;
#pragma nounroll
          for (unsigned it = 0; it < SPIN_CAP; ++it) {
#pragma unroll
            for (int gq = 2 * hh; gq < 2 * hh + 2; ++gq)
              if (pend & (1u << (gq & 1))) {
                f4 t0_ = (f4)(0.f), t1_ = (f4)(0.f); unsigned mx = 0;
#pragma unroll
                for (int p = 4 * gq; p < 4 * gq + 4; ++p) { mx = mx4(mx, v[p]); t0_ = MFMA16(BC8(prm[0]), BC8(v[p]), t0_); t1_ = MFMA16(BC8(prm[1]), BC8(v[p]), t1_); }
                kg[gq][0] = t0_; kg[gq][1] = t1_;
                if (clean(mx)) pend &= ~(1u << (gq & 1));
              }
            if ((pend & 4u) && clean(mx4(0u, zv[hh]))) pend &= ~4u;
            if (!pend) break;
            if (KSL) __builtin_amdgcn_s_sleep(KSL);
            CLOB();
            if (pend & 4u) ZLD(hh);
            if (pend & 1u) KVLD(2 * hh);
            if (pend & 2u) KVLD(2 * hh + 1);
            if (hh == 0) { ZLD(1); KVLD(2); KVLD(3); }
            SBAR();
          }
          if (hh) STMP(25); else STMP(4);
          const f4 k0 = kg[2 * hh][0] + kg[2 * hh + 1][0], k1 = kg[2 * hh][1] + kg[2 * hh + 1][1];
          *(u4*)(lds + L_KX + ((2 * hh + (w & 1)) * 2 + ((w >> 1) & 1)) * 1024 + l * 16) = (u4){pk2(k0[0], k0[1]), pk2(k0[2], k0[3]), pk2(k1[0], k1[1]), pk2(k1[2], k1[3])};
          if (!(w & 2)) {
            f4 z = __builtin_bit_cast(f4, zv[hh]);
#pragma unroll
            for (int e = 0; e < 4; ++e) { z[e] += DPPF(z[e], 0x122, 0xf); z[e] += DPPF(z[e], 0x124, 0xf); z[e] += DPPF(z[e], 0x128, 0xf); }
            if (np < 2) *(u2*)(lds + L_ZX + (2 * hh + (w & 1)) * 64 + g * 16 + np * 8) = (u2){pk2(z[0], z[1]), pk2(z[2], z[3])};
          }
          LBAR();
        }
        __builtin_amdgcn_s_setprio(0);
        CPL(3);
        TPOLL(PDQ);
        OINIT();
      } else {
      const int kd = j * 8 + xcd, dc = kd >> 1;
      const bool down = kd < 112;
      u4 dw[2][8];
      const u4 rsD = mkrs((const char*)wd + (size_t)dc * 64 * 2048 + (kd & 1) * 1024 + (w - 4) * 256, down ? 63 * 2048 + 256 : 0);
#define DLD(a, b) do { const unsigned o_ = g * 8 * 2048 + np * 16; _Pragma("unroll") for (int i = a; i < b; ++i) \
        asm volatile("buffer_load_dwordx4 %0, %1, %2, %3 offen offset:%4 nt" : "=v"(dw[i >> 3][i & 7]) : "v"(o_), "s"(rsD), "s"((unsigned)((i >> 3) * 65536 + (i & 6) * 2048)), "i"((i & 1) * 2048) : "memory"); } while (0)
#define DS0 8
        DLD(0, DS0);
        LBAR(); STMP(59);
        DLD(DS0, 16);
        const unsigned ho = down ? WS_H + (dc * 128 + sl * 16 + g * 2) * 16 : WS_OFF;
        u4 hv[4];
#define HLD() do { hv[0] = LD128(ho); hv[1] = LD128(ho + 16); hv[2] = LD128(ho + 128); hv[3] = LD128(ho + 144); SBAR(); } while (0)
        const u2 tg = *(const u2*)(lds + L_TAG);
        tlo = RFL(tg[0]); thi = RFL(tg[1]);
        BAR(); BAR();
        HLD();
        KEYS();
        WAITV(0);
#pragma unroll
        for (int i = 0; i < 16; ++i) PIN(dw[i >> 3][i & 7]);
        if (down) {
#pragma nounroll
          for (unsigned it = 0; it < SPIN_CAP; ++it) {
            if (all64((GBAD(hv[0]) | GBAD(hv[1]) | GBAD(hv[2]) | GBAD(hv[3])) == 0u)) break;
            __builtin_amdgcn_s_sleep(8);
            CLOB();
            HLD();
          }
          __builtin_amdgcn_s_setprio(2);
          f4 acd[4];
#pragma unroll
          for (int c = 0; c < 4; ++c) acd[c] = (f4)(0.f);
#pragma unroll
          for (int fb = 0; fb < 2; ++fb) {
            u4 hl, hh;
#pragma unroll
            for (int e = 0; e < 4; ++e) { const unsigned ha = hv[2 * fb + (e >> 1)][(e & 1) * 2]; hl[e] = np < 8 ? ha : 0u; hh[e] = np < 8 ? 0u : ha; }
#pragma unroll
            for (int c = 0; c < 4; ++c) {
              u4 d0, d1; TRFRAG(d0, dw[fb], c); TRFRAG(d1, dw[fb], c + 4);
              acd[c] = MFMA16(BC8(hh), BC8(d1), MFMA16(BC8(hl), BC8(d0), acd[c]));
            }
          }
          lds_wait<2>((LAS unsigned*)(lds + L_CNT) + 2, 4);
#pragma unroll
          for (int e = 0; e < 4; ++e)
            ST128(tag4((f4){acd[0][e], acd[1][e], acd[2][e], acd[3][e]}, tlo), WS_YP + (dc * 8 + 4 * (g & 1) + e) * 4096 + ((kd & 1) * 4 + w - 4) * 512 + (g >> 1) * 256 + np * 16);
          __builtin_amdgcn_s_setprio(0);
          STMP(14);
        }
      }
    } else {
      const int wm = w >> 1, wn = w & 1;
      if (!isQ) {
        constexpr bool TRN = false;
        KLOOP();
        float cc[4]; f4 rsk[8], mk[8];
#define TB(n) do { if ((n) < 4) RD32(cc[(n) & 3], tC, ((n) >> 1) * 256 + ((n) & 1) * 64); else if ((n) < 12) { if ((n) & 1) RD128(rsk[(((n) - 4) >> 1) & 3], tR, 1024 + (((n) - 4) >> 1) * 64); else RD128(mk[(((n) - 4) >> 1) & 3], tR, (((n) - 4) >> 1) * 64); } SBAR(); } while (0)
        KLAST();
#undef TB
#pragma unroll
        for (int i = 0; i < 4; ++i) { PIN(mk[i]); PIN(rsk[i]); PIN(cc[i]); }
#pragma unroll
        for (int i = 4; i < 8; ++i) { RD128(mk[i], tR, i * 64); RD128(rsk[i], tR, 1024 + i * 64); }
        SBAR();
        STMP(2); STMC(41);
        f4 kv[2][2], zz[2];
        kv[0][0] = kv[0][1] = kv[1][0] = kv[1][1] = zz[0] = zz[1] = (f4)(0.f);
        const u4 ones = (u4){0x3F803F80u, 0x3F803F80u, 0x3F803F80u, 0x3F803F80u};
        u4 kf[2], vf[2];
#define LN2(i, jj, e0) (__builtin_elementwise_fma(H2(mk[i], e0), (f2)(cc[jj]), H2(c[i][jj], e0)) * H2(rsk[i], e0))
#define KVM(n) do { if ((n) < 4) kv[(n) >> 1][(n) & 1] = MFMA16(BC8(kf[(n) >> 1]), BC8(vf[(n) & 1]), kv[(n) >> 1][(n) & 1]); else zz[(n) & 1] = MFMA16(BC8(kf[(n) & 1]), BC8(ones), zz[(n) & 1]); } while (0)
#pragma unroll
        for (int ip = 0; ip < 4; ++ip) {
          if (ip == 2) { WAITL(0);
#pragma unroll
            for (int i = 4; i < 8; ++i) { PIN(mk[i]); PIN(rsk[i]); } }
          f2 ak[8], av[8], ek[8];
#pragma unroll
          for (int n = 0; n < 8; ++n) { const int i = 2 * ip + ((n >> 1) & 1), e0 = (n & 1) * 2, t = n >> 2; ak[n] = LN2(i, t, e0); av[n] = LN2(i, 2 + t, e0); }
          SBAR();
#pragma unroll
          for (int n = 0; n < 8; ++n) {
            if (ip > 0 && n < 6) KVM(n);
            ek[n] = (f2){__builtin_amdgcn_fmed3f(__builtin_amdgcn_exp2f(ak[n][0]), 0.f, 1.f), __builtin_amdgcn_fmed3f(__builtin_amdgcn_exp2f(ak[n][1]), 0.f, 1.f)};
            ak[n] = (f2){fmaxf(ak[n][0], 1.44269504e-6f), fmaxf(ak[n][1], 1.44269504e-6f)};
            SBAR();
          }
#pragma unroll
          for (int n = 0; n < 8; ++n) { const f2 r = __builtin_elementwise_fma(ak[n], (f2)(0.693147181f), ek[n]); kf[n >> 2][n & 3] = pk2(r[0], r[1]); vf[n >> 2][n & 3] = pk2(av[n][0], av[n][1]); }
          SBAR();
        }
        KVM(0); KVM(1); KVM(2); KVM(3); KVM(4); KVM(5);
        STMP(19);
        LANE();
        const f4 zs = (np & 1) ? zz[1] : zz[0];
        char* ex = lds + L_EX + l * 16;
        *(f4*)(ex + w * 4096) = wm ? kv[0][0] : kv[0][1]; *(f4*)(ex + w * 4096 + 1024) = wm ? kv[1][0] : kv[1][1]; *(f4*)(ex + w * 4096 + 2048) = zs;
        LBAR();
        const f4 o0 = ((wm ? kv[0][1] : kv[0][0]) + *(const f4*)(ex + (w ^ 2) * 4096)) * 0.693147181f;
        const f4 o1 = ((wm ? kv[1][1] : kv[1][0]) + *(const f4*)(ex + (w ^ 2) * 4096 + 1024)) * 0.693147181f;
        const f4 zt = zs + *(const f4*)(ex + (w ^ 2) * 4096 + 2048);
        const unsigned kb = WS_KVP + ((hp * 2 + wn) * 8 + rg) * KVS;
        ST128(san4((u4){pk2(o0[0], o0[1]), pk2(o0[2], o0[3]), pk2(o1[0], o1[1]), pk2(o1[2], o1[3])}), kb + wm * 1024 + l * 16);
        ST128(san4(__builtin_bit_cast(u4, zt)), wm == 0 && np < 2 ? kb + 2048 + np * 64 + g * 16 : WS_OFF);
        SBAR();
        STMP(3);
        TPOLL(PDK);
        BAR();
        PACE(2);
        XOWN();
        { WO_SETUP(); (void)vX; (void)rsX; WOL(w, 128, 4); }
        WAITV(0);
        lds_arrive((LAS unsigned*)(lds + L_CNT) + 2);
        lds_wait<1>((LAS unsigned*)(lds + L_CNT) + 3, 1);
        OINIT();
        lds_wait<1>((LAS unsigned*)(lds + L_CNT) + 2, 4);
        CPL(1);
        STMP(20);
      } else {
        constexpr bool TRN = true;
        KLOOP();
        f4 cc[4]; float rs[8], mn[8];
#define TB(n) do { if ((n) < 4) RD128(cc[(n) & 3], tC, ((n) >> 1) * 256 + ((n) & 1) * 64); else if ((n) < 20) { if ((n) & 1) RD32(rs[(((n) - 4) >> 1) & 7], tR, 1024 + (((n) - 4) >> 1) * 64); else RD32(mn[(((n) - 4) >> 1) & 7], tR, (((n) - 4) >> 1) * 64); } SBAR(); } while (0)
        KLAST();
#undef TB
#pragma unroll
        for (int i = 0; i < 8; ++i) { PIN(mn[i]); PIN(rs[i]); PIN(cc[i & 3]); }
        STMP(2); STMC(41);
        LANE();
        u4 pf[8][2];
#pragma unroll
        for (int i = 0; i < 8; ++i)
#pragma unroll
          for (int hh = 0; hh < 2; ++hh) {
#pragma unroll
            for (int e = 0; e < 4; ++e) { const int jj = 2 * hh + (e >> 1), e0 = (e & 1) * 2;
              pf[i][hh][e] = phi2(__builtin_elementwise_fma(H2(cc[jj], e0), (f2)(mn[i]), H2(c[i][jj], e0)) * rs[i]); }
            PIN(pf[i][hh]);
          }
        const u2 tg = *(const u2*)(lds + L_TAG);
        const unsigned ofl = l < 16 ? WS_OFL + ((rg * 4 + wm * 2 + rr) * 32 + (qg * 2 + wn) * 2) * 128 + sl * 16 : WS_OFF;
        unsigned oa[8];
#pragma unroll
        for (int i = 0; i < 8; ++i) { oa[i] = WS_OF + ((rg * 16 + wm * 8 + i) * 32 + qg * 4 + wn) * 1024 + l * 16; PIN(oa[i]); }
        STMP(19);
        BAR();
        STMP(22);
        u4 kA[2][2], zA[2];
#define KRD(hh) do { zA[hh] = *(const u4*)(lds + L_ZX + (2 * (hh) + wn) * 64 + g * 16); \
          kA[hh][0] = *(const u4*)(lds + L_KX + (2 * (hh) + wn) * 2048 + l * 16); kA[hh][1] = *(const u4*)(lds + L_KX + (2 * (hh) + wn) * 2048 + 1024 + l * 16); SBAR(); } while (0)
        KRD(0);
        f4 dn[16], x0[16], x1[16];
#define OM(n) do { dn[n] = MFMA16(BC8(zA[(n) >> 3]), BC8(pf[(n) & 7][(n) >> 3]), (f4)(1e-6f)); x0[n] = MFMA16(BC8(kA[(n) >> 3][0]), BC8(pf[(n) & 7][(n) >> 3]), (f4)(0.f)); \
          x1[n] = MFMA16(BC8(kA[(n) >> 3][1]), BC8(pf[(n) & 7][(n) >> 3]), (f4)(0.f)); } while (0)
        OM(0); OM(1);
#define FI 13
#pragma unroll
        for (int n = 0; n < 16; ++n) {
          SBAR();
          const float inv = __builtin_amdgcn_rcpf(dn[n][0]);
          asm volatile("" :: "v"(dn[n]));
          if (n + 2 < 16 && n != 6 && n != 7) OM(n + 2);
          float y[8];
#pragma unroll
          for (int e = 0; e < 4; ++e) { y[e] = x0[n][e] * inv; y[4 + e] = x1[n][e] * inv; PIN(y[e]); PIN(y[4 + e]); }
          ST128(((u4){pk2(y[0], y[1]), pk2(y[2], y[3]), pk2(y[4], y[5]), pk2(y[6], y[7])}), oa[n & 7] + (n >> 3) * 2048);
          if (n == 7) { SBAR(); BAR(); KRD(1); OM(8); OM(9); }
          if (n == FI) { SBAR(); WAITV(FI - 7); ST128(((u4){tg[0], tg[1], tg[0], tg[1]}), ofl); SBAR(); STMP(24); }
        }
        SBAR();
        STMP(23);
        WAITV(0);
        ST128(((u4){tg[0], tg[1], tg[0], tg[1]}), ofl + 128);
        SBAR();
        CPL(2);
        STMP(5);
      }
    }
  } else {
    LANE();
    const int jm = j - 24, jsl = (3 - (int)(((size_t)wg >> 7) & 7) - xcd) & 7;
    const bool master = jm == 7 && jsl == 7, help = jm == 7 && jsl != 7, half = jm == 7 || jm == jsl;
    const int jc = help ? jsl : jm, hc = jc * 8 + ((xcd + jc) & 7);
    const int row0 = (xcd & 1) * 1024 + (xq * 8 + jm) * 32 + w * 4;
    u4 xr[8], wv[2];
#pragma unroll
    for (int i = 0; i < 8; ++i) xr[i] = *(const u4*)(x + row0 * D + i * 512 + l * 8);
    wv[0] = *(const u4*)(wr + l * 8); wv[1] = *(const u4*)(wr + 512 + l * 8);
    u2 ep = LD64(WS_EP);
    SBAR();
    u4 cw[12];
    const u16* cp = wqkv + (size_t)(jm * 128 + w * 16 + g) * 3072 + xcd * 128 + np * 8;
#define CL(a, b) do { _Pragma("unroll") for (int i = a; i < b; ++i) cw[i] = *(const u4*)(cp + ((i & 3) * 12 + ((i >> 2) + 1) % 3) * 1024); SBAR(); } while (0)
    if (w == 0 && l < 8) ((unsigned*)(lds + L_CNT))[l] = 0u;
    CL(0, CL0);
    PIN(ep);
    const u64 tag = (((u64)RFL(ep[1]) << 32) | RFL(ep[0])) + TAG_STEP;
    const unsigned tlo = (unsigned)tag, thi = (unsigned)(tag >> 32);
    const u2 tgv = (u2){tlo, thi};
    {
      float a[13];
#pragma unroll
      for (int i = 0; i < 13; ++i) a[i] = 0.f;
      unsigned one2 = 0x3F803F80u; PIN(one2);
#pragma unroll
      for (int jj = 0; jj < 8; ++jj) {
        const unsigned wv_ = wv[jj >> 2][jj & 3];
#pragma unroll
        for (int i = 0; i < 4; ++i) {
          const unsigned xv = xr[2 * i + (jj >> 2)][jj & 3];
          a[3 * i] = DOT2(xv, one2, a[3 * i]); a[3 * i + 1] = DOT2(xv, xv, a[3 * i + 1]); a[3 * i + 2] = DOT2(xv, wv_, a[3 * i + 2]);
        }
        a[12] = DOT2(wv_, one2, a[12]);
      }
#define RSTEP(n, ctrl) _Pragma("unroll") for (int i = 0; i < n; ++i) a[i] += DPPF(a[i], ctrl, 0xf)
      RSTEP(13, 0xB1); RSTEP(13, 0x4E);
#pragma unroll
      for (int i = 0; i < 3; ++i) { const float u0 = (l & 1) ? a[3 + i] : a[i], u1 = (l & 1) ? a[9 + i] : a[6 + i]; a[i] = (l & 2) ? u1 : u0; }
      a[3] = a[12];
      RSTEP(4, 0x124); RSTEP(4, 0x128);
#pragma unroll
      for (int i = 0; i < 4; ++i) {
        auto s16 = __builtin_amdgcn_permlane16_swap(F2U(a[i]), F2U(a[i]), false, false);
        a[i] = U2F(s16[0]) + U2F(s16[1]);
        auto s32 = __builtin_amdgcn_permlane32_swap(F2U(a[i]), F2U(a[i]), false, false);
        a[i] = U2F(s32[0]) + U2F(s32[1]);
      }
      const float mf = a[0] * (1.f / D), m = rbf(mf);
      const float var = rbf(fmaxf(a[1] * (1.f / D) - mf * mf, 0.f));
      const float rs = __builtin_amdgcn_rcpf(rbf(__builtin_amdgcn_sqrtf(rbf(var + 1e-5f))));
      const float dsc = rs * (a[2] - m * a[3]);
      ST128(((u4){pk2(dsc, m), tlo, F2U(rs), thi}), l < 4 ? WS_MS + (row0 + l) * 16 : WS_OFF);
    }
    SBAR();
    STMP(18);
    CL(CL0, 8);
    LBAR();
    if (w == 7) ST64(tgv, l == 0 ? WS_FLAG + gb * 8 : WS_OFF);
    SBAR();
    CL(8, 12);
    u4 gw[4][8], gf[4][8];
    const u16* wp = ((np >> 3) ? wu : wg) + (size_t)(128 * w + (help ? 64 : 0) + 8 * g) * F + 64 * hc + 8 * sl;
#define GL(b, on) do { const u16* p_ = (on) ? wp + (size_t)(32 * (b)) * F : wr + l * 8; const unsigned st_ = (on) ? F : 0; \
      _Pragma("unroll") for (int i_ = 0; i_ < 8; ++i_) gw[b][i_] = NTL((const u4*)(p_ + (size_t)(i_ * st_))); SBAR(); } while (0)
#define GT(b) do { _Pragma("unroll") for (int c_ = 0; c_ < 8; ++c_) { TRFRAG(gf[b][c_], gw[b], c_); PIN(gf[b][c_]); } SBAR(); } while (0)
    if (master && (w == 1 || w == 2)) {
      u4 rv[16];
      const unsigned ra = WS_MS + ((w - 1) * 1024 + l) * 16;
      PACE(MD1);
#pragma unroll
      for (int i = 0; i < 16; ++i) rv[i] = LD128(ra + i * 1024);
      SBAR();
#pragma nounroll
      for (unsigned it = 0; it < SPIN_CAP; ++it) {
        unsigned bad = 0;
#pragma unroll
        for (int i = 0; i < 16; ++i) bad |= GBAD(rv[i]) ? 1u << i : 0u;
        if (all64(bad == 0u)) break;
        __builtin_amdgcn_s_sleep(4);
        CLOB();
#pragma unroll
        for (int i = 0; i < 16; ++i) {
          const u4 v = LD128((bad >> i) & 1u ? ra + i * 1024 : WS_OFF);
          rv[i] = (bad >> i) & 1u ? v : rv[i];
        }
      }
      unsigned key[16];
#pragma unroll
      for (int i = 0; i < 16; ++i) {
        unsigned b = rv[i][0] & 0xFFFFu;
        unsigned mono = (b & 0x8000u) ? (~b & 0xFFFFu) : (b | 0x8000u);
        key[i] = (mono << 16) | (unsigned)(0xFFFF - ((w - 1) * 1024 + i * 64 + l));
      }
      unsigned* cand = (unsigned*)(lds + L_TOP);
#define CE(i, j) do { const unsigned hi_ = max(key[i], key[j]), lo_ = min(key[i], key[j]); key[i] = hi_; key[j] = lo_; } while (0)
#define SORT8(o) do { CE(o + 0, o + 2); CE(o + 1, o + 3); CE(o + 4, o + 6); CE(o + 5, o + 7); CE(o + 0, o + 4); CE(o + 1, o + 5); CE(o + 2, o + 6); CE(o + 3, o + 7); \
    CE(o + 0, o + 1); CE(o + 2, o + 3); CE(o + 4, o + 5); CE(o + 6, o + 7); CE(o + 2, o + 4); CE(o + 3, o + 5); CE(o + 1, o + 4); CE(o + 3, o + 6); CE(o + 1, o + 2); CE(o + 3, o + 4); CE(o + 5, o + 6); } while (0)
      SORT8(0); SORT8(8);
#pragma unroll
      for (int i = 0; i < 8; ++i) key[i] = max(key[i], key[15 - i]);
      CE(0, 4); CE(1, 5); CE(2, 6); CE(3, 7); CE(0, 2); CE(1, 3); CE(4, 6); CE(5, 7); CE(0, 1); CE(2, 3); CE(4, 5); CE(6, 7);
      unsigned cv = 0;
#pragma unroll
      for (int it = 0; it < 8; ++it) {
        const unsigned m = wave_max(key[0]);
        const bool win = key[0] == m;
#pragma unroll
        for (int i = 0; i < 7 - it; ++i) key[i] = win ? key[i + 1] : key[i];
        cv = sl == it ? m : cv;
      }
      LAS unsigned* cnt = (LAS unsigned*)(lds + L_CNT) + 1;
      if (w == 1) {
        if (l < 8) cand[l] = cv;
        lds_arrive(cnt);
      } else {
        lds_wait<1>(cnt, 1);
        u2 t; t[0] = max(cv, cand[7 - sl]); t[1] = tlo;
        ST64(t, WS_MSG + rr * 256 + 64 + sl * 8);
        STMP(29);
      }
    }
    if (master && w == 4) {
      PACE(MD2);
      bool ok = false;
#pragma nounroll
      for (unsigned it = 0; it < SPIN_CAP; ++it) {
        if (!ok) {
          u4 f0 = LD128(WS_FLAG + l * 32), f1 = LD128(WS_FLAG + l * 32 + 16);
          ok = f0[0] == tlo && f0[1] == thi && f0[2] == tlo && f0[3] == thi && f1[0] == tlo && f1[1] == thi && f1[2] == tlo && f1[3] == thi;
        }
        if (all64(ok)) break;
        __builtin_amdgcn_s_sleep(8);
        CLOB();
      }
      CLOB();
      if (l < 8) ST64(tgv, WS_MSG + l * 256);
      if (l == 0) ST64(tgv, WS_EP);
      STMP(30);
    }
    GL(0, !master);
    float* red = (float*)(lds + L_RED);
    {
      u4 sel8;
      const unsigned one = (np & 1) ? 0x3F800000u : 0x00003F80u;
#pragma unroll
      for (int e = 0; e < 4; ++e) sel8[e] = ((np >> 1) == e) ? one : 0u;
#pragma unroll
      for (int p = 0; p < 3; ++p) {
        f4 c = (f4)(0.f);
#pragma unroll
        for (int i = 0; i < 4; ++i) c = MFMA16(BC8(sel8), BC8(cw[p * 4 + i]), c);
        if (g < 2) *(f4*)(red + (w * 3 + p) * 128 + 8 * np + 4 * g) = c;
      }
    }
    LBAR();
    if (w < 3) {
      const float* rp = red + w * 128 + g * 32 + np;
      float c0 = rp[0], c1 = rp[16];
#pragma unroll
      for (int i = 1; i < 8; ++i) { c0 += rp[i * 384]; c1 += rp[i * 384 + 16]; }
      ST128(((u4){F2U(c0), tlo, F2U(c1), thi}), WS_CS + ((jm * 96 + (w + 1) % 3 * 32 + xcd * 4) * 16 + l) * 16);
    }
    SBAR();
    STMP(17);
    GL(1, !master);
    { WO_SETUP(); (void)vX; WOL(w, 32, 8); }
    GT(0);
    u2 f = LD64(msg);
    SBAR();
    GL(2, !half);
    GT(1);
#pragma nounroll
    for (unsigned it = 0; it < SPIN_CAP; ++it) {
      if (KEYOK(f)) break;
      __builtin_amdgcn_s_sleep(16);
      CLOB();
      f = LD64(msg);
    }
    CLOB();
    STMP(9);
    if (w == 0) {
      if (l < 8) ((unsigned*)(lds + L_TAG + 16))[l] = f[0];
      if (l == 8) *(u2*)(lds + L_TAG) = tgv;
    }
    if (!master) {
      const unsigned key_ = (unsigned)__builtin_amdgcn_update_dpp((int)f[0], (int)f[0], 0x128, 0xf, 0xc, false);
      const int idx = (0xFFFF - (int)(key_ & 0xFFFF)) & 2047;
      const u16* xp = x + idx * D + 128 * w + (help ? 64 : 0) + 32 * (np >> 3) + 8 * g;
      const u4 r0 = *(const u4*)xp, r1 = *(const u4*)(xp + (half ? 0 : 64));
      const u4 msx = LD128(WS_MS + idx * 16);
      SBAR();
      GL(3, !half);
      GT(2); GT(3);
      f4 acc[4];
#pragma unroll
      for (int c2 = 0; c2 < 4; ++c2) acc[c2] = (f4)(0.f);
      const float m = hi(msx[0]), rs = U2F(msx[2]);
#pragma unroll
      for (int b2 = 0; b2 < 2; ++b2) {
        if (b2 && half) break;
        u4 xl[2], xh[2];
#pragma unroll
        for (int e = 0; e < 4; ++e) {
          const unsigned r = b2 ? r1[e] : r0[e];
          const unsigned v = pk2((lo(r) - m) * rs, (hi(r) - m) * rs);
          const unsigned o = (unsigned)__builtin_amdgcn_update_dpp(0, (int)v, 0x128, 0xf, 0xf, false);
          xl[0][e] = np < 8 ? v : 0u; xh[0][e] = np < 8 ? 0u : o; xl[1][e] = np < 8 ? o : 0u; xh[1][e] = np < 8 ? 0u : v;
        }
#pragma unroll
        for (int it = 0; it < 2; ++it)
#pragma unroll
          for (int c = 0; c < 8; ++c) {
            acc[c >> 1] = MFMA16(BC8((c & 1) ? xh[it] : xl[it]), BC8(gf[2 * b2 + it][c]), acc[c >> 1]);
          }
        SBAR();
      }
      STMP(12);
      red += 3072;
#pragma unroll
      for (int c2 = 0; c2 < 4; ++c2) *(f4*)(red + (w * 4 + c2) * 256 + l * 4) = acc[c2];
      LBAR();
      f2 sv = *(const f2*)(red + (w >> 1) * 256 + l * 4 + (w & 1) * 2);
#pragma unroll
      for (int i = 1; i < 8; ++i) sv += *(const f2*)(red + (i * 4 + (w >> 1)) * 256 + l * 4 + (w & 1) * 2);
      const unsigned gp = WS_GP + (xcd * 512 + w * 64 + l) * 16;
      if (help) ST128(((u4){F2U(sv[0]), tlo, F2U(sv[1]), thi}), gp);
      else {
      if (half) {
        u4 pv;
#pragma nounroll
        for (unsigned it = 0; it < SPIN_CAP; ++it) {
          pv = LD128(gp);
          if (all64(GBAD(pv) == 0u)) break;
          __builtin_amdgcn_s_sleep(8);
          CLOB();
        }
        sv += (f2){U2F(pv[0]), U2F(pv[2])};
      }
      u16* hb = (u16*)(lds + L_H);
#pragma unroll
      for (int e = 0; e < 2; ++e) {
        const float gg = rbf(sv[e]), u1 = DPPF(gg, 0x128, 0xf);
        const float hv = rbf(gg * __builtin_amdgcn_rcpf(1.f + __builtin_amdgcn_exp2f(gg * -1.44269504f))) * u1;
        if (np < 8) hb[(4 * (g & 1) + 2 * (w & 1) + e) * 64 + 8 * np + 2 * (w >> 1) + (g >> 1)] = (u16)(pk2(hv, hv) & 0xFFFFu);
      }
      LBAR();
      if (w < 2) { const u2 hv = *(const u2*)(hb + (w * 64 + l) * 4); ST128(((u4){hv[0], tlo, hv[1], thi}), WS_H + (hc * 128 + w * 64 + l) * 16); }
      }
      STMP(14);
    }
    LBAR();
    if (w < 4) { LANE(); XOWN(); WAITV(0); OINIT(); }
    else { LANE(); WO_SETUP(); (void)vX; WOL(28 + w, 128, 4); WAITV(0); }
    STMP(21);
    if (w == 7) {
#pragma nounroll
      for (unsigned it = 0; it < SPIN_CAP; ++it) {
        unsigned bad = 0;
        untag4(LD128(WS_YP + (master ? 0 : hc) * 32768), tlo, bad);
        if (tagok(bad)) break;
        __builtin_amdgcn_s_sleep(8);
        CLOB();
      }
#define PDM 30
    }
    BAR();
    TPOLL(PDM);
    CPL(4);
    STMP(20);
  }

  LANE();
  const u2 tg = *(const u2*)(lds + L_TAG);
  LAS unsigned* cnt = (LAS unsigned*)(lds + L_CNT);
  if (ow >= 4) {
#define YD 16
    lds_wait<4>(cnt + 4, 1);
    PACE(YD);
    const unsigned mykey = ((const unsigned*)(lds + L_TAG + 16))[sl], tlo = RFL(tg[0]);
#pragma unroll 1
    for (int i = ow - 4; i < 8; i += 4) {
      const unsigned kk = (unsigned)__builtin_amdgcn_readlane((int)mykey, i);
      const int ti = 0xFFFF - (int)(kk & 0xFFFF);
      if ((ti >> 7) != mb2) continue;
      const unsigned yb = WS_YP + (g * 14 * 8 + i) * 4096 + (nb >> 1) * 512 + (np >> 3) * 256 + ((nb & 1) * 8 + sl) * 16;
      f4 y;
#pragma nounroll
      for (unsigned it = 0; it < SPIN_CAP; ++it) {
        u4 yv[14];
#pragma unroll
        for (int k = 0; k < 14; ++k) yv[k] = LD128(yb + k * 32768);
        SBAR();
        unsigned bad = 0;
        y = untag4(yv[0], tlo, bad);
#pragma unroll
        for (int k = 1; k < 14; ++k) { const f4 t = untag4(yv[k], tlo, bad); y = (f4){add1(y[0], t[0]), add1(y[1], t[1]), add1(y[2], t[2]), add1(y[3], t[3])}; }
        if (tagok(bad)) break;
        __builtin_amdgcn_s_sleep(32);
        CLOB();
      }
#pragma unroll
      for (int e = 0; e < 4; ++e) {
        auto a16 = __builtin_amdgcn_permlane16_swap(F2U(y[e]), F2U(y[e]), false, false);
        y[e] = add1(U2F(a16[0]), U2F(a16[1]));
        auto a32 = __builtin_amdgcn_permlane32_swap(F2U(y[e]), F2U(y[e]), false, false);
        y[e] = add1(U2F(a32[0]), U2F(a32[1]));
      }
      const unsigned mono = kk >> 16;
      const float rv = U2F(((mono & 0x8000u) ? (mono & 0x7FFFu) : (~mono & 0xFFFFu)) << 16);
      const float sg = rbf(__builtin_amdgcn_rcpf(1.f + __builtin_amdgcn_exp2f(rv * -1.44269504f)));
#pragma unroll
      for (int e = 0; e < 4; ++e) y[e] = rbf(mul1(rbf(y[e]), sg));
      if (g == 0) *(f4*)(lds + L_YS + i * 256 + sl * 32 + (np >> 3) * 16) = y;
    }
    lds_arrive(cnt);
    CPL(5);
    STMP(32);
    STDUMP();
    return;
  }
  char* xt = lds + L_XT + (ow * 32 + np) * 128 + (g & 1) * 8;
  unsigned woad[2][4];
  { const int q = np >> 2, p = l & 3, row = 4 * g + q;
#pragma unroll
    for (int ct = 0; ct < 4; ++ct) { woad[0][ct] = ldsb + L_WO + row * 128 + ((ct ^ ((row >> 1) & 3)) * 32) + p * 8; woad[1][ct] = woad[0][ct] + 65536; } }
  s4 b[2][16];
#define K3RD(hc, i) RDTR(b[(hc) & 1][i], woad[((hc) >> 2) & 1][((i) >> 1) & 3], ((((hc) & 3) * 4 + ((hc) >> 3) * 2 + ((i) >> 3)) * 4096 + ((i) & 1) * 2048))
  K3RD(0, 0); K3RD(0, 1); K3RD(0, 2); K3RD(0, 3); K3RD(0, 4); K3RD(0, 5); K3RD(0, 6); K3RD(0, 7);
  K3RD(0, 8); K3RD(0, 9); K3RD(0, 10); K3RD(0, 11); K3RD(0, 12); K3RD(0, 13); K3RD(0, 14); K3RD(0, 15);
  const unsigned flg = l < 32 ? WS_OFL + ((mb2 * 2 + (ow >> 1)) * 32 + (np ^ (hx >> 1)) * 2 + g) * 128 : WS_OFF;
#pragma nounroll
  while ((int)((unsigned)__builtin_amdgcn_s_memrealtime() - tpoll) < 0) __builtin_amdgcn_s_sleep(1);
  STMP(26);
  unsigned rdy = 0;
#define OPOLL(MASK, SL) _Pragma("nounroll") for (unsigned it = 0; it < SPIN_CAP && (rdy & (MASK)) != (MASK); ++it) { \
    u2 f_ = LD64(((rdy >> (l & 31)) & 1u) ? WS_OFF : flg); \
    rdy |= (unsigned)__builtin_amdgcn_ballot_w64(f_[0] == tg[0] && f_[1] == tg[1]); \
    if ((SL) && rdy == 0) __builtin_amdgcn_s_sleep(SL); \
    CLOB(); }
#define OM0 0x00FFu
  OPOLL(OM0, OSL);
  CLOB();
  STMP(6);
#define OPF 3
#define OW0 6
#define OW 12
  u4 a[32]; u2 fl;
  const unsigned abase = WS_OF + (mb2 * 8 + ow * 2) * 32768 + l * 16, abase1 = abase + 32768;
#define OMK(f) ((f) == 16 ? 0xFFFFu : (f) == 32 ? 0xFFFFFFu : 0xFFFFFFFFu)
#define OLD(f) do { if ((f) < 64) { if ((f) && !((f) & 15) && (rdy & OMK(f)) != OMK(f)) { rdy |= (unsigned)__builtin_amdgcn_ballot_w64(fl[0] == tg[0] && fl[1] == tg[1]); OPOLL(OMK(f), 0); CLOB(); } \
    a[(f) & 31] = __builtin_amdgcn_raw_buffer_load_b128(wrs, (((f) & 1) ? abase1 : abase) + (((f) >> 5) * 2 + (((f) >> 1) & 1)) * 1024, ((((f) >> 2) & 7) * 4096) ^ (hx * 1024), 16); \
    if (((f) & 15) == OPF && (f) < 48) fl = LD64(((rdy >> (l & 31)) & 1u) ? WS_OFF : flg); } } while (0)
#pragma unroll
  for (int f = 0; f < OW0; ++f) OLD(f);
  if (ow == 0) { WAITL(0); lds_arrive(cnt + 4); WAITL(0); }
#define OHI(c) ((OW0 + 2 * (c)) < (OW + (c)) ? (OW0 + 2 * (c)) : (OW + (c)))
#define K3R(i) (((i) ^ ((i) >> 1)) & 1)
#define K3C(hc, i) ((hc) * 4 + ((i) >> 2))
#define K3M(hc, i) do { if (!((i) & 1)) WAITL((hc) < 15 ? 14 : 14 - (i)); SBAR(); \
    oc[K3R(i)][((i) >> 1) & 3] = MFMA16(CAT(b[(hc) & 1][(i) & ~1], b[(hc) & 1][(i) | 1]), BC8(a[((hc) * 4 + ((i) >> 3) * 2 + K3R(i)) & 31]), oc[K3R(i)][((i) >> 1) & 3]); SBAR(); \
    if ((hc) < 15) { K3RD((hc) + 1, i); } \
    if (((i) & 3) == 3) { OLD(OHI(K3C(hc, i) + 1) - 1); } else if (((i) & 3) == 1 && OHI(K3C(hc, i) + 1) - OHI(K3C(hc, i)) == 2) { OLD(OHI(K3C(hc, i))); } \
    SBAR(); } while (0)
#define K3H(hc) do { K3M(hc, 0); K3M(hc, 1); K3M(hc, 2); K3M(hc, 3); K3M(hc, 4); K3M(hc, 5); K3M(hc, 6); K3M(hc, 7); \
    K3M(hc, 8); K3M(hc, 9); K3M(hc, 10); K3M(hc, 11); K3M(hc, 12); K3M(hc, 13); K3M(hc, 14); K3M(hc, 15); } while (0)
  K3H(0); STMP(27); K3H(1); K3H(2); K3H(3); K3H(4); K3H(5); K3H(6); K3H(7); STMP(31); K3H(8); K3H(9); K3H(10); K3H(11); K3H(12); K3H(13); K3H(14); K3H(15);
  STMP(7);
  if (!all64(sel[0] < 0 && sel[1] < 0)) {
    lds_wait<0>(cnt, 4);
#pragma unroll
    for (int r = 0; r < 2; ++r)
      if (sel[r] >= 0) {
#pragma unroll
        for (int ct = 0; ct < 4; ++ct) oc[r][ct] += *(const f4*)(lds + L_YS + sel[r] * 256 + ct * 64 + g * 16);
      }
  }
#pragma unroll
  for (int r = 0; r < 2; ++r)
#pragma unroll
    for (int ct = 0; ct < 4; ++ct) *(u2*)XT(r, ct) = (u2){pk2(oc[r][ct][0], oc[r][ct][1]), pk2(oc[r][ct][2], oc[r][ct][3])};
  {
    const __amdgpu_buffer_rsrc_t ors = __builtin_amdgcn_make_buffer_rsrc(out, 0, S * D * 2, 0x00020000);
#pragma unroll
    for (int i = 0; i < 4; ++i) {
      const u4 v = *(const u4*)(lds + L_XT + (ow * 32 + 8 * i + rr) * 128 + ((sl ^ rr) * 16));
      __builtin_amdgcn_raw_buffer_store_b128(v, ors, (mb2 * 128 + ow * 32 + 8 * i + rr) * 2048 + nb * 128 + sl * 16, 0, 16);
    }
  }
  CPL(6);
  STMP(16);
  STDUMP();
}

extern "C" void kernel_launch(void* const* d_in, const int* in_sizes, int n_in, void* d_out, int out_size,
                              void* d_ws, size_t ws_size, hipStream_t stream) {
  const u16* x = (const u16*)d_in[0];
  const u16* wqkv = (const u16*)d_in[1];
  const u16* wo = (const u16*)d_in[2];
  const u16* wr = (const u16*)d_in[4];
  const u16* wg = (const u16*)d_in[5];
  const u16* wu = (const u16*)d_in[6];
  const u16* wd = (const u16*)d_in[7];
  (void)hipFuncSetAttribute((const void*)fused, hipFuncAttributeMaxDynamicSharedMemorySize, K_LDS);
  fused<<<256, 512, K_LDS, stream>>>(x, wqkv, wo, wr, wg, wu, wd, (char*)d_ws, (u16*)d_out);
}
```

```cpp
#include <hip/hip_runtime.h>
#include <hip/hip_bf16.h>
#include <stdint.h>

typedef unsigned short u16;
typedef unsigned long long u64;
typedef short s4 __attribute__((ext_vector_type(4)));
typedef short s8 __attribute__((ext_vector_type(8)));
typedef float f4 __attribute__((ext_vector_type(4)));
typedef float f2 __attribute__((ext_vector_type(2)));
typedef unsigned u4 __attribute__((ext_vector_type(4)));
typedef unsigned u2 __attribute__((ext_vector_type(2)));
typedef __bf16 bf2 __attribute__((ext_vector_type(2)));
#define LAS __attribute__((address_space(3)))
#define DEV __device__ __forceinline__

DEV float U2F(unsigned u) { return __builtin_bit_cast(float, u); }
DEV unsigned F2U(float f) { return __builtin_bit_cast(unsigned, f); }
DEV float I2F(int u) { return __builtin_bit_cast(float, u); }
DEV int F2I(float f) { return __builtin_bit_cast(int, f); }

constexpr int S = 2048, D = 1024, F = 3584;

constexpr unsigned WS_EP = 0;
constexpr unsigned WS_FLAG = 4096;
constexpr unsigned WS_MSG = 6144;
constexpr unsigned WS_HU = 8192;
constexpr unsigned WS_MS = 32768;
constexpr unsigned WS_CS = 256u << 10;
constexpr unsigned WS_GP = 512u << 10;
constexpr unsigned WS_H = 640u << 10;
constexpr unsigned WS_YP = 1280u << 10;
constexpr unsigned WS_KVP = 3328u << 10;
constexpr unsigned WS_OF = 6u << 20;
constexpr unsigned WS_OFL = 10u << 20;
constexpr unsigned WS_END = (10u << 20) + (128u << 10);
constexpr unsigned WS_OFF = 0x7FFF0000u;
constexpr unsigned KVS = 2304;
constexpr unsigned POISON = 0xFFFFFFFFu;

DEV unsigned pk2(float a, float b) { bf2 v; v[0] = (__bf16)a; v[1] = (__bf16)b; return __builtin_bit_cast(unsigned, v); }
DEV float lo(unsigned u) { return U2F(u << 16); }
DEV float hi(unsigned u) { return U2F(u & 0xFFFF0000u); }
DEV float rbf(float a) { return lo(pk2(a, a)); }
DEV float add1(float a, float b) { float r; asm("v_add_f32 %0, %1, %2" : "=v"(r) : "v"(a), "v"(b)); return r; }
DEV float mul1(float a, float b) { float r; asm("v_mul_f32 %0, %1, %2" : "=v"(r) : "v"(a), "v"(b)); return r; }
DEV unsigned san(unsigned u) { return min(u, 0xFFFFFFFEu); }
DEV u4 san4(u4 v) { v[0] = san(v[0]); v[1] = san(v[1]); v[2] = san(v[2]); v[3] = san(v[3]); return v; }
DEV unsigned mx3(unsigned a, unsigned b, unsigned c) { return max(max(a, b), c); }
DEV unsigned mx4(unsigned m, u4 v) { return mx3(mx3(m, v[0], v[1]), v[2], v[3]); }
DEV u4 tag4(f4 v, unsigned t) { u4 r; _Pragma("unroll") for (int e = 0; e < 4; ++e) r[e] = (F2U(v[e]) & 0xFFFFFF00u) | ((t >> (8 * e)) & 0xFFu); return r; }
DEV f4 untag4(u4 v, unsigned t, unsigned& bad) {
  f4 r; _Pragma("unroll") for (int e = 0; e < 4; ++e) { const unsigned u = v[e] ^ ((t >> (8 * e)) & 0xFFu); bad |= u; r[e] = U2F(u); } return r; }
DEV bool tagok(unsigned bad) { return __builtin_amdgcn_ballot_w64((bad & 0xFFu) != 0u) == 0ull; }
DEV bool clean(unsigned m) { return __builtin_amdgcn_ballot_w64(m == POISON) == 0ull; }
DEV bool all64(bool c) { return __builtin_amdgcn_ballot_w64(!c) == 0ull; }
DEV float phi1(float a) { return __builtin_fmaf(fmaxf(a, 1.44269504e-6f), 0.693147181f, __builtin_amdgcn_fmed3f(__builtin_amdgcn_exp2f(a), 0.f, 1.f)); }
DEV unsigned phi2(f2 a) {
  const f2 e = (f2){__builtin_amdgcn_fmed3f(__builtin_amdgcn_exp2f(a[0]), 0.f, 1.f), __builtin_amdgcn_fmed3f(__builtin_amdgcn_exp2f(a[1]), 0.f, 1.f)};
  const f2 r = __builtin_elementwise_fma((f2){fmaxf(a[0], 1.44269504e-6f), fmaxf(a[1], 1.44269504e-6f)}, (f2)(0.693147181f), e);
  return pk2(r[0], r[1]);
}
#define RD128(dst, addr, OFF) asm volatile("ds_read_b128 %0, %1 offset:%2" : "=v"(dst) : "v"(addr), "i"(OFF) : "memory")
#define RD64(dst, addr, OFF) asm volatile("ds_read_b64 %0, %1 offset:%2" : "=v"(dst) : "v"(addr), "i"(OFF) : "memory")
#define RD32(dst, addr, OFF) asm volatile("ds_read_b32 %0, %1 offset:%2" : "=v"(dst) : "v"(addr), "i"(OFF) : "memory")
#define RDTR(dst, addr, OFF) asm volatile("ds_read_b64_tr_b16 %0, %1 offset:%2" : "=v"(dst) : "v"(addr), "i"(OFF) : "memory")
#define SBAR() __builtin_amdgcn_sched_barrier(0)
#define CAT(x, y) __builtin_shufflevector(x, y, 0, 1, 2, 3, 4, 5, 6, 7)
#define WAITV(n) asm volatile("s_waitcnt vmcnt(%0)" ::"i"(n) : "memory")
#define WAITL(n) asm volatile("s_waitcnt lgkmcnt(%0)" ::"i"(n) : "memory")
#define CLOB() asm volatile("" ::: "memory")
#define BAR() asm volatile("s_barrier" ::: "memory")
#define LBAR() asm volatile("s_waitcnt lgkmcnt(0)\n s_barrier" ::: "memory")
#define PIN(v) asm volatile("" : "+v"(v))
#define PACE(n) do { SBAR(); __builtin_amdgcn_s_sleep(n); SBAR(); } while (0)
#define RFL(v) ((unsigned)__builtin_amdgcn_readfirstlane(v))
DEV const char* uptr(const char* p) {
  u64 v = (u64)p;
  return (const char*)(((u64)RFL((unsigned)(v >> 32)) << 32) | RFL((unsigned)v));
}
DEV u4 mkrs(const char* p, unsigned bytes) {
  const u64 v = (u64)uptr(p);
  u4 r; r[0] = (unsigned)v; r[1] = (unsigned)(v >> 32) & 0xFFFFu; r[2] = RFL(bytes); r[3] = 0x00020000u;
  return r;
}
#define BLDS(voff, rs, so, OFF) asm volatile("buffer_load_dwordx4 %0, %1, %2 offen offset:%3 lds" ::"v"(voff), "s"(rs), "s"(so), "i"(OFF) : "memory")
#define MFMA16(a, b, c) __builtin_amdgcn_mfma_f32_16x16x32_bf16(a, b, c, 0, 0, 0)
#define MFMAH(a, b, c) __builtin_amdgcn_mfma_f32_16x16x16bf16_1k(__builtin_bit_cast(s4, (u2){(a), 0u}), __builtin_bit_cast(s4, (u2){(b), 0u}), c, 0, 0, 0)
#define DOT2(a, b, c) __builtin_amdgcn_fdot2_f32_bf16(__builtin_bit_cast(bf2, (unsigned)(a)), __builtin_bit_cast(bf2, (unsigned)(b)), c, false)
#define BC8(v) __builtin_bit_cast(s8, v)
#define LD128(off) __builtin_amdgcn_raw_buffer_load_b128(wrs, (off), 0, 16)
#define LD64(off) __builtin_amdgcn_raw_buffer_load_b64(wrs, (off), 0, 16)
#define LD32(off) __builtin_amdgcn_raw_buffer_load_b32(wrs, (off), 0, 16)
#define ST128(v, off) __builtin_amdgcn_raw_buffer_store_b128((v), wrs, (off), 0, 16)
#define ST64(v, off) __builtin_amdgcn_raw_buffer_store_b64((v), wrs, (off), 0, 16)
#define ST32(v, off) __builtin_amdgcn_raw_buffer_store_b32((v), wrs, (off), 0, 16)
#define ALD128(dst, off) asm volatile("buffer_load_dwordx4 %0, %1, %2, 0 offen sc1" : "=v"(dst) : "v"(off), "s"(rsW) : "memory")
#define ALD64(dst, off) asm volatile("buffer_load_dwordx2 %0, %1, %2, 0 offen sc1" : "=v"(dst) : "v"(off), "s"(rsW) : "memory")
#define ALD32(dst, off) asm volatile("buffer_load_dword %0, %1, %2, 0 offen sc1" : "=v"(dst) : "v"(off), "s"(rsW) : "memory")
constexpr unsigned SPIN_CAP = 1u << 20;

DEV void lds_arrive(LAS unsigned* c) {
  asm volatile("s_mov_b64 exec, 1\n ds_add_u32 %0, %1\n s_mov_b64 exec, -1" ::"v"((unsigned)(size_t)c), "v"(1u) : "memory");
}
template <int SL> DEV void lds_wait(LAS unsigned* c, unsigned target) {
#pragma nounroll
  while (RFL(__hip_atomic_load(c, __ATOMIC_RELAXED, __HIP_MEMORY_SCOPE_WORKGROUP)) < target) { if (SL) __builtin_amdgcn_s_sleep(SL); }
  CLOB();
}

#define STMP(idx)
#define STMC(idx)
#define STDUMP()
#define DPPF(v, ctrl, rm) I2F(__builtin_amdgcn_update_dpp(0, F2I(v), ctrl, rm, 0xf, false))
DEV unsigned wave_max(unsigned v) {
  v = max(v, (unsigned)__builtin_amdgcn_update_dpp(0, (int)v, 0x111, 0xf, 0xf, false));
  v = max(v, (unsigned)__builtin_amdgcn_update_dpp(0, (int)v, 0x112, 0xf, 0xf, false));
  v = max(v, (unsigned)__builtin_amdgcn_update_dpp(0, (int)v, 0x114, 0xf, 0xf, false));
  v = max(v, (unsigned)__builtin_amdgcn_update_dpp(0, (int)v, 0x118, 0xf, 0xf, false));
  v = max(v, (unsigned)__builtin_amdgcn_update_dpp(0, (int)v, 0x142, 0xa, 0xf, false));
  v = max(v, (unsigned)__builtin_amdgcn_update_dpp(0, (int)v, 0x143, 0xc, 0xf, false));
  return (unsigned)__builtin_amdgcn_readlane((int)v, 63);
}

constexpr int SLOT = 49152, L_B = 32768;
constexpr int L_EX = SLOT;
constexpr int L_ROWM = 159744;
constexpr int L_ROWS = L_ROWM + 1024;
constexpr int L_COL = L_ROWS + 1024;
constexpr int L_TAG = L_COL + 512;
constexpr int L_CNT = L_TAG + 64;
constexpr int L_TOP = L_CNT + 64;
constexpr int L_XT = 0;
constexpr int L_KX = 16384;
constexpr int L_ZX = L_KX + 8192;
constexpr int L_YS = L_ZX + 256;
constexpr int L_WO = 28672;
constexpr int K_LDS = 163840;

#define NTL(p) __builtin_nontemporal_load(p)
#define TRFRAG(f, W, c) do { _Pragma("unroll") for (int q_ = 0; q_ < 4; ++q_) { unsigned e_ = W[2 * q_][(c) >> 1], o_ = W[2 * q_ + 1][(c) >> 1]; \
    f[q_] = ((c) & 1) ? __builtin_amdgcn_perm(o_, e_, 0x07060302u) : __builtin_amdgcn_perm(o_, e_, 0x05040100u); } } while (0)
constexpr u64 TAG_STEP = 0x9E3779B97F4A7C15ull;

struct FW { s8 a[8]; s4 b[8]; };
#define RA(G, s, u, i) RD128(G.a[i], aad[((s) % 3) == 2][u], (((s) % 3) & 1) * SLOT + (i) * 2048)
#define RB(G, s, u, n) RDTR(G.b[n], bad[((s) % 3) == 2][((n) >> 1) & 1], (((s) % 3) & 1) * SLOT + ((n) >> 2) * 8192 + (u) * 4096 + ((n) & 1) * 512)
#define FB(G, j) CAT(G.b[2 * (j)], G.b[2 * (j) + 1])
#define RW(G, s, u, n) do { if ((n) < 8) RB(G, s, u, (n) & 7); else RA(G, s, u, ((n) - 8) & 7); SBAR(); } while (0)
#define SNK(n) ((((n) >> 2) & 1) ? 3 - ((n) & 3) : (n) & 3)
#define MW(G, n) do { if (TRN) c[(n) >> 2][SNK(n)] = MFMA16(FB(G, SNK(n)), G.a[(n) >> 2], c[(n) >> 2][SNK(n)]); \
    else c[(n) >> 2][SNK(n)] = MFMA16(G.a[(n) >> 2], FB(G, SNK(n)), c[(n) >> 2][SNK(n)]); SBAR(); } while (0)
#define MFA(c_, a_, b_) asm volatile("v_mfma_f32_16x16x32_bf16 %0, %1, %2, %0" : "+v"(c_) : "v"(a_), "v"(b_))
#define MA(G, n) do { if (TRN) MFA(c[(n) >> 2][SNK(n)], FB(G, SNK(n)), G.a[(n) >> 2]); else MFA(c[(n) >> 2][SNK(n)], G.a[(n) >> 2], FB(G, SNK(n))); SBAR(); } while (0)
#define MW2(Fq, G, sn, un, n) do { MA(Fq, 2 * (n)); if ((n) < 12) RW(G, sn, un, (n) + ((n) + 2) / 3); MA(Fq, 2 * (n) + 1); if ((n) < 12 && (n) % 3 == 0) RW(G, sn, un, (n) + (n) / 3 + 1); } while (0)
#define HALFW(Fq, G, sn, un) do { MW2(Fq, G, sn, un, 0); MW2(Fq, G, sn, un, 1); MW2(Fq, G, sn, un, 2); MW2(Fq, G, sn, un, 3); MW2(Fq, G, sn, un, 4); MW2(Fq, G, sn, un, 5); MW2(Fq, G, sn, un, 6); MW2(Fq, G, sn, un, 7); \
    MW2(Fq, G, sn, un, 8); MW2(Fq, G, sn, un, 9); MW2(Fq, G, sn, un, 10); MW2(Fq, G, sn, un, 11); MW2(Fq, G, sn, un, 12); MW2(Fq, G, sn, un, 13); MW2(Fq, G, sn, un, 14); MW2(Fq, G, sn, un, 15); WAITL(0); SBAR(); } while (0)
#define STAGEW(s) do { HALFW(f0, f1, s, 1); BAR(); SBAR(); HALFW(f1, f0, (s) + 1, 0); } while (0)
#define KLOOP() \
      unsigned aad[2][2], bad[2][2]; \
      { LANE(); const int q = np >> 2, p = l & 3, f = ((q >> 1) & 1) | ((g & 1) << 1); \
        _Pragma("unroll") for (int u = 0; u < 2; ++u) { aad[0][u] = ldsb + (wm * 128 + np) * 128 + (((4 * u + g) ^ (np >> 1)) * 16); aad[1][u] = aad[0][u] + 2 * SLOT; \
          bad[0][u] = ldsb + L_B + (8 * g + q) * 128 + (((2 * wn + u) ^ f) * 32) + p * 8; bad[1][u] = bad[0][u] + 2 * SLOT; \
          PIN(aad[0][u]); PIN(aad[1][u]); PIN(bad[0][u]); PIN(bad[1][u]); } } \
      f4 c[8][4]; \
      _Pragma("unroll") for (int i = 0; i < 8; ++i) _Pragma("unroll") for (int jj = 0; jj < 4; ++jj) { c[i][jj] = (f4)(0.f); PIN(c[i][jj]); } \
      FW f0, f1; \
      BAR(); \
      STMP(1); STMC(40); \
      RW(f0, 0, 0, 0); RW(f0, 0, 0, 1); RW(f0, 0, 0, 8); RW(f0, 0, 0, 2); RW(f0, 0, 0, 3); RW(f0, 0, 0, 4); RW(f0, 0, 0, 5); RW(f0, 0, 0, 6); RW(f0, 0, 0, 7); \
      RW(f0, 0, 0, 9); RW(f0, 0, 0, 10); RW(f0, 0, 0, 11); RW(f0, 0, 0, 12); RW(f0, 0, 0, 13); RW(f0, 0, 0, 14); RW(f0, 0, 0, 15); \
      WAITL(0); SBAR(); \
      STAGEW(0); STAGEW(1); STAGEW(2); STAGEW(3); STAGEW(4); STAGEW(5); STAGEW(6); STAGEW(7); \
      STAGEW(8); STAGEW(9); STAGEW(10); STAGEW(11); STAGEW(12); STAGEW(13); STAGEW(14); \
      unsigned tR, tC; \
      { LANE(); tR = ldsb + L_ROWM + (wm * 128 + (TRN ? np : 4 * g)) * 4; tC = ldsb + L_COL + (wn * 32 + (TRN ? 4 * g : np)) * 4; PIN(tR); PIN(tC); SBAR(); } \
      HALFW(f0, f1, 15, 1); if (TRN) BAR(); SBAR()
#define MT(n) do { MW(f1, n); TB(n); } while (0)
#define KLAST() do { MT(0); MT(1); MT(2); MT(3); MT(4); MT(5); MT(6); MT(7); MT(8); MT(9); MT(10); MT(11); MT(12); MT(13); MT(14); MT(15); \
      MT(16); MT(17); MT(18); MT(19); MT(20); MT(21); MT(22); MT(23); MT(24); MT(25); MT(26); MT(27); MT(28); MT(29); MT(30); MT(31); WAITL(0); } while (0)
#define H2(v, e0) ((f2){(v)[e0], (v)[(e0) + 1]})

#define WQ0 56
#define HXM 0
#define QROT2 1
#define MORD 3
#define PO 0
#define FI (PO ? 3 : 11)
#define PM 2
#define YW 1
#define HSL 16
#define DD0 0
#define DD1 0
#define KD 30
#define KSL 2
#define PD5 8
#define OSL 1
#define MD1 4
#define MD2 1
#define TKP 13
#define CL0 9

__global__ __launch_bounds__(512, 2) void fused(const u16* __restrict__ x, const u16* __restrict__ wqkv, const u16* __restrict__ wo, const u16* __restrict__ wr,
                                                const u16* __restrict__ wg, const u16* __restrict__ wu, const u16* __restrict__ wd, char* ws,
                                                u16* __restrict__ out) {
  extern __shared__ __attribute__((aligned(16))) char lds[];
  const int w = __builtin_amdgcn_readfirstlane(threadIdx.x >> 6);
#define LANE() int l; asm volatile("v_mbcnt_lo_u32_b32 %0, -1, 0\n v_mbcnt_hi_u32_b32 %0, -1, %0" : "=v"(l)); \
  const int np = l & 15, g = l >> 4, rr = l >> 3, sl = l & 7; (void)np; (void)g; (void)rr; (void)sl
  STMP(0);
  const int gb = blockIdx.x, xcd = gb & 7, j = gb >> 3, xq = xcd >> 1;
  const unsigned ldsb = (unsigned)(size_t)(LAS char*)lds;
  const bool isQ = j >= 16;
  const int rg = (xcd & 1) * 4 + (j & 3), hp = xq * 4 + (j >> 2), qg = xq * 2 + ((j >> 2) & 1);
  const int hd0 = isQ ? qg * 4 : 32 + hp * 2, hd1 = isQ ? 2 : 32;
  const bool isB = w == 6;
  unsigned mb_, step, d1, d2, d3, so, lim; u4 rs;
#define VSET() unsigned v[4]; { const unsigned pitch = isB ? 6144 : 2048, sm = isB ? 2 : 7; \
    _Pragma("unroll") for (int i = 0; i < 4; ++i) v[i] = (8 * i + rr) * pitch + ((sl ^ ((isB ? rr : rr >> 1) & sm) ^ ((i & 1) << 2)) * 16) - i * 1024; }
#define QROT 1
#define G4(s, q, dq) do { unsigned t_; asm volatile("s_add_u32 m0, %1, %2\n s_add_u32 %0, %3, %4" : "=s"(t_) : "s"(mb_), "i"(((s) % 3) * SLOT + (q) * 4096), "s"(so), "s"(dq) : "scc", "memory"); \
        BLDS(v[0], rs, t_, 0); BLDS(v[1], rs, t_, 1024); BLDS(v[2], rs, t_, 2048); BLDS(v[3], rs, t_, 3072); } while (0)
#define PA(s) do { G4(s, 0, 0u); G4(s, 1, d1); } while (0)
#define PB(s) do { G4(s, 2, d2); G4(s, 3, d3); asm volatile("s_add_u32 %0, %0, %1\n s_cmp_eq_u32 %0, %2\n s_cselect_b32 %0, 0, %0" : "+s"(so) : "s"(step), "s"(lim) : "scc"); } while (0)
  if (__builtin_expect(j < 24 && w >= 4 && w < 7, 1)) {
    LANE();
    rs = isB ? mkrs((const char*)wqkv + hd0 * 64, 1024 * 6144 - hd0 * 64) : mkrs((const char*)x + (size_t)(rg * 256 + (w - 4) * 128) * 2048, 128 * 2048);
    mb_ = RFL(ldsb + (w - 4) * 16384); step = isB ? 64 * 6144 : 128;
    d1 = isB ? 32 * 6144 : 65536; d2 = isB ? hd1 * 64 : 131072; d3 = d1 + d2;
    so = isQ ? (QROT + QROT2 * (qg & 1)) * step : 0; lim = 16 * step;
    VSET();
    PA(0); PB(0); PA(1);
  }
#define CPF 1
  u4 rsC;
  { u64 pc; unsigned len;
    asm volatile("s_getpc_b64 %0\n.Lcp0:\n s_mov_b32 %1, .Lcp1-.Lcp0\n s_max_i32 %1, %1, .Lcp2-.Lcp0\n s_max_i32 %1, %1, .Lcp3-.Lcp0\n s_max_i32 %1, %1, .Lcp4-.Lcp0\n s_max_i32 %1, %1, .Lcp5-.Lcp0\n s_max_i32 %1, %1, .Lcp6-.Lcp0\n s_max_i32 %1, %1, 0" : "=&s"(pc), "=&s"(len) :: "scc");
    rsC = (u4){(unsigned)pc & ~127u, (unsigned)(pc >> 32) & 0xFFFFu, len, 0x00020000u}; }
#define CODE(n) asm volatile("s_mov_b32 m0, %2\n s_nop 0\n buffer_load_dwordx4 %0, %1, 0 offen lds" :: "v"((n) * 1024 + l * 16), "s"(rsC), "s"(RFL(ldsb + 3 * SLOT)) : "memory")
  if (CPF && j < 24 && w < 4) { LANE(); CODE(j * 4 + w); }
#define CPL(n) asm volatile(".Lcp" #n ":")
  const __amdgpu_buffer_rsrc_t wrs = __builtin_amdgcn_make_buffer_rsrc(ws, 0, WS_END, 0x00020000);
  const int mb2 = (xcd & 1) * 8 + (xcd >> 2) * 4 + (j >> 3), nb = ((xcd >> 1) & 1) * 8 + (j & 7), lq = (mb2 + (mb2 >> 2)) & 3, hx = (nb * 4) & HXM;
#define msg (WS_MSG + xcd * 256 + ((l & 8) ? 0 : 64 + sl * 8))
#define MSGOK(f) all64((((l & 8) ? ((f)[0] ^ tlo) | ((f)[1] ^ thi) : (f)[1] ^ tlo)) == 0u)
#define KEYOK(f) all64((l & 8) || (f)[1] == tlo)
#define GBAD(v) (((v)[1] ^ tlo) | ((v)[3] ^ thi))
#define WO_SETUP() const u4 rsO = mkrs((const char*)wo + nb * 128, 1024 * 2048 - nb * 128), rsX = mkrs((const char*)x + (size_t)mb2 * 128 * 2048 + nb * 128, 128 * 2048 - nb * 128); \
  const unsigned vW = rr * 2048 + ((sl ^ (((rr >> 1) & 3) << 1)) * 16), vX = rr * 2048 + ((sl ^ rr) * 16)
#define DMA1(v, rs, m, so) asm volatile("s_mov_b32 m0, %2\n s_nop 0\n buffer_load_dwordx4 %0, %1, %3 offen lds" ::"v"(v), "s"(rs), "s"(RFL(m)), "s"(RFL(so)) : "memory")
#define WOL(n0, n1, st) _Pragma("nounroll") for (int n_ = (n0); n_ < (n1); n_ += (st)) DMA1(vW, rsO, ldsb + L_WO + n_ * 1024, (n_ * 16384) ^ (hx * 65536))
#define WOR(n0, n1, st) _Pragma("nounroll") for (int n_ = (n0); n_ < (n1); n_ += (st)) DMA1(vW, rsO, ldsb + L_WO + (n_ ^ (lq * 32) ^ (hx * 4)) * 1024, (n_ ^ (lq * 32)) * 16384)
#define XTL(n0, st) _Pragma("nounroll") for (int n_ = (n0); n_ < 16; n_ += (st)) DMA1(vX, rsX, ldsb + L_XT + n_ * 1024, n_ * 16384)
  const int ow = isQ && j < 24 ? w ^ 4 : w;
  f4 oc[2][4];
  u4 st2[2];
  unsigned tpoll = 0;
#define TPOLL(d) tpoll = (unsigned)__builtin_amdgcn_s_memrealtime() + (d)
#define PDK 280
#define PDQ 80
#define XT(r, ct) (xt + (r) * 2048 + (((2 * (ct) + (g >> 1)) ^ sl) * 16))
#define OINIT() do { const char* xt = lds + L_XT + (ow * 32 + np) * 128 + (g & 1) * 8; \
    { const u2 t_ = *(const u2*)(lds + L_TAG); _Pragma("nounroll") for (unsigned it = 0; it < SPIN_CAP && !all64(((st2[0][1] ^ t_[0]) | (st2[0][3] ^ t_[1]) | (st2[1][1] ^ t_[0]) | (st2[1][3] ^ t_[1])) == 0u); ++it) { CLOB(); ST2(); } } \
    _Pragma("unroll") for (int r = 0; r < 2; ++r) { \
      const float c1 = 1.f + U2F(st2[r][2]), c0 = -hi(st2[r][0]) * U2F(st2[r][2]); \
      _Pragma("unroll") for (int ct = 0; ct < 4; ++ct) { const u2 v = *(const u2*)XT(r, ct); \
        oc[r][ct] = (f4){__builtin_fmaf(lo(v[0]), c1, c0), __builtin_fmaf(hi(v[0]), c1, c0), __builtin_fmaf(lo(v[1]), c1, c0), __builtin_fmaf(hi(v[1]), c1, c0)}; PIN(oc[r][ct]); } \
      } } while (0)
#define XOWN() do { WO_SETUP(); (void)vW; (void)rsO; _Pragma("unroll") for (int n_ = 0; n_ < 4; ++n_) DMA1(vX, rsX, ldsb + L_XT + (ow * 4 + n_) * 1024, (ow * 4 + n_) * 16384); ST2(); } while (0)
#define ST2() do { const unsigned sto = WS_MS + (mb2 * 128 + (w & 3) * 32 + np) * 16; st2[0] = LD128(sto); st2[1] = LD128(sto + 256); } while (0)

  if (j < 24) {
    if (w >= 4) {
      LANE();
      unsigned tlo = 0, thi = 0;
      WO_SETUP();
      u2 f = (u2){0u, 0u};
      if (w == 7) {
        const u4 rsW = mkrs(ws, WS_END);
        int nbar = 16;
#define TICK() do { if (nbar) { BAR(); --nbar; } } while (0)
        if (l < 16) ((unsigned*)(lds + L_CNT))[l] = 0u;
        WAITL(0);
        TICK();
        u2 ep; unsigned z0 = 0; PIN(z0);
        ALD64(ep, z0);
        { const u4 pz = (u4){POISON, POISON, POISON, POISON};
          if (isQ) {
            ST128(pz, WS_OFL + ((rg * 2 + (l >> 5)) * 32 + qg * 4) * 128 + (l & 31) * 16);
          } else {
            const unsigned kb = WS_KVP + (hp * 16 + rg) * KVS + l * 16;
            ST128(pz, kb); ST128(pz, kb + 1024); ST128(pz, kb + 8 * KVS); ST128(pz, kb + 8 * KVS + 1024);
            ST128(pz, l < 16 ? WS_KVP + (hp * 16 + rg + (l & 8)) * KVS + 2048 + sl * 16 : WS_OFF);
          } }
        SBAR();
        if (PO && isQ) {
#define PPS 8
          TICK();
#pragma nounroll
          for (int i = 0; i < 16; i += PPS) {
            TICK();
            const unsigned ob = WS_OF + ((rg * 16 + i) * 32 + qg * 4) * 1024 + l * 16; const u4 pz = (u4){POISON, POISON, POISON, POISON};
#pragma unroll
            for (int k = 0; k < PPS; ++k) { ST128(pz, ob + k * 32768); ST128(pz, ob + k * 32768 + 1024); }
          }
        } else TICK();
        TICK(); TICK();
        WAITV(0); PIN(ep);
        const u64 tag = (((u64)RFL(ep[1]) << 32) | RFL(ep[0])) + TAG_STEP;
        tlo = (unsigned)tag; thi = (unsigned)(tag >> 32);
        ST64(((u2){tlo, thi}), l == 0 ? WS_FLAG + gb * 8 : WS_OFF);
        STMP(8);
#pragma nounroll
        while (nbar > 16 - TKP) { TICK();
#define CPF2 10
          if (CPF2 && nbar == 16 - CPF2) { CODE(j * 4); CODE(j * 4 + 1); CODE(j * 4 + 2); CODE(j * 4 + 3); }
        }
        u4 ms[4], cs[8];
        const unsigned a0 = WS_MS + (rg * 256 + l) * 16, a2 = WS_CS + ((g * 96 + hd0) * 16 + np) * 16, a3 = a2 + hd1 * 256, a4 = a2 + 4 * 96 * 256, a5 = a3 + 4 * 96 * 256, a6 = msg;
#pragma nounroll
        for (unsigned it = 0; it < SPIN_CAP; ++it) {
#pragma unroll
          for (int i = 0; i < 4; ++i) asm volatile("buffer_load_dwordx4 %0, %1, %2, 0 offen offset:%3 sc1" : "=v"(ms[i]) : "v"(a0), "s"(rsW), "i"(i * 1024) : "memory");
#pragma unroll
          for (int i = 0; i < 8; ++i) asm volatile("buffer_load_dwordx4 %0, %1, %2, 0 offen offset:%3 sc1" : "=v"(cs[i]) : "v"(i < 2 ? a2 : i < 4 ? a3 : i < 6 ? a4 : a5), "s"(rsW), "i"((i & 1) * 256) : "memory");
          ALD64(f, a6);
          if (nbar > 1) TICK();
          if (nbar > 1) TICK();
          WAITV(0);
          unsigned bad = 0;
#pragma unroll
          for (int i = 0; i < 4; ++i) { PIN(ms[i]); PIN(cs[i]); PIN(cs[4 + i]); bad |= GBAD(ms[i]) | GBAD(cs[i]) | GBAD(cs[4 + i]); }
          PIN(f);
          if (all64(bad == 0u)) break;
        }
        STMP(9);
        float ct[2];
#pragma unroll
        for (int e = 0; e < 2; ++e) {
          float t[4];
#pragma unroll
          for (int i = 0; i < 4; ++i) t[i] = add1(U2F(cs[i][2 * e]), U2F(cs[4 + i][2 * e]));
#pragma unroll
          for (int i = 0; i < 2; ++i) { auto r = __builtin_amdgcn_permlane32_swap(F2U(t[i]), F2U(t[i + 2]), false, false); t[i] = add1(U2F(r[0]), U2F(r[1])); }
          auto r = __builtin_amdgcn_permlane16_swap(F2U(t[0]), F2U(t[1]), false, false);
          ct[e] = add1(U2F(r[0]), U2F(r[1])); PIN(ct[e]);
        }
#pragma unroll
        for (int i = 0; i < 4; ++i) { ms[i][0] = (ms[i][0] & 0xFFFF0000u) ^ 0x80000000u; ms[i][1] = F2U(mul1(U2F(ms[i][2]), 1.44269504f)); PIN(ms[i]); }
#pragma nounroll
        while (nbar) TICK();
#pragma unroll
        for (int i = 0; i < 4; ++i) { ((unsigned*)(lds + L_ROWM))[i * 64 + l] = ms[i][0]; ((unsigned*)(lds + L_ROWS))[i * 64 + l] = ms[i][1]; }
        ((float*)(lds + L_COL))[g * 32 + np] = ct[0]; ((float*)(lds + L_COL))[g * 32 + 16 + np] = ct[1];
        if (l == 8) *(u2*)(lds + L_TAG) = (u2){tlo, thi};
        if (!isQ) lds_arrive((LAS unsigned*)(lds + L_CNT) + 5);
      } else {
        VSET();
        WAITV(8); BAR(); PB(1); PA(2); WAITV(8); BAR(); STMP(44); PB(2); PA(0); PB(0);
#define LS(s) do { if ((s) < 14) WAITV(16); else WAITV(0); BAR(); STMP(44 + (s)); if ((s) + 3 < 16) { PA(s); PB(s); } } while (0)
        LS(1); LS(2); LS(3); LS(4); LS(5); LS(6); LS(7); LS(8); LS(9); LS(10); LS(11); LS(12);
      }
#define KEYS() do { if (w == 7) { _Pragma("nounroll") for (unsigned it = 0; it < SPIN_CAP && !MSGOK(f); ++it) { f = LD64(msg); CLOB(); } \
        if (l < 8) ((unsigned*)(lds + L_TAG + 16))[l] = f[0]; \
        lds_arrive((LAS unsigned*)(lds + L_CNT) + 3); } } while (0)
      if (isQ) {
        if (w < 7) { WAITV(16); BAR(); STMP(57); WAITV(0); BAR(); STMP(58); WOL(20 + w - 4, WQ0, 3); }
        LBAR(); STMP(59);
        XOWN(); SBAR(); WOL(WQ0 + w - 4, 128, 4); WOL(w - 4, 20, 4);
        const unsigned tq = (unsigned)__builtin_amdgcn_s_memrealtime();
        KEYS();
        WAITV(0);
        OINIT();
        STMP(21);
        const unsigned sb = WS_KVP + (qg * 4 + (w & 1)) * 8 * KVS + ((w >> 1) & 1) * 1024 + l * 16;
        const unsigned sz = (w & 2) ? WS_OFF : WS_KVP + ((qg * 4 + (w & 1)) * 8 + (np >> 1)) * KVS + 2048 + (np & 1) * 64 + g * 16;
        u4 prm[2];
#pragma unroll
        for (int e = 0; e < 4; ++e) {
          const unsigned o = (np >> 2) == g ? ((np & 1) ? 0x3F800000u : 0x00003F80u) : 0u;
          prm[0][e] = e == ((np >> 1) & 1) ? o : 0u; prm[1][e] = e == 2 + ((np >> 1) & 1) ? o : 0u;
        }
        PIN(prm[0]); PIN(prm[1]);
        __builtin_amdgcn_s_setprio(2);
#pragma nounroll
        while ((unsigned)__builtin_amdgcn_s_memrealtime() - tq < KD) __builtin_amdgcn_s_sleep(1);
        lds_wait<1>((LAS unsigned*)(lds + L_CNT) + 3, 1);
        u4 v[16], zv[2]; f4 kg[4][2];
#define KVLD(gq) do { _Pragma("unroll") for (int p = 4 * (gq); p < 4 * (gq) + 4; ++p) v[p] = __builtin_amdgcn_raw_buffer_load_b128(wrs, sb, ((p & 7) + (p >> 3) * 16) * KVS, 16); } while (0)
#define ZLD(hh) zv[hh] = __builtin_amdgcn_raw_buffer_load_b128(wrs, sz, (hh) * 16 * KVS, 16)
        ZLD(0); KVLD(0); KVLD(1); SBAR(); ZLD(1); KVLD(2); KVLD(3); SBAR();
#pragma unroll
        for (int hh = 0; hh < 2; ++hh) {
          unsigned pend = 7;
#pragma nounroll
          for (unsigned it = 0; it < SPIN_CAP; ++it) {
#pragma unroll
            for (int gq = 2 * hh; gq < 2 * hh + 2; ++gq)
              if (pend & (1u << (gq & 1))) {
                f4 t0_ = (f4)(0.f), t1_ = (f4)(0.f); unsigned mx = 0;
#pragma unroll
                for (int p = 4 * gq; p < 4 * gq + 4; ++p) { mx = mx4(mx, v[p]); t0_ = MFMA16(BC8(prm[0]), BC8(v[p]), t0_); t1_ = MFMA16(BC8(prm[1]), BC8(v[p]), t1_); }
                kg[gq][0] = t0_; kg[gq][1] = t1_;
                if (clean(mx)) pend &= ~(1u << (gq & 1));
              }
            if ((pend & 4u) && clean(mx4(0u, zv[hh]))) pend &= ~4u;
            if (!pend) break;
            if (KSL) __builtin_amdgcn_s_sleep(KSL);
            CLOB();
            if (pend & 4u) ZLD(hh);
            if (pend & 1u) KVLD(2 * hh);
            if (pend & 2u) KVLD(2 * hh + 1);
            if (hh == 0) { ZLD(1); KVLD(2); KVLD(3); }
            SBAR();
          }
          if (hh) STMP(25); else STMP(4);
          const f4 k0 = kg[2 * hh][0] + kg[2 * hh + 1][0], k1 = kg[2 * hh][1] + kg[2 * hh + 1][1];
          *(u4*)(lds + L_KX + ((2 * hh + (w & 1)) * 2 + ((w >> 1) & 1)) * 1024 + l * 16) = (u4){pk2(k0[0], k0[1]), pk2(k0[2], k0[3]), pk2(k1[0], k1[1]), pk2(k1[2], k1[3])};
          if (!(w & 2)) {
            f4 z = __builtin_bit_cast(f4, zv[hh]);
#pragma unroll
            for (int e = 0; e < 4; ++e) { z[e] += DPPF(z[e], 0x122, 0xf); z[e] += DPPF(z[e], 0x124, 0xf); z[e] += DPPF(z[e], 0x128, 0xf); }
            if (np < 2) *(u2*)(lds + L_ZX + (2 * hh + (w & 1)) * 64 + g * 16 + np * 8) = (u2){pk2(z[0], z[1]), pk2(z[2], z[3])};
          }
          LBAR();
        }
        __builtin_amdgcn_s_setprio(0);
        CPL(3);
        TPOLL(PDQ);
      } else {
        if (w >= 6) {
          if (w == 6) { WAITV(16); BAR(); WAITV(0); BAR(); }
          KEYS();
        } else {
          const int kd = j * 8 + xcd, dc = kd >> 2;
          const bool down = kd < 112;
          u4 dw[4][8];
          const u4 rsD = mkrs((const char*)wd + (size_t)dc * 128 * 2048 + (kd & 3) * 512 + (w - 4) * 256, down ? 127 * 2048 + 256 : 0);
#define DLD(a, b) do { const unsigned o_ = g * 8 * 2048 + np * 16; _Pragma("unroll") for (int i = a; i < b; ++i) \
          asm volatile("buffer_load_dwordx4 %0, %1, %2, %3 offen offset:%4 nt" : "=v"(dw[i >> 3][i & 7]) : "v"(o_), "s"(rsD), "s"((unsigned)((i >> 3) * 65536 + (i & 6) * 2048)), "i"((i & 1) * 2048) : "memory"); } while (0)
          WAITV(16); BAR(); STMP(57);
          WAITV(0); BAR(); STMP(58);
          if (DD0) PACE(DD0);
          DLD(0, 8); if (DD1) PACE(DD1); DLD(8, 16); if (DD1) PACE(DD1); DLD(16, 24); if (DD1) PACE(DD1); DLD(24, 32);
          const unsigned ho = down ? WS_H + (dc * 256 + sl * 16 + g * 2) * 16 : WS_OFF;
          u4 hv[8];
#define HLD() do { _Pragma("unroll") for (int i = 0; i < 8; ++i) hv[i] = LD128(ho + (i >> 2) * 2048 + ((i >> 1) & 1) * 128 + (i & 1) * 16); SBAR(); } while (0)
          lds_wait<1>((LAS unsigned*)(lds + L_CNT) + 8, 4);
          const u2 tg = *(const u2*)(lds + L_TAG);
          tlo = RFL(tg[0]); thi = RFL(tg[1]);
          WAITV(0);
#pragma unroll
          for (int i = 0; i < 32; ++i) PIN(dw[i >> 3][i & 7]);
          HLD();
          u4 df[4][8];
#pragma unroll
          for (int i = 0; i < 32; ++i) { TRFRAG(df[i >> 3][i & 7], dw[i >> 3], i & 7); PIN(df[i >> 3][i & 7]); }
          if (down) {
#pragma nounroll
            for (unsigned it = 0; it < SPIN_CAP; ++it) {
              if (all64((GBAD(hv[0]) | GBAD(hv[1]) | GBAD(hv[2]) | GBAD(hv[3]) | GBAD(hv[4]) | GBAD(hv[5]) | GBAD(hv[6]) | GBAD(hv[7])) == 0u)) break;
              __builtin_amdgcn_s_sleep(HSL);
              CLOB();
              HLD();
            }
            __builtin_amdgcn_s_setprio(2);
            f4 acd[4];
#pragma unroll
            for (int c = 0; c < 4; ++c) acd[c] = (f4)(0.f);
#pragma unroll
            for (int fb = 0; fb < 4; ++fb) {
              u4 hl, hh;
#pragma unroll
              for (int e = 0; e < 4; ++e) { const unsigned ha = hv[2 * fb + (e >> 1)][(e & 1) * 2]; hl[e] = np < 8 ? ha : 0u; hh[e] = np < 8 ? 0u : ha; }
#pragma unroll
              for (int c = 0; c < 4; ++c) acd[c] = MFMA16(BC8(hh), BC8(df[fb][c + 4]), MFMA16(BC8(hl), BC8(df[fb][c]), acd[c]));
            }
            u4 yo[4];
#pragma unroll
            for (int e = 0; e < 4; ++e) { yo[e] = tag4((f4){acd[0][e], acd[1][e], acd[2][e], acd[3][e]}, tlo); PIN(yo[e]); }
            if (YW) lds_wait<2>((LAS unsigned*)(lds + L_CNT) + 2, 4);
#pragma unroll
            for (int e = 0; e < 4; ++e) ST128(yo[e], WS_YP + (dc * 8 + 4 * (g & 1) + e) * 4096 + ((kd & 3) * 2 + w - 4) * 512 + (g >> 1) * 256 + np * 16);
            __builtin_amdgcn_s_setprio(0);
            STMP(14);
          }
        }
      }
    } else {
      const int wm = w >> 1, wn = w & 1;
      if (!isQ) {
        constexpr bool TRN = false;
        KLOOP();
        float cc[4]; f4 rsk[8], mk[8]; unsigned tf, cA = ldsb + L_CNT; PIN(cA);
#define TB1(n) do { if ((n) < 4) RD32(cc[(n) & 3], tC, ((n) >> 1) * 256 + ((n) & 1) * 64); else if ((n) & 1) RD128(rsk[(((n) - 4) >> 1) & 3], tR, 1024 + (((n) - 4) >> 1) * 64); else RD128(mk[(((n) - 4) >> 1) & 3], tR, (((n) - 4) >> 1) * 64); } while (0)
#define TB(n) do { if ((n) == 8) RD32(tf, cA, 20); else if ((n) > 8 && (n) < 21) TB1(((n) - 9) % 12); SBAR(); } while (0)
        KLAST();
#undef TB
        PIN(tf);
        if (__builtin_expect(RFL(tf) == 0u, 0)) {
          lds_wait<0>((LAS unsigned*)(lds + L_CNT) + 5, 1);
          TB1(0); TB1(1); TB1(2); TB1(3); TB1(4); TB1(5); TB1(6); TB1(7); TB1(8); TB1(9); TB1(10); TB1(11); WAITL(0);
        }
#pragma unroll
        for (int i = 0; i < 4; ++i) { PIN(mk[i]); PIN(rsk[i]); PIN(cc[i]); }
#pragma unroll
        for (int i = 4; i < 8; ++i) { RD128(mk[i], tR, i * 64); RD128(rsk[i], tR, 1024 + i * 64); }
        SBAR();
        STMP(2); STMC(41);
        f4 kv[2][2], zz[2];
        kv[0][0] = kv[0][1] = kv[1][0] = kv[1][1] = zz[0] = zz[1] = (f4)(0.f);
        const u4 ones = (u4){0x3F803F80u, 0x3F803F80u, 0x3F803F80u, 0x3F803F80u};
        u4 kf[2], vf[2];
#define LN2(i, jj, e0) (__builtin_elementwise_fma(H2(mk[i], e0), (f2)(cc[jj]), H2(c[i][jj], e0)) * H2(rsk[i], e0))
#define KVM(n) do { if ((n) < 4) kv[(n) >> 1][(n) & 1] = MFMA16(BC8(kf[(n) >> 1]), BC8(vf[(n) & 1]), kv[(n) >> 1][(n) & 1]); else zz[(n) & 1] = MFMA16(BC8(kf[(n) & 1]), BC8(ones), zz[(n) & 1]); } while (0)
#pragma unroll
        for (int ip = 0; ip < 4; ++ip) {
          if (ip == 2) { WAITL(0);
#pragma unroll
            for (int i = 4; i < 8; ++i) { PIN(mk[i]); PIN(rsk[i]); } }
          f2 ak[8], av[8], ek[8];
#pragma unroll
          for (int n = 0; n < 8; ++n) { const int i = 2 * ip + ((n >> 1) & 1), e0 = (n & 1) * 2, t = n >> 2; ak[n] = LN2(i, t, e0); av[n] = LN2(i, 2 + t, e0); }
          SBAR();
#pragma unroll
          for (int n = 0; n < 8; ++n) {
            if (ip > 0 && n < 6) KVM(n);
            ek[n] = (f2){__builtin_amdgcn_fmed3f(__builtin_amdgcn_exp2f(ak[n][0]), 0.f, 1.f), __builtin_amdgcn_fmed3f(__builtin_amdgcn_exp2f(ak[n][1]), 0.f, 1.f)};
            ak[n] = (f2){fmaxf(ak[n][0], 1.44269504e-6f), fmaxf(ak[n][1], 1.44269504e-6f)};
            SBAR();
          }
#pragma unroll
          for (int n = 0; n < 8; ++n) { const f2 r = __builtin_elementwise_fma(ak[n], (f2)(0.693147181f), ek[n]); kf[n >> 2][n & 3] = pk2(r[0], r[1]); vf[n >> 2][n & 3] = pk2(av[n][0], av[n][1]); }
          SBAR();
        }
        KVM(0); KVM(1); KVM(2); KVM(3); KVM(4); KVM(5);
        LANE();
        const unsigned kb = WS_KVP + ((hp * 2 + wn) * 8 + rg) * KVS;
        const unsigned exa = ldsb + L_EX + l * 16 + (w ^ 2) * 4096, pka = kb + wm * 1024 + l * 16, pza = wm == 0 && np < 2 ? kb + 2048 + np * 64 + g * 16 : WS_OFF;
        STMP(19);
        const f4 zs = (np & 1) ? zz[1] : zz[0];
        *(f4*)(lds + L_EX + l * 16 + w * 4096) = wm ? kv[0][0] : kv[0][1]; *(f4*)(lds + L_EX + l * 16 + w * 4096 + 1024) = wm ? kv[1][0] : kv[1][1];
        *(f4*)(lds + L_EX + l * 16 + w * 4096 + 2048) = zs;
        lds_arrive((LAS unsigned*)(lds + L_CNT) + 6 + wn);
        f4 e0, e1, e2;
        cA += wn * 4;
#pragma nounroll
        do {
          RD32(tf, cA, 24); RD128(e0, exa, 0); RD128(e1, exa, 1024); RD128(e2, exa, 2048);
          WAITL(0); PIN(tf); PIN(e0); PIN(e1); PIN(e2);
        } while (RFL(tf) < 2u);
        const f4 o0 = ((wm ? kv[0][1] : kv[0][0]) + e0) * 0.693147181f, o1 = ((wm ? kv[1][1] : kv[1][0]) + e1) * 0.693147181f;
        ST128(san4((u4){pk2(o0[0], o0[1]), pk2(o0[2], o0[3]), pk2(o1[0], o1[1]), pk2(o1[2], o1[3])}), pka);
        ST128(san4(__builtin_bit_cast(u4, zs + e2)), pza);
        SBAR();
        STMP(3);
        TPOLL(PDK);
        if (j == 1 && w == 0) { const u2 t_ = *(const u2*)(lds + L_TAG); ST128(((u4){t_[0], t_[1], tpoll, 0u}), l < 8 ? WS_HU + xcd * 128 + l * 16 : WS_OFF); }
        lds_arrive((LAS unsigned*)(lds + L_CNT) + 8);
        lds_wait<1>((LAS unsigned*)(lds + L_CNT) + 8, 4);
        PACE(2);
        XOWN(); SBAR();
        { WO_SETUP(); (void)vX; (void)rsX; WOR(w, 128, 4); }
        WAITV(0);
        lds_arrive((LAS unsigned*)(lds + L_CNT) + 2);
        OINIT();
        lds_wait<1>((LAS unsigned*)(lds + L_CNT) + 3, 1);
        lds_wait<1>((LAS unsigned*)(lds + L_CNT) + 2, 4);
        CPL(1);
        STMP(20);
      } else {
        constexpr bool TRN = true;
        KLOOP();
        f4 cc[4]; float rs[8], mn[8];
#define TB(n) do { if ((n) < 4) RD128(cc[(n) & 3], tC, ((n) >> 1) * 256 + ((n) & 1) * 64); else if ((n) < 20) { if ((n) & 1) RD32(rs[(((n) - 4) >> 1) & 7], tR, 1024 + (((n) - 4) >> 1) * 64); else RD32(mn[(((n) - 4) >> 1) & 7], tR, (((n) - 4) >> 1) * 64); } SBAR(); } while (0)
        KLAST();
#undef TB
#pragma unroll
        for (int i = 0; i < 8; ++i) { PIN(mn[i]); PIN(rs[i]); PIN(cc[i & 3]); }
        STMP(2); STMC(41);
        LANE();
        u4 pf[8][2];
#pragma unroll
        for (int i = 0; i < 8; ++i)
#pragma unroll
          for (int hh = 0; hh < 2; ++hh) {
#pragma unroll
            for (int e = 0; e < 4; ++e) { const int jj = 2 * hh + (e >> 1), e0 = (e & 1) * 2;
              pf[i][hh][e] = phi2(__builtin_elementwise_fma(H2(cc[jj], e0), (f2)(mn[i]), H2(c[i][jj], e0)) * rs[i]); }
            PIN(pf[i][hh]);
          }
        const u2 tg = *(const u2*)(lds + L_TAG);
        const unsigned ofl = l < 8 ? WS_OFL + ((rg * 2 + wm) * 32 + (qg * 2 + wn) * 2) * 128 + sl * 16 : WS_OFF;
        unsigned oa[8];
#pragma unroll
        for (int i = 0; i < 8; ++i) { oa[i] = WS_OF + ((rg * 16 + wm * 8 + i) * 32 + qg * 4 + wn) * 1024 + l * 16; PIN(oa[i]); }
        STMP(19);
        BAR();
        STMP(22);
        u4 kA[2][2], zA[2];
#define KRD(hh) do { zA[hh] = *(const u4*)(lds + L_ZX + (2 * (hh) + wn) * 64 + g * 16); \
          kA[hh][0] = *(const u4*)(lds + L_KX + (2 * (hh) + wn) * 2048 + l * 16); kA[hh][1] = *(const u4*)(lds + L_KX + (2 * (hh) + wn) * 2048 + 1024 + l * 16); SBAR(); } while (0)
        KRD(0);
        if (FI < 0) { ST128(((u4){tg[0], tg[1], tg[0], tg[1]}), ofl); SBAR(); STMP(24); }
        f4 dn[16], x0[16], x1[16];
#define OM(n) do { dn[n] = MFMA16(BC8(zA[(n) >> 3]), BC8(pf[(n) & 7][(n) >> 3]), (f4)(1e-6f)); x0[n] = MFMA16(BC8(kA[(n) >> 3][0]), BC8(pf[(n) & 7][(n) >> 3]), (f4)(0.f)); \
          x1[n] = MFMA16(BC8(kA[(n) >> 3][1]), BC8(pf[(n) & 7][(n) >> 3]), (f4)(0.f)); } while (0)
        OM(0); OM(1);
#pragma unroll
        for (int n = 0; n < 16; ++n) {
          SBAR();
          const float inv = __builtin_amdgcn_rcpf(dn[n][0]);
          asm volatile("" :: "v"(dn[n]));
          if (n + 2 < 16 && n != 6 && n != 7) OM(n + 2);
          float y[8];
#pragma unroll
          for (int e = 0; e < 4; ++e) { y[e] = x0[n][e] * inv; y[4 + e] = x1[n][e] * inv; PIN(y[e]); PIN(y[4 + e]); }
          const u4 ov = (u4){pk2(y[0], y[1]), pk2(y[2], y[3]), pk2(y[4], y[5]), pk2(y[6], y[7])};
          ST128(PO && n < 8 ? san4(ov) : ov, oa[n & 7] + (n >> 3) * 2048);
          if (n == FI) { SBAR(); if (!PO) WAITV(FI > 7 ? FI - 7 : 0); ST128(((u4){tg[0], tg[1], tg[0], tg[1]}), ofl); SBAR(); STMP(24); }
          if (n == 7) { SBAR(); BAR(); KRD(1); OM(8); OM(9); }
        }
        SBAR();
        STMP(23);
        WAITV(0);
        ST128(((u4){tg[0], tg[1], tg[0], tg[1]}), ofl + 128);
        SBAR();
        CPL(2);
        STMP(5);
      }
    }
  } else {
    LANE();
    const int jm = j - 24, jsl = (3 - (int)(((size_t)wg >> 7) & 7) - xcd) & 7;
    const bool master = jm == 7 && jsl == 7, help = jm == 7 && jsl != 7, half = jm == 7 || jm == jsl;
    const int jc = help ? jsl : jm, hc = jc * 8 + ((xcd + jc) & 7);
    const int row0 = (xcd & 1) * 1024 + (xq * 8 + jm) * 32 + w * 4;
    u4 cw[12], xr[8], wv[2];
    const u16* cp = wqkv + (size_t)(jm * 32 + w * 4 + g) * 3072 + xcd * 128 + np * 8;
#define CI(n) (((n) % 3) * 4 + (n) / 3)
#define CL(a, b) do { _Pragma("unroll") for (int n = a; n < b; ++n) cw[CI(n)] = *(const u4*)(cp + ((CI(n) & 3) * 768 + ((CI(n) >> 2) + 1) % 3) * 1024); SBAR(); } while (0)
    if (w == 0 && l < 8) ((unsigned*)(lds + L_CNT))[l] = 0u;
    CL(0, MORD);
    LBAR();
#pragma unroll
    for (int i = 0; i < 8; ++i) { const int n = (i & 3) * 2 + (i >> 2); xr[n] = *(const u4*)(x + row0 * D + n * 512 + l * 8); if (i == 3) SBAR(); }
    wv[0] = *(const u4*)(wr + l * 8); wv[1] = *(const u4*)(wr + 512 + l * 8);
    u2 ep = LD64(WS_EP);
    SBAR();
    CL(MORD, CL0);
    PIN(ep);
    const u64 tag = (((u64)RFL(ep[1]) << 32) | RFL(ep[0])) + TAG_STEP;
    const unsigned tlo = (unsigned)tag, thi = (unsigned)(tag >> 32);
    const u2 tgv = (u2){tlo, thi};
    {
      float a[13];
#pragma unroll
      for (int i = 0; i < 13; ++i) a[i] = 0.f;
      unsigned one2 = 0x3F803F80u; PIN(one2);
#pragma unroll
      for (int jj = 0; jj < 8; ++jj) {
        const unsigned wv_ = wv[jj >> 2][jj & 3];
#pragma unroll
        for (int i = 0; i < 4; ++i) {
          const unsigned xv = xr[2 * i + (jj >> 2)][jj & 3];
          a[3 * i] = DOT2(xv, one2, a[3 * i]); a[3 * i + 1] = DOT2(xv, xv, a[3 * i + 1]); a[3 * i + 2] = DOT2(xv, wv_, a[3 * i + 2]);
        }
        a[12] = DOT2(wv_, one2, a[12]);
      }
#define RSTEP(n, ctrl) _Pragma("unroll") for (int i = 0; i < n; ++i) a[i] += DPPF(a[i], ctrl, 0xf)
      RSTEP(13, 0xB1); RSTEP(13, 0x4E);
#pragma unroll
      for (int i = 0; i < 3; ++i) { const float u0 = (l & 1) ? a[3 + i] : a[i], u1 = (l & 1) ? a[9 + i] : a[6 + i]; a[i] = (l & 2) ? u1 : u0; }
      a[3] = a[12];
      RSTEP(4, 0x124); RSTEP(4, 0x128);
#pragma unroll
      for (int i = 0; i < 4; ++i) {
        auto s16 = __builtin_amdgcn_permlane16_swap(F2U(a[i]), F2U(a[i]), false, false);
        a[i] = U2F(s16[0]) + U2F(s16[1]);
        auto s32 = __builtin_amdgcn_permlane32_swap(F2U(a[i]), F2U(a[i]), false, false);
        a[i] = U2F(s32[0]) + U2F(s32[1]);
      }
      const float mf = a[0] * (1.f / D), m = rbf(mf);
      const float var = rbf(fmaxf(a[1] * (1.f / D) - mf * mf, 0.f));
      const float rs = __builtin_amdgcn_rcpf(rbf(__builtin_amdgcn_sqrtf(rbf(var + 1e-5f))));
      const float dsc = rs * (a[2] - m * a[3]);
      ST128(((u4){pk2(dsc, m), tlo, F2U(rs), thi}), l < 4 ? WS_MS + (row0 + l) * 16 : WS_OFF);
    }
    SBAR();
    STMP(18);
    CL(CL0, 12);
    u4 gw[4][8], gf[4][8];
    const u16* wp = ((np >> 3) ? wu : wg) + (size_t)(128 * w + (help ? 64 : 0) + 8 * g) * F + 64 * hc + 8 * sl;
#define GL(b, on) do { const u16* p_ = (on) ? wp + (size_t)(32 * (b)) * F : wr + l * 8; const unsigned st_ = (on) ? F : 0; \
      _Pragma("unroll") for (int i_ = 0; i_ < 8; ++i_) gw[b][i_] = NTL((const u4*)(p_ + (size_t)(i_ * st_))); SBAR(); } while (0)
#define GT(b) do { _Pragma("unroll") for (int c_ = 0; c_ < 8; ++c_) { TRFRAG(gf[b][c_], gw[b], c_); PIN(gf[b][c_]); } SBAR(); } while (0)
    GL(0, !master);
    char* scr = lds + L_WO + (((lq ^ (hx >> 3)) & 2) ? 0 : 65536);
    float* red = (float*)scr;
    {
      u4 sel8;
      const unsigned one = (np & 1) ? 0x3F800000u : 0x00003F80u;
#pragma unroll
      for (int e = 0; e < 4; ++e) sel8[e] = ((np >> 1) == e) ? one : 0u;
#pragma unroll
      for (int p = 0; p < 3; ++p) {
        f4 c = (f4)(0.f);
#pragma unroll
        for (int i = 0; i < 4; ++i) c = MFMA16(BC8(sel8), BC8(cw[p * 4 + i]), c);
        if (g < 2) *(f4*)(red + (w * 3 + p) * 128 + 8 * np + 4 * g) = c;
      }
    }
    lds_arrive((LAS unsigned*)(lds + L_CNT) + 2);
    if (w < 3) {
      lds_wait<1>((LAS unsigned*)(lds + L_CNT) + 2, 8);
      if (w == 0) ST64(tgv, l == 0 ? WS_FLAG + gb * 8 : WS_OFF);
      const float* rp = red + w * 128 + g * 32 + np;
      float c0 = rp[0], c1 = rp[16];
#pragma unroll
      for (int i = 1; i < 8; ++i) { c0 += rp[i * 384]; c1 += rp[i * 384 + 16]; }
      ST128(((u4){F2U(c0), tlo, F2U(c1), thi}), WS_CS + ((jm * 96 + (w + 1) % 3 * 32 + xcd * 4) * 16 + l) * 16);
    }
    SBAR();
    STMP(17);
    if (master) {
      u4 rv[4];
      const unsigned ra = WS_MS + (w * 256 + l) * 16;
      unsigned bad = 15;
#pragma nounroll
      for (unsigned it = 0; it < SPIN_CAP; ++it) {
#pragma unroll
        for (int i = 0; i < 4; ++i) {
          const u4 v = LD128((bad >> i) & 1u ? ra + i * 1024 : WS_OFF);
          rv[i] = (bad >> i) & 1u ? v : rv[i];
        }
        bad = 0;
#pragma unroll
        for (int i = 0; i < 4; ++i) bad |= GBAD(rv[i]) ? 1u << i : 0u;
        if (all64(bad == 0u)) break;
        __builtin_amdgcn_s_sleep(4);
        CLOB();
      }
      unsigned key[4];
#pragma unroll
      for (int i = 0; i < 4; ++i) {
        const unsigned b = rv[i][0] << 16;
        key[i] = ((b ^ ((unsigned)((int)b >> 31) | 0x80000000u)) & 0xFFFF0000u) | (unsigned)(0xFFFF - (w * 256 + i * 64 + l));
      }
#define CE(i, j) do { const unsigned hi_ = max(key[i], key[j]), lo_ = min(key[i], key[j]); key[i] = hi_; key[j] = lo_; } while (0)
      CE(0, 1); CE(2, 3); CE(0, 2); CE(1, 3); CE(1, 2);
      unsigned* cand = (unsigned*)(lds + L_ROWM);
      LAS unsigned* cnt = (LAS unsigned*)(lds + L_CNT) + 1;
      unsigned cv = 0;
#pragma nounroll
      for (int pass = 0; pass < 2; ++pass) {
#pragma nounroll
        for (int it = 0; it < 8; ++it) {
          const unsigned m = wave_max(key[0]);
          const bool win = key[0] == m;
          key[0] = win ? key[1] : key[0]; key[1] = win ? key[2] : key[1]; key[2] = win ? key[3] : key[2]; key[3] = win ? 0u : key[3];
          cv = sl == it ? m : cv;
        }
        if (pass) break;
        if (l < 8) cand[w * 8 + l] = cv;
        lds_arrive(cnt);
        if (w) break;
        lds_wait<0>(cnt, 8);
        key[0] = cand[l]; key[1] = 0u;
      }
      if (w == 0) { ST64(((u2){cv, tlo}), WS_MSG + rr * 256 + 64 + sl * 8); STMP(29); }
    }
    if (master && w == 4) {
      PACE(MD2);
      bool ok = false;
#pragma nounroll
      for (unsigned it = 0; it < SPIN_CAP; ++it) {
        if (!ok) {
          u4 f0 = LD128(WS_FLAG + l * 32), f1 = LD128(WS_FLAG + l * 32 + 16);
          ok = f0[0] == tlo && f0[1] == thi && f0[2] == tlo && f0[3] == thi && f1[0] == tlo && f1[1] == thi && f1[2] == tlo && f1[3] == thi;
        }
        if (all64(ok)) break;
        __builtin_amdgcn_s_sleep(8);
        CLOB();
      }
      CLOB();
      if (l < 8) ST64(tgv, WS_MSG + l * 256);
      if (l == 0) ST64(tgv, WS_EP);
      STMP(30);
    }
    GL(1, !master);
    { WO_SETUP(); (void)vX; WOR(w, 32, 8); }
    GT(0);
    u2 f = LD64(msg);
    SBAR();
    GL(2, !half);
    GT(1);
#pragma nounroll
    for (unsigned it = 0; it < SPIN_CAP; ++it) {
      if (KEYOK(f)) break;
      __builtin_amdgcn_s_sleep(16);
      CLOB();
      f = LD64(msg);
    }
    CLOB();
    STMP(9);
    if (w == 0) {
      if (l < 8) ((unsigned*)(lds + L_TAG + 16))[l] = f[0];
      if (l == 8) *(u2*)(lds + L_TAG) = tgv;
    }
    if (!master) {
      const unsigned key_ = (unsigned)__builtin_amdgcn_update_dpp((int)f[0], (int)f[0], 0x128, 0xf, 0xc, false);
      const int idx = (0xFFFF - (int)(key_ & 0xFFFF)) & 2047;
      const u16* xp = x + idx * D + 128 * w + (help ? 64 : 0) + 32 * (np >> 3) + 8 * g;
      const u4 r0 = *(const u4*)xp, r1 = *(const u4*)(xp + (half ? 0 : 64));
      const u4 msx = LD128(WS_MS + idx * 16);
      SBAR();
      GL(3, !half);
      GT(2); GT(3);
      f4 acc[4];
#pragma unroll
      for (int c2 = 0; c2 < 4; ++c2) acc[c2] = (f4)(0.f);
      const float m = hi(msx[0]), rs = U2F(msx[2]);
#pragma unroll
      for (int b2 = 0; b2 < 2; ++b2) {
        if (b2 && half) break;
        u4 xl[2], xh[2];
#pragma unroll
        for (int e = 0; e < 4; ++e) {
          const unsigned r = b2 ? r1[e] : r0[e];
          const unsigned v = pk2((lo(r) - m) * rs, (hi(r) - m) * rs);
          const unsigned o = (unsigned)__builtin_amdgcn_update_dpp(0, (int)v, 0x128, 0xf, 0xf, false);
          xl[0][e] = np < 8 ? v : 0u; xh[0][e] = np < 8 ? 0u : o; xl[1][e] = np < 8 ? o : 0u; xh[1][e] = np < 8 ? 0u : v;
        }
#pragma unroll
        for (int it = 0; it < 2; ++it)
#pragma unroll
          for (int c = 0; c < 8; ++c) {
            acc[c >> 1] = MFMA16(BC8((c & 1) ? xh[it] : xl[it]), BC8(gf[2 * b2 + it][c]), acc[c >> 1]);
          }
        SBAR();
      }
      STMP(12);
      red += 3072;
#pragma unroll
      for (int c2 = 0; c2 < 4; ++c2) *(f4*)(red + (w * 4 + c2) * 256 + l * 4) = acc[c2];
      LBAR();
      f2 sv = *(const f2*)(red + (w >> 1) * 256 + l * 4 + (w & 1) * 2);
#pragma unroll
      for (int i = 1; i < 8; ++i) sv += *(const f2*)(red + (i * 4 + (w >> 1)) * 256 + l * 4 + (w & 1) * 2);
      const unsigned gp = WS_GP + (xcd * 512 + w * 64 + l) * 16;
      if (help) ST128(((u4){F2U(sv[0]), tlo, F2U(sv[1]), thi}), gp);
      else {
      if (half) {
        u4 pv;
#pragma nounroll
        for (unsigned it = 0; it < SPIN_CAP; ++it) {
          pv = LD128(gp);
          if (all64(GBAD(pv) == 0u)) break;
          __builtin_amdgcn_s_sleep(8);
          CLOB();
        }
        sv += (f2){U2F(pv[0]), U2F(pv[2])};
      }
      u16* hb = (u16*)(scr + 45056);
#pragma unroll
      for (int e = 0; e < 2; ++e) {
        const float gg = rbf(sv[e]), u1 = DPPF(gg, 0x128, 0xf);
        const float hv = rbf(gg * __builtin_amdgcn_rcpf(1.f + __builtin_amdgcn_exp2f(gg * -1.44269504f))) * u1;
        if (np < 8) hb[(4 * (g & 1) + 2 * (w & 1) + e) * 64 + 8 * np + 2 * (w >> 1) + (g >> 1)] = (u16)(pk2(hv, hv) & 0xFFFFu);
      }
      LBAR();
      if (w < 2) { const u2 hv = *(const u2*)(hb + (w * 64 + l) * 4); ST128(((u4){hv[0], tlo, hv[1], thi}), WS_H + (hc * 128 + w * 64 + l) * 16); }
      }
      STMP(14);
    }
    LBAR();
    if (w < 4) { LANE(); XOWN(); WAITV(0); OINIT(); }
    else { LANE(); WO_SETUP(); (void)vX; WOR(28 + w, 128, 4); WAITV(0); }
    STMP(21);
    if (w == 7) {
#pragma nounroll
      for (unsigned it = 0; it < SPIN_CAP; ++it) {
        const u4 v = LD128(WS_HU + xcd * 128);
        if (v[0] == tlo && v[1] == thi) { if (l == 0) *(unsigned*)(lds + L_TAG + 8) = v[2]; break; }
        __builtin_amdgcn_s_sleep(8);
        CLOB();
      }
#pragma nounroll
      for (unsigned it = 0; PO && it < SPIN_CAP && !MSGOK(f); ++it) { f = LD64(msg); CLOB(); }
    }
    LBAR();
    tpoll = RFL(*(const unsigned*)(lds + L_TAG + 8));
    { const unsigned now = (unsigned)__builtin_amdgcn_s_memrealtime(); if (tpoll - now > PDK) tpoll = now; }
    CPL(4);
    STMP(20);
  }

  LANE();
  LAS unsigned* cnt = (LAS unsigned*)(lds + L_CNT);
  if (ow >= 4) {
#define YD 16
    lds_wait<4>(cnt + 4, 1);
    PACE(YD);
    const unsigned mykey = ((const unsigned*)(lds + L_TAG + 16))[sl], tlo = RFL(*(const unsigned*)(lds + L_TAG));
#pragma unroll 1
    for (int i = ow - 4; i < 8; i += 4) {
      const unsigned kk = (unsigned)__builtin_amdgcn_readlane((int)mykey, i);
      const int ti = 0xFFFF - (int)(kk & 0xFFFF);
      if ((ti >> 7) != mb2) continue;
      const unsigned yb = WS_YP + (g * 7 * 8 + i) * 4096 + (nb >> 1) * 512 + (np >> 3) * 256 + ((nb & 1) * 8 + sl) * 16;
      f4 y;
#pragma nounroll
      for (unsigned it = 0; it < SPIN_CAP; ++it) {
        u4 yv[7];
#pragma unroll
        for (int k = 0; k < 7; ++k) yv[k] = LD128(yb + k * 32768);
        SBAR();
        unsigned bad = 0;
        y = untag4(yv[0], tlo, bad);
#pragma unroll
        for (int k = 1; k < 7; ++k) { const f4 t = untag4(yv[k], tlo, bad); y = (f4){add1(y[0], t[0]), add1(y[1], t[1]), add1(y[2], t[2]), add1(y[3], t[3])}; }
        if (tagok(bad)) break;
        __builtin_amdgcn_s_sleep(32);
        CLOB();
      }
#pragma unroll
      for (int e = 0; e < 4; ++e) {
        auto a16 = __builtin_amdgcn_permlane16_swap(F2U(y[e]), F2U(y[e]), false, false);
        y[e] = add1(U2F(a16[0]), U2F(a16[1]));
        auto a32 = __builtin_amdgcn_permlane32_swap(F2U(y[e]), F2U(y[e]), false, false);
        y[e] = add1(U2F(a32[0]), U2F(a32[1]));
      }
      const unsigned mono = kk >> 16;
      const float rv = U2F(((mono & 0x8000u) ? (mono & 0x7FFFu) : (~mono & 0xFFFFu)) << 16);
      const float sg = rbf(__builtin_amdgcn_rcpf(1.f + __builtin_amdgcn_exp2f(rv * -1.44269504f)));
#pragma unroll
      for (int e = 0; e < 4; ++e) y[e] = rbf(mul1(rbf(y[e]), sg));
      if (g == 0) *(f4*)(lds + L_YS + i * 256 + sl * 32 + (np >> 3) * 16) = y;
    }
    lds_arrive(cnt);
    CPL(5);
    STMP(32);
    STDUMP();
    return;
  }
  const u2 tg = *(const u2*)(lds + L_TAG);
  char* xt = lds + L_XT + (ow * 32 + np) * 128 + (g & 1) * 8;
  const int trow = 0xFFFF - (int)(((const unsigned*)(lds + L_TAG + 16))[sl] & 0xFFFFu);
  const unsigned hit = (unsigned)__builtin_amdgcn_ballot_w64((trow >> 5) == mb2 * 4 + ow) & 0xFFu;
  unsigned woad[2][4];
  { const int q = np >> 2, p = l & 3, row = 4 * g + q;
#pragma unroll
    for (int ct = 0; ct < 4; ++ct) { woad[0][ct] = ldsb + L_WO + row * 128 + ((ct ^ ((row >> 1) & 3)) * 32) + p * 8; woad[1][ct] = woad[0][ct] + 65536; } }
  s4 b[2][16];
#define K3RD(hc, i) RDTR(b[(hc) & 1][i], woad[((hc) >> 2) & 1][((i) >> 1) & 3], ((((hc) & 3) * 4 + ((hc) >> 3) * 2 + ((i) >> 3)) * 4096 + ((i) & 1) * 2048))
  K3RD(0, 0); K3RD(0, 1); K3RD(0, 2); K3RD(0, 3); K3RD(0, 4); K3RD(0, 5); K3RD(0, 6); K3RD(0, 7);
  K3RD(0, 8); K3RD(0, 9); K3RD(0, 10); K3RD(0, 11); K3RD(0, 12); K3RD(0, 13); K3RD(0, 14); K3RD(0, 15);
  const unsigned flg = l < 32 ? WS_OFL + (mb2 * 32 + (np ^ (hx >> 1)) * 2 + g) * 128 : WS_OFF;
#pragma nounroll
  while ((int)((unsigned)__builtin_amdgcn_s_memrealtime() - tpoll) < 0) __builtin_amdgcn_s_sleep(1);
  STMP(26);
  unsigned rdy = 0;
#define OPOLL(MASK, SL) _Pragma("nounroll") for (unsigned it = 0; it < SPIN_CAP && (rdy & (MASK)) != (MASK); ++it) { \
    u2 f_ = LD64((((PM ? (MASK) & ~rdy : ~rdy) >> (l & 31)) & 1u) ? flg : WS_OFF); \
    rdy |= (unsigned)__builtin_amdgcn_ballot_w64(f_[0] == tg[0] && f_[1] == tg[1]); \
    if ((SL) && rdy == 0) __builtin_amdgcn_s_sleep(SL); \
    CLOB(); }
#define OG 16
  OPOLL((1u << (OG / 2)) - 1, OSL);
  CLOB();
  STMP(6);
#define OPF 3
#define OW0 6
#define OW 12
  u4 a[32]; u2 fl;
  const unsigned abase = WS_OF + (mb2 * 8 + ow * 2) * 32768 + l * 16, abase1 = abase + 32768;
#define OL1(f) a[(f) & 31] = __builtin_amdgcn_raw_buffer_load_b128(wrs, (((f) & 1) ? abase1 : abase) + (((f) >> 5) * 2 + (((f) >> 1) & 1)) * 1024, ((((f) >> 2) & 7) * 4096) ^ (hx * 1024), 16)
  unsigned mxo;
#define OMX(hc, k) mxo = mx3((k) ? mxo : 0u, a[(hc) * 4 + ((k) >> 1)][((k) & 1) * 2], a[(hc) * 4 + ((k) >> 1)][((k) & 1) * 2 + 1])
#define OFIX(hc) do { if (__builtin_expect(!clean(mxo), 0)) { NFIX(); _Pragma("nounroll") for (unsigned it = 0; it < SPIN_CAP; ++it) { CLOB(); \
      OL1((hc) * 4); OL1((hc) * 4 + 1); OL1((hc) * 4 + 2); OL1((hc) * 4 + 3); \
      if (clean(mx4(mx4(mx4(mx4(0u, a[(hc) * 4]), a[(hc) * 4 + 1]), a[(hc) * 4 + 2]), a[(hc) * 4 + 3]))) break; } } } while (0)
#define NFIX()
#define OMK(f) ((unsigned)((1ull << (((f) + OG) / 2)) - 1))
#define OLD(f) do { if ((f) < 64) { if ((f) && !((f) % OG) && (rdy & OMK(f)) != OMK(f)) { rdy |= (unsigned)__builtin_amdgcn_ballot_w64(fl[0] == tg[0] && fl[1] == tg[1]); OPOLL(OMK(f), 0); CLOB(); } \
    OL1(f); \
    if (((f) % OG) == OPF && (f) < 64 - OG) fl = LD64((((PM == 1 ? OMK((f) - OPF + OG) & ~rdy : ~rdy) >> (l & 31)) & 1u) ? flg : WS_OFF); } } while (0)
#pragma unroll
  for (int f = 0; f < OW0; ++f) { OLD(f); SBAR(); }
  if (ow == 0) { WAITL(0); lds_arrive(cnt + 4); WAITL(0); }
#define OHI(c) ((OW0 + 2 * (c)) < (OW + (c)) ? (OW0 + 2 * (c)) : (OW + (c)))
#define K3R(i) (((i) ^ ((i) >> 1)) & 1)
#define K3C(hc, i) ((hc) * 4 + ((i) >> 2))
#define K3M(hc, i) do { if (!((i) & 1)) WAITL((hc) < 15 ? 14 : 14 - (i)); SBAR(); \
    oc[K3R(i)][((i) >> 1) & 3] = MFMA16(CAT(b[(hc) & 1][(i) & ~1], b[(hc) & 1][(i) | 1]), BC8(a[((hc) * 4 + ((i) >> 3) * 2 + K3R(i)) & 31]), oc[K3R(i)][((i) >> 1) & 3]); SBAR(); \
    if ((hc) < 15) { K3RD((hc) + 1, i); } \
    if (PO && (hc) < 7 && (i) >= 6 && (i) < 14) { OMX(((hc) + 1) & 7, ((i) - 6) & 7); PIN(mxo); } \
    if (PO && (hc) < 7 && (i) == 15) { SBAR(); OFIX(((hc) + 1) & 7); } \
    if (((i) & 3) == 3) { OLD(OHI(K3C(hc, i) + 1) - 1); } else if (((i) & 3) == 1 && OHI(K3C(hc, i) + 1) - OHI(K3C(hc, i)) == 2) { OLD(OHI(K3C(hc, i))); } \
    SBAR(); } while (0)
#define K3H(hc) do { K3M(hc, 0); K3M(hc, 1); K3M(hc, 2); K3M(hc, 3); K3M(hc, 4); K3M(hc, 5); K3M(hc, 6); K3M(hc, 7); \
    K3M(hc, 8); K3M(hc, 9); K3M(hc, 10); K3M(hc, 11); K3M(hc, 12); K3M(hc, 13); K3M(hc, 14); K3M(hc, 15); } while (0)
  if (PO) { OMX(0, 0); OMX(0, 1); OMX(0, 2); OMX(0, 3); OMX(0, 4); OMX(0, 5); OMX(0, 6); OMX(0, 7); OFIX(0); }
  STMP(28);
  K3H(0); STMP(27); K3H(1); K3H(2); K3H(3); K3H(4); K3H(5); K3H(6); K3H(7); STMP(31); STMC(42); K3H(8); K3H(9); K3H(10); K3H(11); K3H(12); K3H(13); K3H(14); K3H(15);
  STMP(7); STMC(43);
  if (hit) {
    lds_wait<0>(cnt, 4);
#pragma unroll 1
    for (int i = 0; i < 8; ++i)
      if ((hit >> i) & 1u) {
        const int row = __builtin_amdgcn_readlane(trow, i) & 31;
#pragma unroll
        for (int r = 0; r < 2; ++r)
          if (row == r * 16 + np) {
#pragma unroll
            for (int ct = 0; ct < 4; ++ct) oc[r][ct] += *(const f4*)(lds + L_YS + i * 256 + ct * 64 + g * 16);
          }
      }
  }
#pragma unroll
  for (int r = 0; r < 2; ++r)
#pragma unroll
    for (int ct = 0; ct < 4; ++ct) *(u2*)XT(r, ct) = (u2){pk2(oc[r][ct][0], oc[r][ct][1]), pk2(oc[r][ct][2], oc[r][ct][3])};
  {
    const __amdgpu_buffer_rsrc_t ors = __builtin_amdgcn_make_buffer_rsrc(out, 0, S * D * 2, 0x00020000);
#pragma unroll
    for (int i = 0; i < 4; ++i) {
      const u4 v = *(const u4*)(lds + L_XT + (ow * 32 + 8 * i + rr) * 128 + ((sl ^ rr) * 16));
      __builtin_amdgcn_raw_buffer_store_b128(v, ors, (mb2 * 128 + ow * 32 + 8 * i + rr) * 2048 + nb * 128 + sl * 16, 0, 16);
    }
  }
  CPL(6);
  STMP(16);
  STDUMP();
}

extern "C" void kernel_launch(void* const* d_in, const int* in_sizes, int n_in, void* d_out, int out_size,
                              void* d_ws, size_t ws_size, hipStream_t stream) {
  const u16* x = (const u16*)d_in[0];
  const u16* wqkv = (const u16*)d_in[1];
  const u16* wo = (const u16*)d_in[2];
  const u16* wr = (const u16*)d_in[4];
  const u16* wg = (const u16*)d_in[5];
  const u16* wu = (const u16*)d_in[6];
  const u16* wd = (const u16*)d_in[7];
  (void)hipFuncSetAttribute((const void*)fused, hipFuncAttributeMaxDynamicSharedMemorySize, K_LDS);
  fused<<<256, 512, K_LDS, stream>>>(x, wqkv, wo, wr, wg, wu, wd, (char*)d_ws, (u16*)d_out);
}
```

```cpp
#include <hip/hip_runtime.h>
#include <hip/hip_bf16.h>
#include <stdint.h>

typedef unsigned short u16;
typedef unsigned long long u64;
typedef short s4 __attribute__((ext_vector_type(4)));
typedef short s8 __attribute__((ext_vector_type(8)));
typedef float f4 __attribute__((ext_vector_type(4)));
typedef float f2 __attribute__((ext_vector_type(2)));
typedef unsigned u4 __attribute__((ext_vector_type(4)));
typedef unsigned u2 __attribute__((ext_vector_type(2)));
typedef __bf16 bf2 __attribute__((ext_vector_type(2)));
#define LAS __attribute__((address_space(3)))
#define DEV __device__ __forceinline__

DEV float U2F(unsigned u) { return __builtin_bit_cast(float, u); }
DEV unsigned F2U(float f) { return __builtin_bit_cast(unsigned, f); }
DEV float I2F(int u) { return __builtin_bit_cast(float, u); }
DEV int F2I(float f) { return __builtin_bit_cast(int, f); }

constexpr int S = 2048, D = 1024, F = 3584;

constexpr unsigned WS_EP = 0;
constexpr unsigned WS_FLAG = 4096;
constexpr unsigned WS_MSG = 6144;
constexpr unsigned WS_HU = 8192;
constexpr unsigned WS_MS = 32768;
constexpr unsigned WS_CS = 256u << 10;
constexpr unsigned WS_GP = 512u << 10;
constexpr unsigned WS_YP = 1280u << 10;
constexpr unsigned WS_KVP = 3328u << 10;
constexpr unsigned WS_OF = 6u << 20;
constexpr unsigned WS_OFL = 10u << 20;
constexpr unsigned WS_END = (10u << 20) + (128u << 10);
constexpr unsigned WS_OFF = 0x7FFF0000u;
constexpr unsigned KVS = 2304;
constexpr unsigned POISON = 0xFFFFFFFFu;

DEV unsigned pk2(float a, float b) { bf2 v; v[0] = (__bf16)a; v[1] = (__bf16)b; return __builtin_bit_cast(unsigned, v); }
DEV float lo(unsigned u) { return U2F(u << 16); }
DEV float hi(unsigned u) { return U2F(u & 0xFFFF0000u); }
DEV float rbf(float a) { return lo(pk2(a, a)); }
DEV float add1(float a, float b) { float r; asm("v_add_f32 %0, %1, %2" : "=v"(r) : "v"(a), "v"(b)); return r; }
DEV float mul1(float a, float b) { float r; asm("v_mul_f32 %0, %1, %2" : "=v"(r) : "v"(a), "v"(b)); return r; }
DEV unsigned san(unsigned u) { return min(u, 0xFFFFFFFEu); }
DEV u4 san4(u4 v) { v[0] = san(v[0]); v[1] = san(v[1]); v[2] = san(v[2]); v[3] = san(v[3]); return v; }
DEV unsigned mx3(unsigned a, unsigned b, unsigned c) { return max(max(a, b), c); }
DEV unsigned mx4(unsigned m, u4 v) { return mx3(mx3(m, v[0], v[1]), v[2], v[3]); }
DEV u4 tag4(f4 v, unsigned t) { u4 r; _Pragma("unroll") for (int e = 0; e < 4; ++e) r[e] = (F2U(v[e]) & 0xFFFFFF00u) | ((t >> (8 * e)) & 0xFFu); return r; }
DEV f4 untag4(u4 v, unsigned t, unsigned& bad) {
  f4 r; _Pragma("unroll") for (int e = 0; e < 4; ++e) { const unsigned u = v[e] ^ ((t >> (8 * e)) & 0xFFu); bad |= u; r[e] = U2F(u); } return r; }
DEV bool tagok(unsigned bad) { return __builtin_amdgcn_ballot_w64((bad & 0xFFu) != 0u) == 0ull; }
DEV bool clean(unsigned m) { return __builtin_amdgcn_ballot_w64(m == POISON) == 0ull; }
DEV bool all64(bool c) { return __builtin_amdgcn_ballot_w64(!c) == 0ull; }
DEV float phi1(float a) { return __builtin_fmaf(fmaxf(a, 1.44269504e-6f), 0.693147181f, __builtin_amdgcn_fmed3f(__builtin_amdgcn_exp2f(a), 0.f, 1.f)); }
DEV unsigned phi2(f2 a) {
  const f2 e = (f2){__builtin_amdgcn_fmed3f(__builtin_amdgcn_exp2f(a[0]), 0.f, 1.f), __builtin_amdgcn_fmed3f(__builtin_amdgcn_exp2f(a[1]), 0.f, 1.f)};
  const f2 r = __builtin_elementwise_fma((f2){fmaxf(a[0], 1.44269504e-6f), fmaxf(a[1], 1.44269504e-6f)}, (f2)(0.693147181f), e);
  return pk2(r[0], r[1]);
}
#define RD128(dst, addr, OFF) asm volatile("ds_read_b128 %0, %1 offset:%2" : "=v"(dst) : "v"(addr), "i"(OFF) : "memory")
#define RD64(dst, addr, OFF) asm volatile("ds_read_b64 %0, %1 offset:%2" : "=v"(dst) : "v"(addr), "i"(OFF) : "memory")
#define RD32(dst, addr, OFF) asm volatile("ds_read_b32 %0, %1 offset:%2" : "=v"(dst) : "v"(addr), "i"(OFF) : "memory")
#define RDTR(dst, addr, OFF) asm volatile("ds_read_b64_tr_b16 %0, %1 offset:%2" : "=v"(dst) : "v"(addr), "i"(OFF) : "memory")
#define SBAR() __builtin_amdgcn_sched_barrier(0)
#define CAT(x, y) __builtin_shufflevector(x, y, 0, 1, 2, 3, 4, 5, 6, 7)
#define WAITV(n) asm volatile("s_waitcnt vmcnt(%0)" ::"i"(n) : "memory")
#define WAITL(n) asm volatile("s_waitcnt lgkmcnt(%0)" ::"i"(n) : "memory")
#define CLOB() asm volatile("" ::: "memory")
#define BAR() asm volatile("s_barrier" ::: "memory")
#define LBAR() asm volatile("s_waitcnt lgkmcnt(0)\n s_barrier" ::: "memory")
#define PIN(v) asm volatile("" : "+v"(v))
#define PACE(n) do { SBAR(); __builtin_amdgcn_s_sleep(n); SBAR(); } while (0)
#define RFL(v) ((unsigned)__builtin_amdgcn_readfirstlane(v))
DEV const char* uptr(const char* p) {
  u64 v = (u64)p;
  return (const char*)(((u64)RFL((unsigned)(v >> 32)) << 32) | RFL((unsigned)v));
}
DEV u4 mkrs(const char* p, unsigned bytes) {
  const u64 v = (u64)uptr(p);
  u4 r; r[0] = (unsigned)v; r[1] = (unsigned)(v >> 32) & 0xFFFFu; r[2] = RFL(bytes); r[3] = 0x00020000u;
  return r;
}
#define BLDS(voff, rs, so, OFF) asm volatile("buffer_load_dwordx4 %0, %1, %2 offen offset:%3 lds" ::"v"(voff), "s"(rs), "s"(so), "i"(OFF) : "memory")
#define MFMA16(a, b, c) __builtin_amdgcn_mfma_f32_16x16x32_bf16(a, b, c, 0, 0, 0)
#define MFMAH(a, b, c) __builtin_amdgcn_mfma_f32_16x16x16bf16_1k(__builtin_bit_cast(s4, (u2){(a), 0u}), __builtin_bit_cast(s4, (u2){(b), 0u}), c, 0, 0, 0)
#define DOT2(a, b, c) __builtin_amdgcn_fdot2_f32_bf16(__builtin_bit_cast(bf2, (unsigned)(a)), __builtin_bit_cast(bf2, (unsigned)(b)), c, false)
#define BC8(v) __builtin_bit_cast(s8, v)
#define LD128(off) __builtin_amdgcn_raw_buffer_load_b128(wrs, (off), 0, 16)
#define LD64(off) __builtin_amdgcn_raw_buffer_load_b64(wrs, (off), 0, 16)
#define LD32(off) __builtin_amdgcn_raw_buffer_load_b32(wrs, (off), 0, 16)
#define ST128(v, off) __builtin_amdgcn_raw_buffer_store_b128((v), wrs, (off), 0, 16)
#define ST64(v, off) __builtin_amdgcn_raw_buffer_store_b64((v), wrs, (off), 0, 16)
#define ST32(v, off) __builtin_amdgcn_raw_buffer_store_b32((v), wrs, (off), 0, 16)
#define ALD128(dst, off) asm volatile("buffer_load_dwordx4 %0, %1, %2, 0 offen sc1" : "=v"(dst) : "v"(off), "s"(rsW) : "memory")
#define ALD64(dst, off) asm volatile("buffer_load_dwordx2 %0, %1, %2, 0 offen sc1" : "=v"(dst) : "v"(off), "s"(rsW) : "memory")
#define ALD32(dst, off) asm volatile("buffer_load_dword %0, %1, %2, 0 offen sc1" : "=v"(dst) : "v"(off), "s"(rsW) : "memory")
constexpr unsigned SPIN_CAP = 1u << 20;

DEV void lds_arrive(LAS unsigned* c) {
  asm volatile("s_mov_b64 exec, 1\n ds_add_u32 %0, %1\n s_mov_b64 exec, -1" ::"v"((unsigned)(size_t)c), "v"(1u) : "memory");
}
template <int SL> DEV void lds_wait(LAS unsigned* c, unsigned target) {
#pragma nounroll
  while (RFL(__hip_atomic_load(c, __ATOMIC_RELAXED, __HIP_MEMORY_SCOPE_WORKGROUP)) < target) { if (SL) __builtin_amdgcn_s_sleep(SL); }
  CLOB();
}

#define STMP(idx)
#define STMC(idx)
#define STDUMP()
#define DPPF(v, ctrl, rm) I2F(__builtin_amdgcn_update_dpp(0, F2I(v), ctrl, rm, 0xf, false))
DEV unsigned wave_max(unsigned v) {
  v = max(v, (unsigned)__builtin_amdgcn_update_dpp(0, (int)v, 0x111, 0xf, 0xf, false));
  v = max(v, (unsigned)__builtin_amdgcn_update_dpp(0, (int)v, 0x112, 0xf, 0xf, false));
  v = max(v, (unsigned)__builtin_amdgcn_update_dpp(0, (int)v, 0x114, 0xf, 0xf, false));
  v = max(v, (unsigned)__builtin_amdgcn_update_dpp(0, (int)v, 0x118, 0xf, 0xf, false));
  v = max(v, (unsigned)__builtin_amdgcn_update_dpp(0, (int)v, 0x142, 0xa, 0xf, false));
  v = max(v, (unsigned)__builtin_amdgcn_update_dpp(0, (int)v, 0x143, 0xc, 0xf, false));
  return (unsigned)__builtin_amdgcn_readlane((int)v, 63);
}

constexpr int SLOT = 49152, L_B = 32768;
constexpr int L_EX = SLOT;
constexpr int L_ROWM = 159744;
constexpr int L_ROWS = L_ROWM + 1024;
constexpr int L_COL = L_ROWS + 1024;
constexpr int L_TAG = L_COL + 512;
constexpr int L_CNT = L_TAG + 64;
constexpr int L_TOP = L_CNT + 64;
constexpr int L_XT = 0;
constexpr int L_KX = 16384;
constexpr int L_ZX = L_KX + 8192;
constexpr int L_YS = L_ZX + 256;
constexpr int L_WO = 28672;
constexpr int K_LDS = 163840;

#define GAS __attribute__((address_space(1)))
#define NTL(p) __builtin_nontemporal_load((const GAS u4*)(p))
#define TRFRAG(f, W, c) do { _Pragma("unroll") for (int q_ = 0; q_ < 4; ++q_) { unsigned e_ = W[2 * q_][(c) >> 1], o_ = W[2 * q_ + 1][(c) >> 1]; \
    f[q_] = ((c) & 1) ? __builtin_amdgcn_perm(o_, e_, 0x07060302u) : __builtin_amdgcn_perm(o_, e_, 0x05040100u); } } while (0)
constexpr u64 TAG_STEP = 0x9E3779B97F4A7C15ull;

struct FW { s8 a[8]; s4 b[8]; };
#define RA(G, s, u, i) RD128(G.a[i], aad[((s) % 3) == 2][u], (((s) % 3) & 1) * SLOT + (i) * 2048)
#define RB(G, s, u, n) RDTR(G.b[n], bad[((s) % 3) == 2][((n) >> 1) & 1], (((s) % 3) & 1) * SLOT + ((n) >> 2) * 8192 + (u) * 4096 + ((n) & 1) * 512)
#define FB(G, j) CAT(G.b[2 * (j)], G.b[2 * (j) + 1])
#define RW(G, s, u, n) do { if ((n) < 8) RB(G, s, u, (n) & 7); else RA(G, s, u, ((n) - 8) & 7); SBAR(); } while (0)
#define SNK(n) ((((n) >> 2) & 1) ? 3 - ((n) & 3) : (n) & 3)
#define MW(G, n) do { if (TRN) c[(n) >> 2][SNK(n)] = MFMA16(FB(G, SNK(n)), G.a[(n) >> 2], c[(n) >> 2][SNK(n)]); \
    else c[(n) >> 2][SNK(n)] = MFMA16(G.a[(n) >> 2], FB(G, SNK(n)), c[(n) >> 2][SNK(n)]); SBAR(); } while (0)
#define MFA(c_, a_, b_) asm volatile("v_mfma_f32_16x16x32_bf16 %0, %1, %2, %0" : "+v"(c_) : "v"(a_), "v"(b_))
#define MA(G, n) do { if (TRN) MFA(c[(n) >> 2][SNK(n)], FB(G, SNK(n)), G.a[(n) >> 2]); else MFA(c[(n) >> 2][SNK(n)], G.a[(n) >> 2], FB(G, SNK(n))); SBAR(); } while (0)
#define MW2(Fq, G, sn, un, n) do { MA(Fq, 2 * (n)); if ((n) < 12) RW(G, sn, un, (n) + ((n) + 2) / 3); MA(Fq, 2 * (n) + 1); if ((n) < 12 && (n) % 3 == 0) RW(G, sn, un, (n) + (n) / 3 + 1); } while (0)
#define HALFW(Fq, G, sn, un) do { MW2(Fq, G, sn, un, 0); MW2(Fq, G, sn, un, 1); MW2(Fq, G, sn, un, 2); MW2(Fq, G, sn, un, 3); MW2(Fq, G, sn, un, 4); MW2(Fq, G, sn, un, 5); MW2(Fq, G, sn, un, 6); MW2(Fq, G, sn, un, 7); \
    MW2(Fq, G, sn, un, 8); MW2(Fq, G, sn, un, 9); MW2(Fq, G, sn, un, 10); MW2(Fq, G, sn, un, 11); MW2(Fq, G, sn, un, 12); MW2(Fq, G, sn, un, 13); MW2(Fq, G, sn, un, 14); MW2(Fq, G, sn, un, 15); WAITL(0); SBAR(); } while (0)
#define STAGEW(s) do { HALFW(f0, f1, s, 1); BAR(); SBAR(); HALFW(f1, f0, (s) + 1, 0); } while (0)
#define KLOOP() \
      unsigned aad[2][2], bad[2][2]; \
      { LANE(); const int q = np >> 2, p = l & 3, f = ((q >> 1) & 1) | ((g & 1) << 1); \
        _Pragma("unroll") for (int u = 0; u < 2; ++u) { aad[0][u] = ldsb + (wm * 128 + np) * 128 + (((4 * u + g) ^ (np >> 1)) * 16); aad[1][u] = aad[0][u] + 2 * SLOT; \
          bad[0][u] = ldsb + L_B + (8 * g + q) * 128 + (((2 * wn + u) ^ f) * 32) + p * 8; bad[1][u] = bad[0][u] + 2 * SLOT; \
          PIN(aad[0][u]); PIN(aad[1][u]); PIN(bad[0][u]); PIN(bad[1][u]); } } \
      f4 c[8][4]; \
      _Pragma("unroll") for (int i = 0; i < 8; ++i) _Pragma("unroll") for (int jj = 0; jj < 4; ++jj) { c[i][jj] = (f4)(0.f); PIN(c[i][jj]); } \
      FW f0, f1; \
      BAR(); \
      STMP(1); STMC(40); \
      RW(f0, 0, 0, 0); RW(f0, 0, 0, 1); RW(f0, 0, 0, 8); RW(f0, 0, 0, 2); RW(f0, 0, 0, 3); RW(f0, 0, 0, 4); RW(f0, 0, 0, 5); RW(f0, 0, 0, 6); RW(f0, 0, 0, 7); \
      RW(f0, 0, 0, 9); RW(f0, 0, 0, 10); RW(f0, 0, 0, 11); RW(f0, 0, 0, 12); RW(f0, 0, 0, 13); RW(f0, 0, 0, 14); RW(f0, 0, 0, 15); \
      WAITL(0); SBAR(); \
      STAGEW(0); STAGEW(1); STAGEW(2); STAGEW(3); STAGEW(4); STAGEW(5); STAGEW(6); STAGEW(7); \
      STAGEW(8); STAGEW(9); STAGEW(10); STAGEW(11); STAGEW(12); STAGEW(13); STAGEW(14); \
      unsigned tR, tC; \
      { LANE(); tR = ldsb + L_ROWM + (wm * 128 + (TRN ? np : 4 * g)) * 4; tC = ldsb + L_COL + (wn * 32 + (TRN ? 4 * g : np)) * 4; PIN(tR); PIN(tC); SBAR(); } \
      HALFW(f0, f1, 15, 1); if (TRN) BAR(); SBAR()
#define MT(n) do { MW(f1, n); TB(n); } while (0)
#define KLAST() do { MT(0); MT(1); MT(2); MT(3); MT(4); MT(5); MT(6); MT(7); MT(8); MT(9); MT(10); MT(11); MT(12); MT(13); MT(14); MT(15); \
      MT(16); MT(17); MT(18); MT(19); MT(20); MT(21); MT(22); MT(23); MT(24); MT(25); MT(26); MT(27); MT(28); MT(29); MT(30); MT(31); WAITL(0); } while (0)
#define H2(v, e0) ((f2){(v)[e0], (v)[(e0) + 1]})

#define WQ0 56
#define HXM 0
#define QROT2 1
#define MORD 3
#define PO 0
#define FI (PO ? 3 : 11)
#define PM 2
#define WE 1
#define KD 70
#define KSL 2
#define PD5 8
#define OSL 1
#define MD1 0
#define MD2 1
#define TKP 13
#define CL0 6

__global__ __launch_bounds__(512, 2) void fused(const u16* __restrict__ x, const u16* __restrict__ wqkv, const u16* __restrict__ wo, const u16* __restrict__ wr,
                                                const u16* __restrict__ wg, const u16* __restrict__ wu, const u16* __restrict__ wd, char* ws,
                                                u16* __restrict__ out) {
  extern __shared__ __attribute__((aligned(16))) char lds[];
  const int w = __builtin_amdgcn_readfirstlane(threadIdx.x >> 6);
#define LANE() int l; asm volatile("v_mbcnt_lo_u32_b32 %0, -1, 0\n v_mbcnt_hi_u32_b32 %0, -1, %0" : "=v"(l)); \
  const int np = l & 15, g = l >> 4, rr = l >> 3, sl = l & 7; (void)np; (void)g; (void)rr; (void)sl
  STMP(0);
  const int gb = blockIdx.x, xcd = gb & 7, j = gb >> 3, xq = xcd >> 1;
  const unsigned ldsb = (unsigned)(size_t)(LAS char*)lds;
  const bool isQ = j >= 16;
  const int rg = (xcd & 1) * 4 + (j & 3), hp = xq * 4 + (j >> 2), qg = xq * 2 + ((j >> 2) & 1);
  const int hd0 = isQ ? qg * 4 : 32 + hp * 2, hd1 = isQ ? 2 : 32;
  const bool isB = w == 6;
  unsigned mb_, step, d1, d2, d3, so, lim; u4 rs;
#define VSET() unsigned v[4]; { const unsigned pitch = isB ? 6144 : 2048, sm = isB ? 2 : 7; \
    _Pragma("unroll") for (int i = 0; i < 4; ++i) v[i] = (8 * i + rr) * pitch + ((sl ^ ((isB ? rr : rr >> 1) & sm) ^ ((i & 1) << 2)) * 16) - i * 1024; }
#define QL 4
#define QROT 1
#define G4(s, q, dq) do { unsigned t_; asm volatile("s_add_u32 m0, %1, %2\n s_add_u32 %0, %3, %4" : "=s"(t_) : "s"(mb_), "i"(((s) % 3) * SLOT + (q) * 4096), "s"(so), "s"(dq) : "scc", "memory"); \
        BLDS(v[0], rs, t_, 0); BLDS(v[1], rs, t_, 1024); BLDS(v[2], rs, t_, 2048); BLDS(v[3], rs, t_, 3072); } while (0)
#define PA(s) do { G4(s, 0, 0u); G4(s, 1, d1); } while (0)
#define PB(s) do { G4(s, 2, d2); G4(s, 3, d3); asm volatile("s_add_u32 %0, %0, %1\n s_cmp_eq_u32 %0, %2\n s_cselect_b32 %0, 0, %0" : "+s"(so) : "s"(step), "s"(lim) : "scc"); } while (0)
  if (__builtin_expect(j < 24 && w >= 4 && w < 7, 1)) {
    LANE();
    rs = isB ? mkrs((const char*)wqkv + hd0 * 64, 1024 * 6144 - hd0 * 64) : mkrs((const char*)x + (size_t)(rg * 256 + (w - 4) * 128) * 2048, 128 * 2048);
    mb_ = RFL(ldsb + (w - 4) * 16384); step = isB ? 64 * 6144 : 128;
    d1 = isB ? 32 * 6144 : 65536; d2 = isB ? hd1 * 64 : 131072; d3 = d1 + d2;
    so = isQ ? (QROT + QROT2 * (qg & 1)) * step : 0; lim = QL * step;
    VSET();
    PA(0); PB(0); PA(1);
  }
#define CPF 1
  u4 rsC;
  { u64 pc; unsigned len;
    asm volatile("s_getpc_b64 %0\n.Lcp0:\n s_mov_b32 %1, .Lcp1-.Lcp0\n s_max_i32 %1, %1, .Lcp2-.Lcp0\n s_max_i32 %1, %1, .Lcp3-.Lcp0\n s_max_i32 %1, %1, .Lcp4-.Lcp0\n s_max_i32 %1, %1, .Lcp5-.Lcp0\n s_max_i32 %1, %1, .Lcp6-.Lcp0\n s_max_i32 %1, %1, 0" : "=&s"(pc), "=&s"(len) :: "scc");
    rsC = (u4){(unsigned)pc & ~127u, (unsigned)(pc >> 32) & 0xFFFFu, len, 0x00020000u}; }
#define CODE(n) asm volatile("s_mov_b32 m0, %2\n s_nop 0\n buffer_load_dwordx4 %0, %1, 0 offen lds" :: "v"((n) * 1024 + l * 16), "s"(rsC), "s"(RFL(ldsb + 3 * SLOT)) : "memory")
  if (CPF && j < 24 && w < 4) { LANE(); CODE(j * 4 + w); }
#define CPL(n) do { __builtin_amdgcn_s_waitcnt(0x0F70); asm volatile(".Lcp" #n ":"); } while (0)
#define CPE(n) asm volatile(".Lcp" #n ":")
  const __amdgpu_buffer_rsrc_t wrs = __builtin_amdgcn_make_buffer_rsrc(ws, 0, WS_END, 0x00020000);
  const int mb2 = (xcd & 1) * 8 + (xcd >> 2) * 4 + (j >> 3), nb = ((xcd >> 1) & 1) * 8 + (j & 7), lq = (mb2 + (mb2 >> 2)) & 3, hx = (nb * 4) & HXM;
#define msg (WS_MSG + xcd * 256 + ((l & 8) ? 0 : 64 + sl * 8))
#define MSGOK(f) all64((((l & 8) ? ((f)[0] ^ tlo) | ((f)[1] ^ thi) : (f)[1] ^ tlo)) == 0u)
#define KEYOK(f) all64((l & 8) || (f)[1] == tlo)
#define GBAD(v) (((v)[1] ^ tlo) | ((v)[3] ^ thi))
#define WO_SETUP() const u4 rsO = mkrs((const char*)wo + nb * 128, 1024 * 2048 - nb * 128), rsX = mkrs((const char*)x + (size_t)mb2 * 128 * 2048 + nb * 128, 128 * 2048 - nb * 128); \
  const unsigned vW = rr * 2048 + ((sl ^ (((rr >> 1) & 3) << 1)) * 16), vX = rr * 2048 + ((sl ^ rr) * 16)
#define DMA1(v, rs, m, so) asm volatile("s_mov_b32 m0, %2\n s_nop 0\n buffer_load_dwordx4 %0, %1, %3 offen lds" ::"v"(v), "s"(rs), "s"(RFL(m)), "s"(RFL(so)) : "memory")
#define WOL(n0, n1, st) _Pragma("nounroll") for (int n_ = (n0); n_ < (n1); n_ += (st)) DMA1(vW, rsO, ldsb + L_WO + n_ * 1024, (n_ * 16384) ^ (hx * 65536))
#define WOR(n0, n1, st) _Pragma("nounroll") for (int n_ = (n0); n_ < (n1); n_ += (st)) DMA1(vW, rsO, ldsb + L_WO + (n_ ^ (lq * 32) ^ (hx * 4)) * 1024, (n_ ^ (lq * 32)) * 16384)
#define XTL(n0, st) _Pragma("nounroll") for (int n_ = (n0); n_ < 16; n_ += (st)) DMA1(vX, rsX, ldsb + L_XT + n_ * 1024, n_ * 16384)
  const int ow = isQ && j < 24 ? w ^ 4 : w;
  f4 oc[2][4];
  u4 st2[2];
  unsigned tpoll = 0;
#define TPOLL(d) tpoll = (unsigned)__builtin_amdgcn_s_memrealtime() + (d)
#define PDK 280
#define PDQ 80
#define XT(r, ct) (xt + (r) * 2048 + (((2 * (ct) + (g >> 1)) ^ sl) * 16))
#define S2OK() all64(((st2[0][1] ^ t_[0]) | (st2[0][3] ^ t_[1]) | (st2[1][1] ^ t_[0]) | (st2[1][3] ^ t_[1])) == 0u)
#define OINIT() do { PIN(st2[0]); PIN(st2[1]); const char* xt = lds + L_XT + (ow * 32 + np) * 128 + (g & 1) * 8; \
    { const u2 t_ = *(const u2*)(lds + L_TAG); if (__builtin_expect(!S2OK(), 0)) { _Pragma("nounroll") for (unsigned it = 0; it < SPIN_CAP; ++it) { CLOB(); ST2(); if (S2OK()) break; } } } \
    _Pragma("unroll") for (int r = 0; r < 2; ++r) { \
      const float c1 = 1.f + U2F(st2[r][2]), c0 = -hi(st2[r][0]) * U2F(st2[r][2]); \
      _Pragma("unroll") for (int ct = 0; ct < 4; ++ct) { const u2 v = *(const u2*)XT(r, ct); \
        oc[r][ct] = (f4){__builtin_fmaf(lo(v[0]), c1, c0), __builtin_fmaf(hi(v[0]), c1, c0), __builtin_fmaf(lo(v[1]), c1, c0), __builtin_fmaf(hi(v[1]), c1, c0)}; PIN(oc[r][ct]); } \
      } } while (0)
#define XOWN() do { WO_SETUP(); (void)vW; (void)rsO; _Pragma("unroll") for (int n_ = 0; n_ < 4; ++n_) DMA1(vX, rsX, ldsb + L_XT + (ow * 4 + n_) * 1024, (ow * 4 + n_) * 16384); \
    const u4 rsW = mkrs(ws, WS_END); const unsigned sto = WS_MS + (mb2 * 128 + (w & 3) * 32 + np) * 16, sto1 = sto + 256; ALD128(st2[0], sto); ALD128(st2[1], sto1); } while (0)
#define ST2() do { const unsigned sto = WS_MS + (mb2 * 128 + (w & 3) * 32 + np) * 16; st2[0] = LD128(sto); st2[1] = LD128(sto + 256); } while (0)

  if (j < 24) {
    if (w >= 4) {
      LANE();
      unsigned tlo = 0, thi = 0;
      WO_SETUP();
      u2 f = (u2){0u, 0u};
      if (w == 7) {
        const u4 rsW = mkrs(ws, WS_END);
        int nbar = 16;
#define TICK() do { if (nbar) { BAR(); --nbar; } } while (0)
        if (l < 16) ((unsigned*)(lds + L_CNT))[l] = 0u;
        WAITL(0);
        TICK();
        u2 ep; unsigned z0 = 0; PIN(z0);
        ALD64(ep, z0);
        { const u4 pz = (u4){POISON, POISON, POISON, POISON};
          if (isQ) {
            ST128(pz, WS_OFL + ((rg * 2 + (l >> 5)) * 32 + qg * 4) * 128 + (l & 31) * 16);
          } else {
            const unsigned kb = WS_KVP + (hp * 16 + rg) * KVS + l * 16;
            ST128(pz, kb); ST128(pz, kb + 1024); ST128(pz, kb + 8 * KVS); ST128(pz, kb + 8 * KVS + 1024);
            ST128(pz, l < 16 ? WS_KVP + (hp * 16 + rg + (l & 8)) * KVS + 2048 + sl * 16 : WS_OFF);
          } }
        SBAR();
        if (PO && isQ) {
#define PPS 8
          TICK();
#pragma nounroll
          for (int i = 0; i < 16; i += PPS) {
            TICK();
            const unsigned ob = WS_OF + ((rg * 16 + i) * 32 + qg * 4) * 1024 + l * 16; const u4 pz = (u4){POISON, POISON, POISON, POISON};
#pragma unroll
            for (int k = 0; k < PPS; ++k) { ST128(pz, ob + k * 32768); ST128(pz, ob + k * 32768 + 1024); }
          }
        } else TICK();
        TICK(); TICK();
        WAITV(0); PIN(ep);
        const u64 tag = (((u64)RFL(ep[1]) << 32) | RFL(ep[0])) + TAG_STEP;
        tlo = (unsigned)tag; thi = (unsigned)(tag >> 32);
        ST64(((u2){tlo, thi}), l == 0 ? WS_FLAG + gb * 8 : WS_OFF);
        STMP(8);
#pragma nounroll
        while (nbar > 16 - TKP) { TICK();
#define CPF2 10
          if (CPF2 && nbar == 16 - CPF2) { CODE(j * 4); CODE(j * 4 + 1); CODE(j * 4 + 2); CODE(j * 4 + 3); }
        }
        u4 ms[4], cs[8];
        const unsigned a0 = WS_MS + (rg * 256 + l) * 16, a2 = WS_CS + ((g * 96 + hd0) * 16 + np) * 16, a3 = a2 + hd1 * 256, a4 = a2 + 4 * 96 * 256, a5 = a3 + 4 * 96 * 256, a6 = msg;
#pragma nounroll
        for (unsigned it = 0; it < SPIN_CAP; ++it) {
#pragma unroll
          for (int i = 0; i < 4; ++i) asm volatile("buffer_load_dwordx4 %0, %1, %2, 0 offen offset:%3 sc1" : "=v"(ms[i]) : "v"(a0), "s"(rsW), "i"(i * 1024) : "memory");
#pragma unroll
          for (int i = 0; i < 8; ++i) asm volatile("buffer_load_dwordx4 %0, %1, %2, 0 offen offset:%3 sc1" : "=v"(cs[i]) : "v"(i < 2 ? a2 : i < 4 ? a3 : i < 6 ? a4 : a5), "s"(rsW), "i"((i & 1) * 256) : "memory");
          ALD64(f, a6);
          if (nbar > 1) TICK();
          if (nbar > 1) TICK();
          WAITV(0);
          unsigned bad = 0;
#pragma unroll
          for (int i = 0; i < 4; ++i) { PIN(ms[i]); PIN(cs[i]); PIN(cs[4 + i]); bad |= GBAD(ms[i]) | GBAD(cs[i]) | GBAD(cs[4 + i]); }
          PIN(f);
          if (all64(bad == 0u)) break;
        }
        STMP(9);
        float ct[2];
#pragma unroll
        for (int e = 0; e < 2; ++e) {
          float t[4];
#pragma unroll
          for (int i = 0; i < 4; ++i) t[i] = add1(U2F(cs[i][2 * e]), U2F(cs[4 + i][2 * e]));
#pragma unroll
          for (int i = 0; i < 2; ++i) { auto r = __builtin_amdgcn_permlane32_swap(F2U(t[i]), F2U(t[i + 2]), false, false); t[i] = add1(U2F(r[0]), U2F(r[1])); }
          auto r = __builtin_amdgcn_permlane16_swap(F2U(t[0]), F2U(t[1]), false, false);
          ct[e] = add1(U2F(r[0]), U2F(r[1])); PIN(ct[e]);
        }
#pragma unroll
        for (int i = 0; i < 4; ++i) { ms[i][0] = (ms[i][0] & 0xFFFF0000u) ^ 0x80000000u; ms[i][1] = F2U(mul1(U2F(ms[i][2]), 1.44269504f)); PIN(ms[i]); }
#pragma nounroll
        while (nbar) TICK();
#pragma unroll
        for (int i = 0; i < 4; ++i) { ((unsigned*)(lds + L_ROWM))[i * 64 + l] = ms[i][0]; ((unsigned*)(lds + L_ROWS))[i * 64 + l] = ms[i][1]; }
        ((float*)(lds + L_COL))[g * 32 + np] = ct[0]; ((float*)(lds + L_COL))[g * 32 + 16 + np] = ct[1];
        if (l == 8) *(u2*)(lds + L_TAG) = (u2){tlo, thi};
        if (!isQ) lds_arrive((LAS unsigned*)(lds + L_CNT) + 5);
      } else {
        VSET();
        WAITV(8); BAR(); PB(1); PA(2); WAITV(8); BAR(); STMP(44); PB(2); if (QL == 3) so = lim; PA(0); PB(0);
#define LS(s) do { if ((s) < 14) WAITV(16); else WAITV(0); BAR(); STMP(44 + (s)); if ((s) + 3 < 16) { if ((s) + 3 == QL) so = lim; PA(s); PB(s); } } while (0)
        LS(1); LS(2); LS(3); LS(4); LS(5); LS(6); LS(7); LS(8); LS(9); LS(10); LS(11); LS(12); LS(13); LS(14);
      }
#define KEYS() do { if (w == 7) { _Pragma("nounroll") for (unsigned it = 0; it < SPIN_CAP && !MSGOK(f); ++it) { f = LD64(msg); CLOB(); } \
        if (l < 8) ((unsigned*)(lds + L_TAG + 16))[l] = f[0]; \
        lds_arrive((LAS unsigned*)(lds + L_CNT) + 3); } } while (0)
      if (isQ) {
        if (w < 7) { WOL(20 + w - 4, WQ0, 3); }
        LBAR(); STMP(59);
        XOWN(); SBAR(); WOL(WQ0 + w - 4, 128, 4); WOL(w - 4, 20, 4);
        const unsigned tq = (unsigned)__builtin_amdgcn_s_memrealtime();
        WAITV(5); OINIT();
        KEYS();
        STMP(21);
        const unsigned sb = WS_KVP + (qg * 4 + (w & 1)) * 8 * KVS + ((w >> 1) & 1) * 1024 + l * 16;
        const unsigned sz = (w & 2) ? WS_OFF : WS_KVP + ((qg * 4 + (w & 1)) * 8 + (np >> 1)) * KVS + 2048 + (np & 1) * 64 + g * 16;
        u4 prm[2];
#pragma unroll
        for (int e = 0; e < 4; ++e) {
          const unsigned o = (np >> 2) == g ? ((np & 1) ? 0x3F800000u : 0x00003F80u) : 0u;
          prm[0][e] = e == ((np >> 1) & 1) ? o : 0u; prm[1][e] = e == 2 + ((np >> 1) & 1) ? o : 0u;
        }
        PIN(prm[0]); PIN(prm[1]);
        __builtin_amdgcn_s_setprio(2);
#pragma nounroll
        while ((unsigned)__builtin_amdgcn_s_memrealtime() - tq < KD) __builtin_amdgcn_s_sleep(1);
        lds_wait<1>((LAS unsigned*)(lds + L_CNT) + 3, 1);
        u4 v[16], zv[2]; f4 kg[4][2];
#define KVLD(gq) do { _Pragma("unroll") for (int p = 4 * (gq); p < 4 * (gq) + 4; ++p) v[p] = __builtin_amdgcn_raw_buffer_load_b128(wrs, sb, ((p & 7) + (p >> 3) * 16) * KVS, 16); } while (0)
#define ZLD(hh) zv[hh] = __builtin_amdgcn_raw_buffer_load_b128(wrs, sz, (hh) * 16 * KVS, 16)
        ZLD(0); KVLD(0); KVLD(1); SBAR(); ZLD(1); KVLD(2); KVLD(3); SBAR();
#pragma unroll
        for (int hh = 0; hh < 2; ++hh) {
          unsigned pend = 7;
#pragma nounroll
          for (unsigned it = 0; it < SPIN_CAP; ++it) {
#pragma unroll
            for (int gq = 2 * hh; gq < 2 * hh + 2; ++gq)
              if (pend & (1u << (gq & 1))) {
                f4 t0_ = (f4)(0.f), t1_ = (f4)(0.f); unsigned mx = 0;
#pragma unroll
                for (int p = 4 * gq; p < 4 * gq + 4; ++p) { mx = mx4(mx, v[p]); t0_ = MFMA16(BC8(prm[0]), BC8(v[p]), t0_); t1_ = MFMA16(BC8(prm[1]), BC8(v[p]), t1_); }
                kg[gq][0] = t0_; kg[gq][1] = t1_;
                if (clean(mx)) pend &= ~(1u << (gq & 1));
              }
            if ((pend & 4u) && clean(mx4(0u, zv[hh]))) pend &= ~4u;
            if (!pend) break;
            if (KSL) __builtin_amdgcn_s_sleep(KSL);
            CLOB();
            if (pend & 4u) ZLD(hh);
            if (pend & 1u) KVLD(2 * hh);
            if (pend & 2u) KVLD(2 * hh + 1);
            if (hh == 0) { ZLD(1); KVLD(2); KVLD(3); }
            SBAR();
          }
          if (hh) STMP(25); else STMP(4);
          const f4 k0 = kg[2 * hh][0] + kg[2 * hh + 1][0], k1 = kg[2 * hh][1] + kg[2 * hh + 1][1];
          *(u4*)(lds + L_KX + ((2 * hh + (w & 1)) * 2 + ((w >> 1) & 1)) * 1024 + l * 16) = (u4){pk2(k0[0], k0[1]), pk2(k0[2], k0[3]), pk2(k1[0], k1[1]), pk2(k1[2], k1[3])};
          if (!(w & 2)) {
            f4 z = __builtin_bit_cast(f4, zv[hh]);
#pragma unroll
            for (int e = 0; e < 4; ++e) { z[e] += DPPF(z[e], 0x122, 0xf); z[e] += DPPF(z[e], 0x124, 0xf); z[e] += DPPF(z[e], 0x128, 0xf); }
            if (np < 2) *(u2*)(lds + L_ZX + (2 * hh + (w & 1)) * 64 + g * 16 + np * 8) = (u2){pk2(z[0], z[1]), pk2(z[2], z[3])};
          }
          LBAR();
        }
        __builtin_amdgcn_s_setprio(0);
        CPL(3);
        TPOLL(PDQ);
      } else {
        if (w < 7) { if (WE) { WOL(32 + w, 128, 3); WAITV(0); STMP(35); } lds_arrive((LAS unsigned*)(lds + L_CNT) + 2); }
        KEYS();
        __builtin_amdgcn_s_waitcnt(0x0F70);
      }
    } else {
      const int wm = w >> 1, wn = w & 1;
      if (!isQ) {
        constexpr bool TRN = false;
        KLOOP();
        float cc[4]; f4 rsk[8], mk[8]; unsigned tf, cA = ldsb + L_CNT; PIN(cA);
#define TB1(n) do { if ((n) < 4) RD32(cc[(n) & 3], tC, ((n) >> 1) * 256 + ((n) & 1) * 64); else if ((n) & 1) RD128(rsk[(((n) - 4) >> 1) & 3], tR, 1024 + (((n) - 4) >> 1) * 64); else RD128(mk[(((n) - 4) >> 1) & 3], tR, (((n) - 4) >> 1) * 64); } while (0)
#define TB(n) do { if ((n) == 8) RD32(tf, cA, 20); else if ((n) > 8 && (n) < 21) TB1(((n) - 9) % 12); SBAR(); } while (0)
        KLAST();
#undef TB
        PIN(tf);
        if (__builtin_expect(RFL(tf) == 0u, 0)) {
          lds_wait<0>((LAS unsigned*)(lds + L_CNT) + 5, 1);
          TB1(0); TB1(1); TB1(2); TB1(3); TB1(4); TB1(5); TB1(6); TB1(7); TB1(8); TB1(9); TB1(10); TB1(11); WAITL(0);
        }
#pragma unroll
        for (int i = 0; i < 4; ++i) { PIN(mk[i]); PIN(rsk[i]); PIN(cc[i]); }
#pragma unroll
        for (int i = 4; i < 8; ++i) { RD128(mk[i], tR, i * 64); RD128(rsk[i], tR, 1024 + i * 64); }
        SBAR();
        STMP(2); STMC(41);
        f4 kv[2][2], zz[2];
        kv[0][0] = kv[0][1] = kv[1][0] = kv[1][1] = zz[0] = zz[1] = (f4)(0.f);
        const u4 ones = (u4){0x3F803F80u, 0x3F803F80u, 0x3F803F80u, 0x3F803F80u};
        u4 kf[2], vf[2];
#define LN2(i, jj, e0) (__builtin_elementwise_fma(H2(mk[i], e0), (f2)(cc[jj]), H2(c[i][jj], e0)) * H2(rsk[i], e0))
#define KVM(n) do { if ((n) < 4) kv[(n) >> 1][(n) & 1] = MFMA16(BC8(kf[(n) >> 1]), BC8(vf[(n) & 1]), kv[(n) >> 1][(n) & 1]); else zz[(n) & 1] = MFMA16(BC8(kf[(n) & 1]), BC8(ones), zz[(n) & 1]); } while (0)
#pragma unroll
        for (int ip = 0; ip < 4; ++ip) {
          if (ip == 2) { WAITL(0);
#pragma unroll
            for (int i = 4; i < 8; ++i) { PIN(mk[i]); PIN(rsk[i]); } }
          f2 ak[8], av[8], ek[8];
#pragma unroll
          for (int n = 0; n < 8; ++n) { const int i = 2 * ip + ((n >> 1) & 1), e0 = (n & 1) * 2, t = n >> 2; ak[n] = LN2(i, t, e0); av[n] = LN2(i, 2 + t, e0); }
          SBAR();
#pragma unroll
          for (int n = 0; n < 8; ++n) {
            if (ip > 0 && n < 6) KVM(n);
            ek[n] = (f2){__builtin_amdgcn_fmed3f(__builtin_amdgcn_exp2f(ak[n][0]), 0.f, 1.f), __builtin_amdgcn_fmed3f(__builtin_amdgcn_exp2f(ak[n][1]), 0.f, 1.f)};
            ak[n] = (f2){fmaxf(ak[n][0], 1.44269504e-6f), fmaxf(ak[n][1], 1.44269504e-6f)};
            SBAR();
          }
#pragma unroll
          for (int n = 0; n < 8; ++n) { const f2 r = __builtin_elementwise_fma(ak[n], (f2)(0.693147181f), ek[n]); kf[n >> 2][n & 3] = pk2(r[0], r[1]); vf[n >> 2][n & 3] = pk2(av[n][0], av[n][1]); }
          SBAR();
        }
        KVM(0); KVM(1); KVM(2); KVM(3); KVM(4); KVM(5);
        LANE();
        const unsigned kb = WS_KVP + ((hp * 2 + wn) * 8 + rg) * KVS;
        const unsigned exa = ldsb + L_EX + l * 16 + (w ^ 2) * 4096, pka = kb + wm * 1024 + l * 16, pza = wm == 0 && np < 2 ? kb + 2048 + np * 64 + g * 16 : WS_OFF;
        STMP(19);
        const f4 zs = (np & 1) ? zz[1] : zz[0];
        *(f4*)(lds + L_EX + l * 16 + w * 4096) = wm ? kv[0][0] : kv[0][1]; *(f4*)(lds + L_EX + l * 16 + w * 4096 + 1024) = wm ? kv[1][0] : kv[1][1];
        *(f4*)(lds + L_EX + l * 16 + w * 4096 + 2048) = zs;
        lds_arrive((LAS unsigned*)(lds + L_CNT) + 6 + wn);
        f4 e0, e1, e2;
        cA += wn * 4;
#pragma nounroll
        do {
          RD32(tf, cA, 24); RD128(e0, exa, 0); RD128(e1, exa, 1024); RD128(e2, exa, 2048);
          WAITL(0); PIN(tf); PIN(e0); PIN(e1); PIN(e2);
        } while (RFL(tf) < 2u);
        const f4 o0 = ((wm ? kv[0][1] : kv[0][0]) + e0) * 0.693147181f, o1 = ((wm ? kv[1][1] : kv[1][0]) + e1) * 0.693147181f;
        ST128(san4((u4){pk2(o0[0], o0[1]), pk2(o0[2], o0[3]), pk2(o1[0], o1[1]), pk2(o1[2], o1[3])}), pka);
        ST128(san4(__builtin_bit_cast(u4, zs + e2)), pza);
        SBAR();
        STMP(3);
        TPOLL(PDK);
        if (j == 1 && w == 0) { const u2 t_ = *(const u2*)(lds + L_TAG); ST128(((u4){t_[0], t_[1], tpoll, 0u}), l < 8 ? WS_HU + xcd * 128 + l * 16 : WS_OFF); }
        lds_arrive((LAS unsigned*)(lds + L_CNT) + 8);
        lds_wait<1>((LAS unsigned*)(lds + L_CNT) + 8, 4);
        XOWN(); SBAR();
        { WO_SETUP(); (void)vX; (void)rsX; if (WE) { WOL(w, 36, 4); } else { WOR(w, 128, 4); } }
        WAITV(WE ? 9 : 32); OINIT();
        WAITV(0);
        lds_arrive((LAS unsigned*)(lds + L_CNT) + 2);
        lds_wait<1>((LAS unsigned*)(lds + L_CNT) + 3, 1);
        lds_wait<1>((LAS unsigned*)(lds + L_CNT) + 2, 7);
        CPL(1);
        STMP(20);
      } else {
        constexpr bool TRN = true;
        KLOOP();
        f4 cc[4]; float rs[8], mn[8];
#define TB(n) do { if ((n) < 4) RD128(cc[(n) & 3], tC, ((n) >> 1) * 256 + ((n) & 1) * 64); else if ((n) < 20) { if ((n) & 1) RD32(rs[(((n) - 4) >> 1) & 7], tR, 1024 + (((n) - 4) >> 1) * 64); else RD32(mn[(((n) - 4) >> 1) & 7], tR, (((n) - 4) >> 1) * 64); } SBAR(); } while (0)
        KLAST();
#undef TB
#pragma unroll
        for (int i = 0; i < 8; ++i) { PIN(mn[i]); PIN(rs[i]); PIN(cc[i & 3]); }
        STMP(2); STMC(41);
        LANE();
        u4 pf[8][2];
#pragma unroll
        for (int i = 0; i < 8; ++i)
#pragma unroll
          for (int hh = 0; hh < 2; ++hh) {
#pragma unroll
            for (int e = 0; e < 4; ++e) { const int jj = 2 * hh + (e >> 1), e0 = (e & 1) * 2;
              pf[i][hh][e] = phi2(__builtin_elementwise_fma(H2(cc[jj], e0), (f2)(mn[i]), H2(c[i][jj], e0)) * rs[i]); }
            PIN(pf[i][hh]);
          }
        const u2 tg = *(const u2*)(lds + L_TAG);
        const unsigned ofl = l < 8 ? WS_OFL + ((rg * 2 + wm) * 32 + (qg * 2 + wn) * 2) * 128 + sl * 16 : WS_OFF;
        unsigned oa[8];
#pragma unroll
        for (int i = 0; i < 8; ++i) { oa[i] = WS_OF + ((rg * 16 + wm * 8 + i) * 32 + qg * 4 + wn) * 1024 + l * 16; PIN(oa[i]); }
        STMP(19);
        BAR();
        STMP(22);
        u4 kA[2][2], zA[2];
#define KRD(hh) do { zA[hh] = *(const u4*)(lds + L_ZX + (2 * (hh) + wn) * 64 + g * 16); \
          kA[hh][0] = *(const u4*)(lds + L_KX + (2 * (hh) + wn) * 2048 + l * 16); kA[hh][1] = *(const u4*)(lds + L_KX + (2 * (hh) + wn) * 2048 + 1024 + l * 16); SBAR(); } while (0)
        KRD(0);
        if (FI < 0) { ST128(((u4){tg[0], tg[1], tg[0], tg[1]}), ofl); SBAR(); STMP(24); }
        f4 dn[16], x0[16], x1[16];
#define OM(n) do { dn[n] = MFMA16(BC8(zA[(n) >> 3]), BC8(pf[(n) & 7][(n) >> 3]), (f4)(1e-6f)); x0[n] = MFMA16(BC8(kA[(n) >> 3][0]), BC8(pf[(n) & 7][(n) >> 3]), (f4)(0.f)); \
          x1[n] = MFMA16(BC8(kA[(n) >> 3][1]), BC8(pf[(n) & 7][(n) >> 3]), (f4)(0.f)); } while (0)
        OM(0); OM(1);
#pragma unroll
        for (int n = 0; n < 16; ++n) {
          SBAR();
          const float inv = __builtin_amdgcn_rcpf(dn[n][0]);
          asm volatile("" :: "v"(dn[n]));
          if (n + 2 < 16 && n != 6 && n != 7) OM(n + 2);
          float y[8];
#pragma unroll
          for (int e = 0; e < 4; ++e) { y[e] = x0[n][e] * inv; y[4 + e] = x1[n][e] * inv; PIN(y[e]); PIN(y[4 + e]); }
          const u4 ov = (u4){pk2(y[0], y[1]), pk2(y[2], y[3]), pk2(y[4], y[5]), pk2(y[6], y[7])};
          ST128(PO && n < 8 ? san4(ov) : ov, oa[n & 7] + (n >> 3) * 2048);
          if (n == FI) { SBAR(); if (!PO) WAITV(FI > 7 ? FI - 7 : 0); ST128(((u4){tg[0], tg[1], tg[0], tg[1]}), ofl); SBAR(); STMP(24); }
          if (n == 7) { SBAR(); BAR(); KRD(1); OM(8); OM(9); }
        }
        SBAR();
        STMP(23);
        WAITV(0);
        ST128(((u4){tg[0], tg[1], tg[0], tg[1]}), ofl + 128);
        SBAR();
        CPL(2);
        STMP(5);
      }
    }
  } else {
    LANE();
    const int jm = j - 24, jsl = (3 - (int)(((size_t)wg >> 7) & 7) - xcd) & 7;
    const bool master = jm == 7 && jsl == 7, help = jm == 7 && jsl != 7, half = jm == 7 || jm == jsl;
    const int jc = help ? jsl : jm, hc = jc * 8 + ((xcd + jc) & 7);
    const int row0 = (xcd & 1) * 1024 + (xq * 8 + jm) * 32 + w * 4;
    u4 cw[12], xr[8], wv[2];
    const u16* cp = wqkv + (size_t)(jm * 32 + w * 4 + g) * 3072 + xcd * 128 + np * 8;
#define CI(n) (((n) % 3) * 4 + (n) / 3)
#define CL(a, b) do { _Pragma("unroll") for (int n = a; n < b; ++n) cw[CI(n)] = *(const u4*)(cp + ((CI(n) & 3) * 768 + ((CI(n) >> 2) + 1) % 3) * 1024); SBAR(); } while (0)
    if (w == 0 && l < 8) ((unsigned*)(lds + L_CNT))[l] = 0u;
    CL(0, MORD);
    LBAR();
#pragma unroll
    for (int i = 0; i < 8; ++i) { const int n = (i & 3) * 2 + (i >> 2); xr[n] = *(const u4*)(x + row0 * D + n * 512 + l * 8); if (i == 3) SBAR(); }
    wv[0] = *(const u4*)(wr + l * 8); wv[1] = *(const u4*)(wr + 512 + l * 8);
    u2 ep = LD64(WS_EP);
    SBAR();
    CL(MORD, CL0);
    PIN(ep);
    const u64 tag = (((u64)RFL(ep[1]) << 32) | RFL(ep[0])) + TAG_STEP;
    const unsigned tlo = (unsigned)tag, thi = (unsigned)(tag >> 32);
    const u2 tgv = (u2){tlo, thi};
    {
      float a[13];
#pragma unroll
      for (int i = 0; i < 13; ++i) a[i] = 0.f;
      unsigned one2 = 0x3F803F80u; PIN(one2);
#pragma unroll
      for (int jj = 0; jj < 8; ++jj) {
        const unsigned wv_ = wv[jj >> 2][jj & 3];
#pragma unroll
        for (int i = 0; i < 4; ++i) {
          const unsigned xv = xr[2 * i + (jj >> 2)][jj & 3];
          a[3 * i] = DOT2(xv, one2, a[3 * i]); a[3 * i + 1] = DOT2(xv, xv, a[3 * i + 1]); a[3 * i + 2] = DOT2(xv, wv_, a[3 * i + 2]);
        }
        a[12] = DOT2(wv_, one2, a[12]);
      }
#define RSTEP(n, ctrl) _Pragma("unroll") for (int i = 0; i < n; ++i) a[i] += DPPF(a[i], ctrl, 0xf)
      RSTEP(13, 0xB1); RSTEP(13, 0x4E);
#pragma unroll
      for (int i = 0; i < 3; ++i) { const float u0 = (l & 1) ? a[3 + i] : a[i], u1 = (l & 1) ? a[9 + i] : a[6 + i]; a[i] = (l & 2) ? u1 : u0; }
      a[3] = a[12];
      RSTEP(4, 0x124); RSTEP(4, 0x128);
#pragma unroll
      for (int i = 0; i < 4; ++i) {
        auto s16 = __builtin_amdgcn_permlane16_swap(F2U(a[i]), F2U(a[i]), false, false);
        a[i] = U2F(s16[0]) + U2F(s16[1]);
        auto s32 = __builtin_amdgcn_permlane32_swap(F2U(a[i]), F2U(a[i]), false, false);
        a[i] = U2F(s32[0]) + U2F(s32[1]);
      }
      const float mf = a[0] * (1.f / D), m = rbf(mf);
      const float var = rbf(fmaxf(a[1] * (1.f / D) - mf * mf, 0.f));
      const float rs = __builtin_amdgcn_rcpf(rbf(__builtin_amdgcn_sqrtf(rbf(var + 1e-5f))));
      const float dsc = rs * (a[2] - m * a[3]);
      ST128(((u4){pk2(dsc, m), tlo, F2U(rs), thi}), l < 4 ? WS_MS + (row0 + l) * 16 : WS_OFF);
    }
    SBAR();
    STMP(18);
    CL(CL0, 12);
    u4 gw[4][8], gf[4][8], dw[2][8];
    const u16* dp = wd + (size_t)(64 * hc + 8 * g) * D + 128 * w + 8 * np;
    const u16* wp = ((np >> 3) ? wu : wg) + (size_t)(128 * w + (help ? 64 : 0) + 8 * g) * F + 64 * hc + 8 * sl;
#define GL(b, on) do { const u16* p_ = (on) ? wp + (size_t)(32 * (b)) * F : wr; const unsigned st_ = (on) ? F : 0; \
      _Pragma("unroll") for (int i_ = 0; i_ < 8; ++i_) gw[b][i_] = NTL((const u4*)(p_ + (size_t)(i_ * st_))); SBAR(); } while (0)
#define DL(b) do { const u16* p_ = jm == 7 ? wr : dp + 32 * (b) * D; const unsigned st_ = jm == 7 ? 0 : D; \
      _Pragma("unroll") for (int i_ = 0; i_ < 8; ++i_) dw[b][i_] = NTL((const u4*)(p_ + (size_t)(i_ * st_))); SBAR(); } while (0)
#define GT(b) do { _Pragma("unroll") for (int c_ = 0; c_ < 8; ++c_) { TRFRAG(gf[b][c_], gw[b], c_); PIN(gf[b][c_]); } SBAR(); } while (0)
    GL(0, !master);
    char* scr = lds + L_WO + (((lq ^ (hx >> 3)) & 2) ? 0 : 65536);
    float* red = (float*)scr;
    {
      u4 sel8;
      const unsigned one = (np & 1) ? 0x3F800000u : 0x00003F80u;
#pragma unroll
      for (int e = 0; e < 4; ++e) sel8[e] = ((np >> 1) == e) ? one : 0u;
#pragma unroll
      for (int p = 0; p < 3; ++p) {
        f4 c = (f4)(0.f);
#pragma unroll
        for (int i = 0; i < 4; ++i) c = MFMA16(BC8(sel8), BC8(cw[p * 4 + i]), c);
        if (g < 2) *(f4*)(red + (w * 3 + p) * 128 + 8 * np + 4 * g) = c;
      }
    }
    lds_arrive((LAS unsigned*)(lds + L_CNT) + 2);
    if (w < 3) {
      lds_wait<1>((LAS unsigned*)(lds + L_CNT) + 2, 8);
      if (w == 0) ST64(tgv, l == 0 ? WS_FLAG + gb * 8 : WS_OFF);
      const float* rp = red + w * 128 + g * 32 + np;
      float c0 = rp[0], c1 = rp[16];
#pragma unroll
      for (int i = 1; i < 8; ++i) { c0 += rp[i * 384]; c1 += rp[i * 384 + 16]; }
      ST128(((u4){F2U(c0), tlo, F2U(c1), thi}), WS_CS + ((jm * 96 + (w + 1) % 3 * 32 + xcd * 4) * 16 + l) * 16);
    }
    SBAR();
    STMP(17);
    if (master) {
      u4 rv[4];
      const unsigned ra = WS_MS + (w * 256 + l) * 16;
      unsigned bad = 15;
      if (MD1) PACE(MD1);
#pragma nounroll
      for (unsigned it = 0; it < SPIN_CAP; ++it) {
#pragma unroll
        for (int i = 0; i < 4; ++i) {
          const u4 v = LD128((bad >> i) & 1u ? ra + i * 1024 : WS_OFF);
          rv[i] = (bad >> i) & 1u ? v : rv[i];
        }
        bad = 0;
#pragma unroll
        for (int i = 0; i < 4; ++i) bad |= GBAD(rv[i]) ? 1u << i : 0u;
        if (all64(bad == 0u)) break;
        __builtin_amdgcn_s_sleep(4);
        CLOB();
      }
      STMP(10);
      unsigned key[4];
#pragma unroll
      for (int i = 0; i < 4; ++i) {
        const unsigned b = rv[i][0] << 16;
        key[i] = ((b ^ ((unsigned)((int)b >> 31) | 0x80000000u)) & 0xFFFF0000u) | (unsigned)(0xFFFF - (w * 256 + i * 64 + l));
      }
#define CE(i, j) do { const unsigned hi_ = max(key[i], key[j]), lo_ = min(key[i], key[j]); key[i] = hi_; key[j] = lo_; } while (0)
      CE(0, 1); CE(2, 3); CE(0, 2); CE(1, 3); CE(1, 2);
      unsigned* cand = (unsigned*)(lds + L_ROWM);
      LAS unsigned* cnt = (LAS unsigned*)(lds + L_CNT) + 1;
      unsigned cv = 0;
#pragma nounroll
      for (int pass = 0; pass < 2; ++pass) {
#pragma nounroll
        for (int it = 0; it < 8; ++it) {
          const unsigned m = wave_max(key[0]);
          const bool win = key[0] == m;
          key[0] = win ? key[1] : key[0]; key[1] = win ? key[2] : key[1]; key[2] = win ? key[3] : key[2]; key[3] = win ? 0u : key[3];
          cv = sl == it ? m : cv;
        }
        if (pass) break;
        STMP(11);
        if (l < 8) cand[w * 8 + l] = cv;
        lds_arrive(cnt);
        if (w) break;
        lds_wait<0>(cnt, 8);
        key[0] = cand[l]; key[1] = 0u;
      }
      if (w == 0) { ST64(((u2){cv, tlo}), WS_MSG + rr * 256 + 64 + sl * 8); STMP(29); lds_arrive(cnt + 2); }
      if (w != 4) lds_wait<4>(cnt + 2, 2);
    }
    if (master && w == 4) {
      PACE(MD2);
      bool ok = false;
#pragma nounroll
      for (unsigned it = 0; it < SPIN_CAP; ++it) {
        if (!ok) {
          u4 f0 = LD128(WS_FLAG + l * 32), f1 = LD128(WS_FLAG + l * 32 + 16);
          ok = f0[0] == tlo && f0[1] == thi && f0[2] == tlo && f0[3] == thi && f1[0] == tlo && f1[1] == thi && f1[2] == tlo && f1[3] == thi;
        }
        if (all64(ok)) break;
        __builtin_amdgcn_s_sleep(8);
        CLOB();
      }
      CLOB();
      if (l < 8) ST64(tgv, WS_MSG + l * 256);
      if (l == 0) ST64(tgv, WS_EP);
      STMP(30);
      lds_arrive((LAS unsigned*)(lds + L_CNT) + 3);
    }
    GL(1, !master);
    { WO_SETUP(); (void)vX; WOR(w, 32, 8); }
    GT(0);
    GL(2, !half);
    GT(1);
#define MP 0
    u2 f;
    if (MP == 0) { f = LD64(msg); SBAR(); }
    GL(3, !half);
    if (MP == 1) { f = LD64(msg); SBAR(); }
    GT(2);
    if (MP >= 1) DL(0);
    if (MP == 2) { f = LD64(msg); SBAR(); DL(1); }
    if (!KEYOK(f)) {
#pragma nounroll
      for (unsigned it = 0; it < SPIN_CAP; ++it) {
        CLOB();
        f = LD64(msg);
        if (KEYOK(f)) break;
        __builtin_amdgcn_s_sleep(16);
      }
    }
    CLOB();
    STMP(9);
    if (w == 0) {
      if (l < 8) ((unsigned*)(lds + L_TAG + 16))[l] = f[0];
      if (l == 8) *(u2*)(lds + L_TAG) = tgv;
    }
    u4 df[2][8];
    u16* hb = (u16*)(lds + L_KX);
#define DT(b) do { _Pragma("unroll") for (int c_ = 0; c_ < 8; ++c_) { TRFRAG(df[b][c_], dw[b], c_); PIN(df[b][c_]); } SBAR(); } while (0)
    if (!master) {
      const unsigned key_ = (unsigned)__builtin_amdgcn_update_dpp((int)f[0], (int)f[0], 0x128, 0xf, 0xc, false);
      const int idx = (0xFFFF - (int)(key_ & 0xFFFF)) & 2047;
      const u16* xp = x + idx * D + 128 * w + (help ? 64 : 0) + 32 * (np >> 3) + 8 * g;
      const u4 r0 = *(const u4*)xp, r1 = *(const u4*)(xp + (half ? 0 : 64));
      const u4 msx = LD128(WS_MS + idx * 16);
      SBAR();
      if (MP == 0) DL(0);
      if (MP <= 1) DL(1);
      f4 acc[4];
#pragma unroll
      for (int c2 = 0; c2 < 4; ++c2) acc[c2] = (f4)(0.f);
      const float m = hi(msx[0]), rs = U2F(msx[2]);
#pragma unroll
      for (int b2 = 0; b2 < 2; ++b2) {
        if (b2 && half) break;
        u4 xl[2], xh[2];
#pragma unroll
        for (int e = 0; e < 4; ++e) {
          const unsigned r = b2 ? r1[e] : r0[e];
          const unsigned v = pk2((lo(r) - m) * rs, (hi(r) - m) * rs);
          const unsigned o = (unsigned)__builtin_amdgcn_update_dpp(0, (int)v, 0x128, 0xf, 0xf, false);
          xl[0][e] = np < 8 ? v : 0u; xh[0][e] = np < 8 ? 0u : o; xl[1][e] = np < 8 ? o : 0u; xh[1][e] = np < 8 ? 0u : v;
        }
#pragma unroll
        for (int it = 0; it < 2; ++it)
#pragma unroll
          for (int c = 0; c < 8; ++c) {
            if (2 * b2 + it == 3) { TRFRAG(gf[3][c], gw[3], c); }
            acc[c >> 1] = MFMA16(BC8((c & 1) ? xh[it] : xl[it]), BC8(gf[2 * b2 + it][c]), acc[c >> 1]);
          }
        SBAR();
      }
      STMP(12);
      red += 3072;
#pragma unroll
      for (int c2 = 0; c2 < 4; ++c2) *(f4*)(red + (w * 4 + c2) * 256 + l * 4) = acc[c2];
      SBAR();
      DT(0);
      LBAR();
      f2 sv = *(const f2*)(red + (w >> 1) * 256 + l * 4 + (w & 1) * 2);
#pragma unroll
      for (int i = 1; i < 8; ++i) sv += *(const f2*)(red + (i * 4 + (w >> 1)) * 256 + l * 4 + (w & 1) * 2);
      const unsigned gp = WS_GP + (xcd * 512 + w * 64 + l) * 16;
      if (help) ST128(((u4){F2U(sv[0]), tlo, F2U(sv[1]), thi}), gp);
      else {
      if (half) {
        u4 pv;
#pragma nounroll
        for (unsigned it = 0; it < SPIN_CAP; ++it) {
          pv = LD128(gp);
          if (all64(GBAD(pv) == 0u)) break;
          __builtin_amdgcn_s_sleep(8);
          CLOB();
        }
        sv += (f2){U2F(pv[0]), U2F(pv[2])};
      }
#pragma unroll
      for (int e = 0; e < 2; ++e) {
        const float gg = rbf(sv[e]), u1 = DPPF(gg, 0x128, 0xf);
        const float hv = rbf(gg * __builtin_amdgcn_rcpf(1.f + __builtin_amdgcn_exp2f(gg * -1.44269504f))) * u1;
        if (np < 8) hb[(4 * (g & 1) + 2 * (w & 1) + e) * 64 + 8 * np + 2 * (w >> 1) + (g >> 1)] = (u16)(pk2(hv, hv) & 0xFFFFu);
      }
      SBAR();
      DT(1);
      }
    }
    LBAR();
    STMP(13);
    u4 hu = LD128(w == 7 ? WS_HU + xcd * 128 : WS_OFF);
    SBAR();
    { WO_SETUP(); (void)vX;
      if (w < 4) { XOWN(); WOR(32 + w, 72, 4); WAITV(10); OINIT(); } else { WOR(68 + w, 128, 4); } }
    u4 yo[4];
    if (jm < 7) {
      f4 acd[4];
#pragma unroll
      for (int c = 0; c < 4; ++c) acd[c] = (f4)(0.f);
#pragma unroll
      for (int fb = 0; fb < 2; ++fb) {
        const u4 ha = *(const u4*)(hb + sl * 64 + 32 * fb + 8 * g);
        u4 hl, hh;
#pragma unroll
        for (int e = 0; e < 4; ++e) { hl[e] = np < 8 ? ha[e] : 0u; hh[e] = np < 8 ? 0u : ha[e]; }
#pragma unroll
        for (int c = 0; c < 4; ++c) acd[c] = MFMA16(BC8(hh), BC8(df[fb][c + 4]), MFMA16(BC8(hl), BC8(df[fb][c]), acd[c]));
      }
#pragma unroll
      for (int e = 0; e < 4; ++e) { yo[e] = tag4((f4){acd[0][e], acd[1][e], acd[2][e], acd[3][e]}, tlo); PIN(yo[e]); }
    }
    __builtin_amdgcn_s_waitcnt(0x0F70); CLOB();
#pragma unroll
    for (int e = 0; e < 4; ++e) ST128(yo[e], jm < 7 ? WS_YP + (hc * 8 + 4 * (g & 1) + e) * 4096 + w * 512 + (g >> 1) * 256 + np * 16 : WS_OFF);
    STMP(14);
    STMP(21);
    if (w == 7) {
#pragma nounroll
      for (unsigned it = 0; it < SPIN_CAP && !all64(hu[0] == tlo && hu[1] == thi); ++it) {
        __builtin_amdgcn_s_sleep(8);
        CLOB();
        hu = LD128(WS_HU + xcd * 128);
      }
      if (l == 0) *(unsigned*)(lds + L_TAG + 8) = hu[2];
#pragma nounroll
      for (unsigned it = 0; PO && it < SPIN_CAP && !MSGOK(f); ++it) { f = LD64(msg); CLOB(); }
    }
    LBAR();
    tpoll = RFL(*(const unsigned*)(lds + L_TAG + 8));
    { const unsigned now = (unsigned)__builtin_amdgcn_s_memrealtime(); if (tpoll - now > PDK) tpoll = now; }
    CPE(4);
    STMP(20);
  }

  LANE();
  LAS unsigned* cnt = (LAS unsigned*)(lds + L_CNT);
  if (ow >= 4) {
#define YD 16
    lds_wait<4>(cnt + 4, 1);
    PACE(YD);
    const unsigned mykey = ((const unsigned*)(lds + L_TAG + 16))[sl], tlo = RFL(*(const unsigned*)(lds + L_TAG));
#pragma unroll 1
    for (int i = ow - 4; i < 8; i += 4) {
      const unsigned kk = (unsigned)__builtin_amdgcn_readlane((int)mykey, i);
      const int ti = 0xFFFF - (int)(kk & 0xFFFF);
      if ((ti >> 7) != mb2) continue;
      const unsigned yb = WS_YP + (g * 14 * 8 + i) * 4096 + (nb >> 1) * 512 + (np >> 3) * 256 + ((nb & 1) * 8 + sl) * 16;
      f4 y;
#pragma nounroll
      for (unsigned it = 0; it < SPIN_CAP; ++it) {
        u4 yv[14];
#pragma unroll
        for (int k = 0; k < 14; ++k) yv[k] = LD128(yb + k * 32768);
        SBAR();
        unsigned bad = 0;
        y = untag4(yv[0], tlo, bad);
#pragma unroll
        for (int k = 1; k < 14; ++k) { const f4 t = untag4(yv[k], tlo, bad); y = (f4){add1(y[0], t[0]), add1(y[1], t[1]), add1(y[2], t[2]), add1(y[3], t[3])}; }
        if (tagok(bad)) break;
        __builtin_amdgcn_s_sleep(32);
        CLOB();
      }
#pragma unroll
      for (int e = 0; e < 4; ++e) {
        auto a16 = __builtin_amdgcn_permlane16_swap(F2U(y[e]), F2U(y[e]), false, false);
        y[e] = add1(U2F(a16[0]), U2F(a16[1]));
        auto a32 = __builtin_amdgcn_permlane32_swap(F2U(y[e]), F2U(y[e]), false, false);
        y[e] = add1(U2F(a32[0]), U2F(a32[1]));
      }
      const unsigned mono = kk >> 16;
      const float rv = U2F(((mono & 0x8000u) ? (mono & 0x7FFFu) : (~mono & 0xFFFFu)) << 16);
      const float sg = rbf(__builtin_amdgcn_rcpf(1.f + __builtin_amdgcn_exp2f(rv * -1.44269504f)));
#pragma unroll
      for (int e = 0; e < 4; ++e) y[e] = rbf(mul1(rbf(y[e]), sg));
      if (g == 0) *(f4*)(lds + L_YS + i * 256 + sl * 32 + (np >> 3) * 16) = y;
    }
    lds_arrive(cnt);
    CPE(5);
    STMP(32);
    STDUMP();
    return;
  }
  const u2 tg = *(const u2*)(lds + L_TAG);
  char* xt = lds + L_XT + (ow * 32 + np) * 128 + (g & 1) * 8;
  const int trow = 0xFFFF - (int)(((const unsigned*)(lds + L_TAG + 16))[sl] & 0xFFFFu);
  const unsigned hit = (unsigned)__builtin_amdgcn_ballot_w64((trow >> 5) == mb2 * 4 + ow) & 0xFFu;
  unsigned woad[2][4];
  { const int q = np >> 2, p = l & 3, row = 4 * g + q;
#pragma unroll
    for (int ct = 0; ct < 4; ++ct) { woad[0][ct] = ldsb + L_WO + row * 128 + ((ct ^ ((row >> 1) & 3)) * 32) + p * 8; woad[1][ct] = woad[0][ct] + 65536; } }
  s4 b[2][16];
#define K3RD(hc, i) RDTR(b[(hc) & 1][i], woad[((hc) >> 2) & 1][((i) >> 1) & 3], ((((hc) & 3) * 4 + ((hc) >> 3) * 2 + ((i) >> 3)) * 4096 + ((i) & 1) * 2048))
  K3RD(0, 0); K3RD(0, 1); K3RD(0, 2); K3RD(0, 3); K3RD(0, 4); K3RD(0, 5); K3RD(0, 6); K3RD(0, 7);
  K3RD(0, 8); K3RD(0, 9); K3RD(0, 10); K3RD(0, 11); K3RD(0, 12); K3RD(0, 13); K3RD(0, 14); K3RD(0, 15);
  const unsigned flg = l < 32 ? WS_OFL + (mb2 * 32 + (np ^ (hx >> 1)) * 2 + g) * 128 : WS_OFF;
#pragma nounroll
  while ((int)((unsigned)__builtin_amdgcn_s_memrealtime() - tpoll) < 0) __builtin_amdgcn_s_sleep(1);
  STMP(26);
  unsigned rdy = 0;
#define OPOLL(MASK, SL) _Pragma("nounroll") for (unsigned it = 0; it < SPIN_CAP && (rdy & (MASK)) != (MASK); ++it) { \
    u2 f_ = LD64((((PM ? (MASK) & ~rdy : ~rdy) >> (l & 31)) & 1u) ? flg : WS_OFF); \
    rdy |= (unsigned)__builtin_amdgcn_ballot_w64(f_[0] == tg[0] && f_[1] == tg[1]); \
    if ((SL) && rdy == 0) __builtin_amdgcn_s_sleep(SL); \
    CLOB(); }
#define OG 16
  OPOLL((1u << (OG / 2)) - 1, OSL);
  CLOB();
  STMP(6);
#define OPF 3
#define OW0 6
#define OW 12
  u4 a[32]; u2 fl;
  const unsigned abase = WS_OF + (mb2 * 8 + ow * 2) * 32768 + l * 16, abase1 = abase + 32768;
#define OL1(f) a[(f) & 31] = __builtin_amdgcn_raw_buffer_load_b128(wrs, (((f) & 1) ? abase1 : abase) + (((f) >> 5) * 2 + (((f) >> 1) & 1)) * 1024, ((((f) >> 2) & 7) * 4096) ^ (hx * 1024), 16)
  unsigned mxo;
#define OMX(hc, k) mxo = mx3((k) ? mxo : 0u, a[(hc) * 4 + ((k) >> 1)][((k) & 1) * 2], a[(hc) * 4 + ((k) >> 1)][((k) & 1) * 2 + 1])
#define OFIX(hc) do { if (__builtin_expect(!clean(mxo), 0)) { NFIX(); _Pragma("nounroll") for (unsigned it = 0; it < SPIN_CAP; ++it) { CLOB(); \
      OL1((hc) * 4); OL1((hc) * 4 + 1); OL1((hc) * 4 + 2); OL1((hc) * 4 + 3); \
      if (clean(mx4(mx4(mx4(mx4(0u, a[(hc) * 4]), a[(hc) * 4 + 1]), a[(hc) * 4 + 2]), a[(hc) * 4 + 3]))) break; } } } while (0)
#define NFIX()
#define OMK(f) ((unsigned)((1ull << (((f) + OG) / 2)) - 1))
#define OLD(f) do { if ((f) < 64) { if ((f) && !((f) % OG) && (rdy & OMK(f)) != OMK(f)) { rdy |= (unsigned)__builtin_amdgcn_ballot_w64(fl[0] == tg[0] && fl[1] == tg[1]); OPOLL(OMK(f), 0); CLOB(); } \
    OL1(f); \
    if (((f) % OG) == OPF && (f) < 64 - OG) fl = LD64((((PM == 1 ? OMK((f) - OPF + OG) & ~rdy : ~rdy) >> (l & 31)) & 1u) ? flg : WS_OFF); } } while (0)
#pragma unroll
  for (int f = 0; f < OW0; ++f) { OLD(f); SBAR(); }
  if (ow == 0) { WAITL(0); lds_arrive(cnt + 4); WAITL(0); }
#define OHI(c) ((OW0 + 2 * (c)) < (OW + (c)) ? (OW0 + 2 * (c)) : (OW + (c)))
#define K3R(i) (((i) ^ ((i) >> 1)) & 1)
#define K3C(hc, i) ((hc) * 4 + ((i) >> 2))
#define K3M(hc, i) do { if (!((i) & 1)) WAITL((hc) < 15 ? 14 : 14 - (i)); SBAR(); \
    oc[K3R(i)][((i) >> 1) & 3] = MFMA16(CAT(b[(hc) & 1][(i) & ~1], b[(hc) & 1][(i) | 1]), BC8(a[((hc) * 4 + ((i) >> 3) * 2 + K3R(i)) & 31]), oc[K3R(i)][((i) >> 1) & 3]); SBAR(); \
    if ((hc) < 15) { K3RD((hc) + 1, i); } \
    if (PO && (hc) < 7 && (i) >= 6 && (i) < 14) { OMX(((hc) + 1) & 7, ((i) - 6) & 7); PIN(mxo); } \
    if (PO && (hc) < 7 && (i) == 15) { SBAR(); OFIX(((hc) + 1) & 7); } \
    if (((i) & 3) == 3) { OLD(OHI(K3C(hc, i) + 1) - 1); } else if (((i) & 3) == 1 && OHI(K3C(hc, i) + 1) - OHI(K3C(hc, i)) == 2) { OLD(OHI(K3C(hc, i))); } \
    SBAR(); } while (0)
#define K3H(hc) do { K3M(hc, 0); K3M(hc, 1); K3M(hc, 2); K3M(hc, 3); K3M(hc, 4); K3M(hc, 5); K3M(hc, 6); K3M(hc, 7); \
    K3M(hc, 8); K3M(hc, 9); K3M(hc, 10); K3M(hc, 11); K3M(hc, 12); K3M(hc, 13); K3M(hc, 14); K3M(hc, 15); } while (0)
  if (PO) { OMX(0, 0); OMX(0, 1); OMX(0, 2); OMX(0, 3); OMX(0, 4); OMX(0, 5); OMX(0, 6); OMX(0, 7); OFIX(0); }
  STMP(28);
  K3H(0); STMP(27); K3H(1); K3H(2); K3H(3); K3H(4); K3H(5); K3H(6); K3H(7); STMP(31); STMC(42); K3H(8); K3H(9); K3H(10); K3H(11); K3H(12); K3H(13); K3H(14); K3H(15);
  STMP(7); STMC(43);
  if (hit) {
    lds_wait<0>(cnt, 4);
#pragma unroll 1
    for (int i = 0; i < 8; ++i)
      if ((hit >> i) & 1u) {
        const int row = __builtin_amdgcn_readlane(trow, i) & 31;
#pragma unroll
        for (int r = 0; r < 2; ++r)
          if (row == r * 16 + np) {
#pragma unroll
            for (int ct = 0; ct < 4; ++ct) oc[r][ct] += *(const f4*)(lds + L_YS + i * 256 + ct * 64 + g * 16);
          }
      }
  }
#pragma unroll
  for (int r = 0; r < 2; ++r)
#pragma unroll
    for (int ct = 0; ct < 4; ++ct) *(u2*)XT(r, ct) = (u2){pk2(oc[r][ct][0], oc[r][ct][1]), pk2(oc[r][ct][2], oc[r][ct][3])};
  {
    const __amdgpu_buffer_rsrc_t ors = __builtin_amdgcn_make_buffer_rsrc(out, 0, S * D * 2, 0x00020000);
#pragma unroll
    for (int i = 0; i < 4; ++i) {
      const u4 v = *(const u4*)(lds + L_XT + (ow * 32 + 8 * i + rr) * 128 + ((sl ^ rr) * 16));
      __builtin_amdgcn_raw_buffer_store_b128(v, ors, (mb2 * 128 + ow * 32 + 8 * i + rr) * 2048 + nb * 128 + sl * 16, 0, 16);
    }
  }
  CPE(6);
  STMP(16);
  STDUMP();
}

extern "C" void kernel_launch(void* const* d_in, const int* in_sizes, int n_in, void* d_out, int out_size,
                              void* d_ws, size_t ws_size, hipStream_t stream) {
  const u16* x = (const u16*)d_in[0];
  const u16* wqkv = (const u16*)d_in[1];
  const u16* wo = (const u16*)d_in[2];
  const u16* wr = (const u16*)d_in[4];
  const u16* wg = (const u16*)d_in[5];
  const u16* wu = (const u16*)d_in[6];
  const u16* wd = (const u16*)d_in[7];
  (void)hipFuncSetAttribute((const void*)fused, hipFuncAttributeMaxDynamicSharedMemorySize, K_LDS);
  fused<<<256, 512, K_LDS, stream>>>(x, wqkv, wo, wr, wg, wu, wd, (char*)d_ws, (u16*)d_out);
}
```

```cpp
#include <hip/hip_runtime.h>
#include <hip/hip_bf16.h>
#include <stdint.h>

typedef unsigned short u16;
typedef unsigned long long u64;
typedef short s4 __attribute__((ext_vector_type(4)));
typedef short s8 __attribute__((ext_vector_type(8)));
typedef float f4 __attribute__((ext_vector_type(4)));
typedef float f2 __attribute__((ext_vector_type(2)));
typedef unsigned u4 __attribute__((ext_vector_type(4)));
typedef unsigned u2 __attribute__((ext_vector_type(2)));
typedef __bf16 bf2 __attribute__((ext_vector_type(2)));
#define LAS __attribute__((address_space(3)))
#define DEV __device__ __forceinline__

DEV float U2F(unsigned u) { return __builtin_bit_cast(float, u); }
DEV unsigned F2U(float f) { return __builtin_bit_cast(unsigned, f); }
DEV float I2F(int u) { return __builtin_bit_cast(float, u); }
DEV int F2I(float f) { return __builtin_bit_cast(int, f); }

constexpr int S = 2048, D = 1024, F = 3584;

constexpr unsigned WS_EP = 0;
constexpr unsigned WS_FLAG = 4096;
constexpr unsigned WS_MSG = 6144;
constexpr unsigned WS_HU = 8192;
constexpr unsigned WS_MS = 32768;
constexpr unsigned WS_CS = 256u << 10;
constexpr unsigned WS_GP = 512u << 10;
constexpr unsigned WS_YP = 1280u << 10;
constexpr unsigned WS_KVP = 3328u << 10;
constexpr unsigned WS_OF = 6u << 20;
constexpr unsigned WS_OFL = 10u << 20;
constexpr unsigned WS_END = (10u << 20) + (128u << 10);
constexpr unsigned WS_OFF = 0x7FFF0000u;
constexpr unsigned KVS = 2304;
constexpr unsigned POISON = 0xFFFFFFFFu;

DEV unsigned pk2(float a, float b) { bf2 v; v[0] = (__bf16)a; v[1] = (__bf16)b; return __builtin_bit_cast(unsigned, v); }
DEV float lo(unsigned u) { return U2F(u << 16); }
DEV float hi(unsigned u) { return U2F(u & 0xFFFF0000u); }
DEV float rbf(float a) { return lo(pk2(a, a)); }
DEV float add1(float a, float b) { float r; asm("v_add_f32 %0, %1, %2" : "=v"(r) : "v"(a), "v"(b)); return r; }
DEV float mul1(float a, float b) { float r; asm("v_mul_f32 %0, %1, %2" : "=v"(r) : "v"(a), "v"(b)); return r; }
DEV unsigned san(unsigned u) { return min(u, 0xFFFFFFFEu); }
DEV u4 san4(u4 v) { v[0] = san(v[0]); v[1] = san(v[1]); v[2] = san(v[2]); v[3] = san(v[3]); return v; }
DEV unsigned mx3(unsigned a, unsigned b, unsigned c) { return max(max(a, b), c); }
DEV unsigned mx4(unsigned m, u4 v) { return mx3(mx3(m, v[0], v[1]), v[2], v[3]); }
DEV u4 tag4(f4 v, unsigned t) { u4 r; _Pragma("unroll") for (int e = 0; e < 4; ++e) r[e] = (F2U(v[e]) & 0xFFFFFF00u) | ((t >> (8 * e)) & 0xFFu); return r; }
DEV f4 untag4(u4 v, unsigned t, unsigned& bad) {
  f4 r; _Pragma("unroll") for (int e = 0; e < 4; ++e) { const unsigned u = v[e] ^ ((t >> (8 * e)) & 0xFFu); bad |= u; r[e] = U2F(u); } return r; }
DEV bool tagok(unsigned bad) { return __builtin_amdgcn_ballot_w64((bad & 0xFFu) != 0u) == 0ull; }
DEV bool clean(unsigned m) { return __builtin_amdgcn_ballot_w64(m == POISON) == 0ull; }
DEV bool all64(bool c) { return __builtin_amdgcn_ballot_w64(!c) == 0ull; }
DEV float phi1(float a) { return __builtin_fmaf(fmaxf(a, 1.44269504e-6f), 0.693147181f, __builtin_amdgcn_fmed3f(__builtin_amdgcn_exp2f(a), 0.f, 1.f)); }
DEV unsigned phi2(f2 a) {
  const f2 e = (f2){__builtin_amdgcn_fmed3f(__builtin_amdgcn_exp2f(a[0]), 0.f, 1.f), __builtin_amdgcn_fmed3f(__builtin_amdgcn_exp2f(a[1]), 0.f, 1.f)};
  const f2 r = __builtin_elementwise_fma((f2){fmaxf(a[0], 1.44269504e-6f), fmaxf(a[1], 1.44269504e-6f)}, (f2)(0.693147181f), e);
  return pk2(r[0], r[1]);
}
#define RD128(dst, addr, OFF) asm volatile("ds_read_b128 %0, %1 offset:%2" : "=v"(dst) : "v"(addr), "i"(OFF) : "memory")
#define RD64(dst, addr, OFF) asm volatile("ds_read_b64 %0, %1 offset:%2" : "=v"(dst) : "v"(addr), "i"(OFF) : "memory")
#define RD32(dst, addr, OFF) asm volatile("ds_read_b32 %0, %1 offset:%2" : "=v"(dst) : "v"(addr), "i"(OFF) : "memory")
#define RDTR(dst, addr, OFF) asm volatile("ds_read_b64_tr_b16 %0, %1 offset:%2" : "=v"(dst) : "v"(addr), "i"(OFF) : "memory")
#define SBAR() __builtin_amdgcn_sched_barrier(0)
#define CAT(x, y) __builtin_shufflevector(x, y, 0, 1, 2, 3, 4, 5, 6, 7)
#define WAITV(n) asm volatile("s_waitcnt vmcnt(%0)" ::"i"(n) : "memory")
#define WAITL(n) asm volatile("s_waitcnt lgkmcnt(%0)" ::"i"(n) : "memory")
#define CLOB() asm volatile("" ::: "memory")
#define BAR() asm volatile("s_barrier" ::: "memory")
#define LBAR() asm volatile("s_waitcnt lgkmcnt(0)\n s_barrier" ::: "memory")
#define PIN(v) asm volatile("" : "+v"(v))
#define PACE(n) do { SBAR(); __builtin_amdgcn_s_sleep(n); SBAR(); } while (0)
#define RFL(v) ((unsigned)__builtin_amdgcn_readfirstlane(v))
DEV const char* uptr(const char* p) {
  u64 v = (u64)p;
  return (const char*)(((u64)RFL((unsigned)(v >> 32)) << 32) | RFL((unsigned)v));
}
DEV u4 mkrs(const char* p, unsigned bytes) {
  const u64 v = (u64)uptr(p);
  u4 r; r[0] = (unsigned)v; r[1] = (unsigned)(v >> 32) & 0xFFFFu; r[2] = RFL(bytes); r[3] = 0x00020000u;
  return r;
}
#define BLDS(voff, rs, so, OFF) asm volatile("buffer_load_dwordx4 %0, %1, %2 offen offset:%3 lds" ::"v"(voff), "s"(rs), "s"(so), "i"(OFF) : "memory")
#define MFMA16(a, b, c) __builtin_amdgcn_mfma_f32_16x16x32_bf16(a, b, c, 0, 0, 0)
#define MFMAH(a, b, c) __builtin_amdgcn_mfma_f32_16x16x16bf16_1k(__builtin_bit_cast(s4, (u2){(a), 0u}), __builtin_bit_cast(s4, (u2){(b), 0u}), c, 0, 0, 0)
#define DOT2(a, b, c) __builtin_amdgcn_fdot2_f32_bf16(__builtin_bit_cast(bf2, (unsigned)(a)), __builtin_bit_cast(bf2, (unsigned)(b)), c, false)
#define BC8(v) __builtin_bit_cast(s8, v)
#define LD128(off) __builtin_amdgcn_raw_buffer_load_b128(wrs, (off), 0, 16)
#define LD64(off) __builtin_amdgcn_raw_buffer_load_b64(wrs, (off), 0, 16)
#define LD32(off) __builtin_amdgcn_raw_buffer_load_b32(wrs, (off), 0, 16)
#define ST128(v, off) __builtin_amdgcn_raw_buffer_store_b128((v), wrs, (off), 0, 16)
#define ST64(v, off) __builtin_amdgcn_raw_buffer_store_b64((v), wrs, (off), 0, 16)
#define ST32(v, off) __builtin_amdgcn_raw_buffer_store_b32((v), wrs, (off), 0, 16)
#define ALD128(dst, off) asm volatile("buffer_load_dwordx4 %0, %1, %2, 0 offen sc1" : "=v"(dst) : "v"(off), "s"(rsW) : "memory")
#define ALD64(dst, off) asm volatile("buffer_load_dwordx2 %0, %1, %2, 0 offen sc1" : "=v"(dst) : "v"(off), "s"(rsW) : "memory")
#define ALD32(dst, off) asm volatile("buffer_load_dword %0, %1, %2, 0 offen sc1" : "=v"(dst) : "v"(off), "s"(rsW) : "memory")
constexpr unsigned SPIN_CAP = 1u << 20;

DEV void lds_arrive(LAS unsigned* c) {
  asm volatile("s_mov_b64 exec, 1\n ds_add_u32 %0, %1\n s_mov_b64 exec, -1" ::"v"((unsigned)(size_t)c), "v"(1u) : "memory");
}
template <int SL> DEV void lds_wait(LAS unsigned* c, unsigned target) {
#pragma nounroll
  while (RFL(__hip_atomic_load(c, __ATOMIC_RELAXED, __HIP_MEMORY_SCOPE_WORKGROUP)) < target) { if (SL) __builtin_amdgcn_s_sleep(SL); }
  CLOB();
}

#define STMP(idx)
#define STMC(idx)
#define STDUMP()
#define DPPF(v, ctrl, rm) I2F(__builtin_amdgcn_update_dpp(0, F2I(v), ctrl, rm, 0xf, false))
DEV unsigned wave_max(unsigned v) {
  v = max(v, (unsigned)__builtin_amdgcn_update_dpp(0, (int)v, 0x111, 0xf, 0xf, false));
  v = max(v, (unsigned)__builtin_amdgcn_update_dpp(0, (int)v, 0x112, 0xf, 0xf, false));
  v = max(v, (unsigned)__builtin_amdgcn_update_dpp(0, (int)v, 0x114, 0xf, 0xf, false));
  v = max(v, (unsigned)__builtin_amdgcn_update_dpp(0, (int)v, 0x118, 0xf, 0xf, false));
  v = max(v, (unsigned)__builtin_amdgcn_update_dpp(0, (int)v, 0x142, 0xa, 0xf, false));
  v = max(v, (unsigned)__builtin_amdgcn_update_dpp(0, (int)v, 0x143, 0xc, 0xf, false));
  return (unsigned)__builtin_amdgcn_readlane((int)v, 63);
}

constexpr int SLOT = 49152, L_B = 32768;
constexpr int L_EX = SLOT;
constexpr int L_ROWM = 159744;
constexpr int L_ROWS = L_ROWM + 1024;
constexpr int L_COL = L_ROWS + 1024;
constexpr int L_TAG = L_COL + 512;
constexpr int L_CNT = L_TAG + 64;
constexpr int L_TOP = L_CNT + 64;
constexpr int L_XT = 0;
constexpr int L_KX = 16384;
constexpr int L_ZX = L_KX + 8192;
constexpr int L_YS = L_ZX + 256;
constexpr int L_WO = 28672;
constexpr int K_LDS = 163840;

#define TRFRAG(f, W, c) do { _Pragma("unroll") for (int q_ = 0; q_ < 4; ++q_) { unsigned e_ = W[2 * q_][(c) >> 1], o_ = W[2 * q_ + 1][(c) >> 1]; \
    f[q_] = ((c) & 1) ? __builtin_amdgcn_perm(o_, e_, 0x07060302u) : __builtin_amdgcn_perm(o_, e_, 0x05040100u); } } while (0)
constexpr u64 TAG_STEP = 0x9E3779B97F4A7C15ull;

struct FW { s8 a[8]; s4 b[8]; };
#define RA(G, s, u, i) RD128(G.a[i], aad[((s) % 3) == 2][u], (((s) % 3) & 1) * SLOT + (i) * 2048)
#define RB(G, s, u, n) RDTR(G.b[n], bad[((s) % 3) == 2][((n) >> 1) & 1], (((s) % 3) & 1) * SLOT + ((n) >> 2) * 8192 + (u) * 4096 + ((n) & 1) * 512)
#define FB(G, j) CAT(G.b[2 * (j)], G.b[2 * (j) + 1])
#define RW(G, s, u, n) do { if ((n) < 8) RB(G, s, u, (n) & 7); else RA(G, s, u, ((n) - 8) & 7); SBAR(); } while (0)
#define SNK(n) ((((n) >> 2) & 1) ? 3 - ((n) & 3) : (n) & 3)
#define MW(G, n) do { if (TRN) c[(n) >> 2][SNK(n)] = MFMA16(FB(G, SNK(n)), G.a[(n) >> 2], c[(n) >> 2][SNK(n)]); \
    else c[(n) >> 2][SNK(n)] = MFMA16(G.a[(n) >> 2], FB(G, SNK(n)), c[(n) >> 2][SNK(n)]); SBAR(); } while (0)
#define MFA(c_, a_, b_) asm volatile("v_mfma_f32_16x16x32_bf16 %0, %1, %2, %0" : "+v"(c_) : "v"(a_), "v"(b_))
#define MA(G, n) do { if (TRN) MFA(c[(n) >> 2][SNK(n)], FB(G, SNK(n)), G.a[(n) >> 2]); else MFA(c[(n) >> 2][SNK(n)], G.a[(n) >> 2], FB(G, SNK(n))); SBAR(); } while (0)
#define MW2(Fq, G, sn, un, n) do { MA(Fq, 2 * (n)); if ((n) < 12) RW(G, sn, un, (n) + ((n) + 2) / 3); MA(Fq, 2 * (n) + 1); if ((n) < 12 && (n) % 3 == 0) RW(G, sn, un, (n) + (n) / 3 + 1); } while (0)
#define HALFW(Fq, G, sn, un) do { MW2(Fq, G, sn, un, 0); MW2(Fq, G, sn, un, 1); MW2(Fq, G, sn, un, 2); MW2(Fq, G, sn, un, 3); MW2(Fq, G, sn, un, 4); MW2(Fq, G, sn, un, 5); MW2(Fq, G, sn, un, 6); MW2(Fq, G, sn, un, 7); \
    MW2(Fq, G, sn, un, 8); MW2(Fq, G, sn, un, 9); MW2(Fq, G, sn, un, 10); MW2(Fq, G, sn, un, 11); MW2(Fq, G, sn, un, 12); MW2(Fq, G, sn, un, 13); MW2(Fq, G, sn, un, 14); MW2(Fq, G, sn, un, 15); WAITL(0); SBAR(); } while (0)
#define STAGEW(s) do { HALFW(f0, f1, s, 1); BAR(); SBAR(); HALFW(f1, f0, (s) + 1, 0); } while (0)
#define KLOOP() \
      unsigned aad[2][2], bad[2][2]; \
      { LANE(); const int q = np >> 2, p = l & 3, f = ((q >> 1) & 1) | ((g & 1) << 1); \
        _Pragma("unroll") for (int u = 0; u < 2; ++u) { aad[0][u] = ldsb + (wm * 128 + np) * 128 + (((4 * u + g) ^ (np >> 1)) * 16); aad[1][u] = aad[0][u] + 2 * SLOT; \
          bad[0][u] = ldsb + L_B + (8 * g + q) * 128 + (((2 * wn + (TRN ? u : u ^ wm)) ^ f) * 32) + p * 8; bad[1][u] = bad[0][u] + 2 * SLOT; \
          PIN(aad[0][u]); PIN(aad[1][u]); PIN(bad[0][u]); PIN(bad[1][u]); } } \
      unsigned tR, tC; \
      { LANE(); tR = ldsb + L_ROWM + (wm * 128 + (TRN ? np : 4 * g)) * 4; tC = ldsb + L_COL + (wn * 32 + (TRN ? 4 * g : wm * 16 + np)) * 4; PIN(tR); PIN(tC); SBAR(); } \
      f4 c[8][4]; \
      _Pragma("unroll") for (int i = 0; i < 8; ++i) _Pragma("unroll") for (int jj = 0; jj < 4; ++jj) { c[i][jj] = (f4)(0.f); PIN(c[i][jj]); } \
      FW f0, f1; \
      BAR(); \
      STMP(1); STMC(40); \
      RW(f0, 0, 0, 0); RW(f0, 0, 0, 1); RW(f0, 0, 0, 8); RW(f0, 0, 0, 2); RW(f0, 0, 0, 3); RW(f0, 0, 0, 4); RW(f0, 0, 0, 5); RW(f0, 0, 0, 6); RW(f0, 0, 0, 7); \
      RW(f0, 0, 0, 9); RW(f0, 0, 0, 10); RW(f0, 0, 0, 11); RW(f0, 0, 0, 12); RW(f0, 0, 0, 13); RW(f0, 0, 0, 14); RW(f0, 0, 0, 15); \
      WAITL(0); SBAR(); \
      STAGEW(0); STAGEW(1); STAGEW(2); STAGEW(3); STAGEW(4); STAGEW(5); STAGEW(6); STAGEW(7); \
      STAGEW(8); STAGEW(9); STAGEW(10); STAGEW(11); STAGEW(12); STAGEW(13); STAGEW(14); \
      HALFW(f0, f1, 15, 1); if (TRN) BAR(); SBAR()
#define MT(n) do { MW(f1, n); TB(n); } while (0)
#define KLAST() do { MT(0); MT(1); MT(2); MT(3); MT(4); MT(5); MT(6); MT(7); MT(8); MT(9); MT(10); MT(11); MT(12); MT(13); MT(14); MT(15); \
      MT(16); MT(17); MT(18); MT(19); MT(20); MT(21); MT(22); MT(23); MT(24); MT(25); MT(26); MT(27); MT(28); MT(29); MT(30); MT(31); WAITL(0); } while (0)
#define H2(v, e0) ((f2){(v)[e0], (v)[(e0) + 1]})

#define WQ0 56
#define HXM 0
#define QROT2 1
#define MORD 3
#define PO 0
#define FI (PO ? 3 : 11)
#define PM 2
#define WE 1
#define FORCE_SLOW 0
#define GP1 3
#define WM 0
#define G0A 4
#define KD 90
#define KSL 2
#define PD5 8
#define OSL 1
#define MD1 0
#define MD2 1
#define TKP 13
#define CL0 6

__global__ __launch_bounds__(512, 2) void fused(const u16* __restrict__ x, const u16* __restrict__ wqkv, const u16* __restrict__ wo, const u16* __restrict__ wr,
                                                const u16* __restrict__ wg, const u16* __restrict__ wu, const u16* __restrict__ wd, char* ws,
                                                u16* __restrict__ out) {
  extern __shared__ __attribute__((aligned(16))) char lds[];
  const int w = __builtin_amdgcn_readfirstlane(threadIdx.x >> 6);
#define LANE() int l; asm volatile("v_mbcnt_lo_u32_b32 %0, -1, 0\n v_mbcnt_hi_u32_b32 %0, -1, %0" : "=v"(l)); \
  const int np = l & 15, g = l >> 4, rr = l >> 3, sl = l & 7; (void)np; (void)g; (void)rr; (void)sl
  STMP(0);
  const int gb = blockIdx.x, xcd = gb & 7, j = gb >> 3, xq = xcd >> 1;
  const unsigned ldsb = (unsigned)(size_t)(LAS char*)lds;
  const bool isQ = j >= 16;
  const int rg = (xcd & 1) * 4 + (j & 3), hp = xq * 4 + (j >> 2), qg = xq * 2 + ((j >> 2) & 1);
  const int hd0 = isQ ? qg * 4 : 32 + hp * 2, hd1 = isQ ? 2 : 32;
  const bool isB = w == 6;
  unsigned mb_, step, d1, d2, d3, so, lim; u4 rs;
#define VSET() unsigned v[4]; { const unsigned pitch = isB ? 6144 : 2048, sm = isB ? 2 : 7; \
    _Pragma("unroll") for (int i = 0; i < 4; ++i) v[i] = (8 * i + rr) * pitch + ((sl ^ ((isB ? rr : rr >> 1) & sm) ^ ((i & 1) << 2)) * 16) - i * 1024; }
#define QL 4
#define QROT 1
#define G4(s, q, dq) do { unsigned t_; asm volatile("s_add_u32 m0, %1, %2\n s_add_u32 %0, %3, %4" : "=s"(t_) : "s"(mb_), "i"(((s) % 3) * SLOT + (q) * 4096), "s"(so), "s"(dq) : "scc", "memory"); \
        BLDS(v[0], rs, t_, 0); BLDS(v[1], rs, t_, 1024); BLDS(v[2], rs, t_, 2048); BLDS(v[3], rs, t_, 3072); } while (0)
#define PA(s) do { G4(s, 0, 0u); G4(s, 1, d1); } while (0)
#define PB(s) do { G4(s, 2, d2); G4(s, 3, d3); asm volatile("s_add_u32 %0, %0, %1\n s_cmp_eq_u32 %0, %2\n s_cselect_b32 %0, 0, %0" : "+s"(so) : "s"(step), "s"(lim) : "scc"); } while (0)
  if (__builtin_expect(j < 24 && w >= 4 && w < 7, 1)) {
    LANE();
    rs = isB ? mkrs((const char*)wqkv + hd0 * 64, 1024 * 6144 - hd0 * 64) : mkrs((const char*)x + (size_t)(rg * 256 + (w - 4) * 128) * 2048, 128 * 2048);
    mb_ = RFL(ldsb + (w - 4) * 16384); step = isB ? 64 * 6144 : 128;
    d1 = isB ? 32 * 6144 : 65536; d2 = isB ? hd1 * 64 : 131072; d3 = d1 + d2;
    so = isQ ? (QROT + QROT2 * (qg & 1)) * step : 0; lim = QL * step;
    VSET();
    STMP(37);
    PA(0); PB(0); PA(1);
    STMP(38);
  }
#define CPF 1
  u4 rsC;
  { u64 pc; unsigned len;
    asm volatile("s_getpc_b64 %0\n.Lcp0:\n s_mov_b32 %1, .Lcp1-.Lcp0\n s_max_i32 %1, %1, .Lcp2-.Lcp0\n s_max_i32 %1, %1, .Lcp3-.Lcp0\n s_max_i32 %1, %1, .Lcp4-.Lcp0\n s_max_i32 %1, %1, .Lcp5-.Lcp0\n s_max_i32 %1, %1, .Lcp6-.Lcp0\n s_max_i32 %1, %1, 0" : "=&s"(pc), "=&s"(len) :: "scc");
    rsC = (u4){(unsigned)pc & ~127u, (unsigned)(pc >> 32) & 0xFFFFu, len, 0x00020000u}; }
#define CODE(n) asm volatile("s_mov_b32 m0, %2\n s_nop 0\n buffer_load_dwordx4 %0, %1, 0 offen lds" :: "v"((n) * 1024 + l * 16), "s"(rsC), "s"(RFL(ldsb + L_ROWM + 3072)) : "memory")
  if (CPF && j < 24 && w < 4) { LANE(); CODE(j * 4 + w); }
#define CPL(n) do { __builtin_amdgcn_s_waitcnt(0x0F70); asm volatile(".Lcp" #n ":"); } while (0)
#define CPE(n) asm volatile(".Lcp" #n ":")
  const __amdgpu_buffer_rsrc_t wrs = __builtin_amdgcn_make_buffer_rsrc(ws, 0, WS_END, 0x00020000);
  const int mb2 = (xcd & 1) * 8 + (xcd >> 2) * 4 + (j >> 3), nb = ((xcd >> 1) & 1) * 8 + (j & 7), lq = (mb2 + (mb2 >> 2)) & 3, hx = (nb * 4) & HXM;
#define msg (WS_MSG + xcd * 256 + ((l & 8) ? 0 : 64 + sl * 8))
#define MSGOK(f) all64((((l & 8) ? ((f)[0] ^ tlo) | ((f)[1] ^ thi) : (f)[1] ^ tlo)) == 0u)
#define KEYOK(f) all64((l & 8) || (f)[1] == tlo)
#define GBAD(v) (((v)[1] ^ tlo) | ((v)[3] ^ thi))
#define WO_SETUP() const u4 rsO = mkrs((const char*)wo + nb * 128, 1024 * 2048 - nb * 128), rsX = mkrs((const char*)x + (size_t)mb2 * 128 * 2048 + nb * 128, 128 * 2048 - nb * 128); \
  const unsigned vW = rr * 2048 + ((sl ^ (((rr >> 1) & 3) << 1)) * 16), vX = rr * 2048 + ((sl ^ rr) * 16)
#define DMA1(v, rs, m, so) asm volatile("s_mov_b32 m0, %2\n s_nop 0\n buffer_load_dwordx4 %0, %1, %3 offen lds" ::"v"(v), "s"(rs), "s"(RFL(m)), "s"(RFL(so)) : "memory")
#define WOL(n0, n1, st) _Pragma("nounroll") for (int n_ = (n0); n_ < (n1); n_ += (st)) DMA1(vW, rsO, ldsb + L_WO + n_ * 1024, (n_ * 16384) ^ (hx * 65536))
#define WOR(n0, n1, st) _Pragma("nounroll") for (int n_ = (n0); n_ < (n1); n_ += (st)) DMA1(vW, rsO, ldsb + L_WO + (n_ ^ (lq * 32) ^ (hx * 4)) * 1024, (n_ ^ (lq * 32)) * 16384)
#define XTL(n0, st) _Pragma("nounroll") for (int n_ = (n0); n_ < 16; n_ += (st)) DMA1(vX, rsX, ldsb + L_XT + n_ * 1024, n_ * 16384)
  const int ow = isQ && j < 24 ? w ^ 4 : w;
  f4 oc[2][4];
  u4 st2[2];
  unsigned tpoll = 0;
#define TPOLL(d) tpoll = (unsigned)__builtin_amdgcn_s_memrealtime() + (d)
#define PDK 280
#define PDQ 80
#define XT(r, ct) (xt + (r) * 2048 + (((2 * (ct) + (g >> 1)) ^ sl) * 16))
#define S2OK() all64(((st2[0][1] ^ t_[0]) | (st2[0][3] ^ t_[1]) | (st2[1][1] ^ t_[0]) | (st2[1][3] ^ t_[1])) == 0u)
#define OINIT() do { PIN(st2[0]); PIN(st2[1]); const char* xt = lds + L_XT + (ow * 32 + np) * 128 + (g & 1) * 8; \
    { const u2 t_ = *(const u2*)(lds + L_TAG); if (__builtin_expect(!S2OK(), 0)) { _Pragma("nounroll") for (unsigned it = 0; it < SPIN_CAP; ++it) { CLOB(); ST2(); if (S2OK()) break; } } } \
    _Pragma("unroll") for (int r = 0; r < 2; ++r) { \
      const float c1 = 1.f + U2F(st2[r][2]), c0 = -hi(st2[r][0]) * U2F(st2[r][2]); \
      _Pragma("unroll") for (int ct = 0; ct < 4; ++ct) { const u2 v = *(const u2*)XT(r, ct); \
        oc[r][ct] = (f4){__builtin_fmaf(lo(v[0]), c1, c0), __builtin_fmaf(hi(v[0]), c1, c0), __builtin_fmaf(lo(v[1]), c1, c0), __builtin_fmaf(hi(v[1]), c1, c0)}; PIN(oc[r][ct]); } \
      } } while (0)
#define XOWN() do { WO_SETUP(); (void)vW; (void)rsO; _Pragma("unroll") for (int n_ = 0; n_ < 4; ++n_) DMA1(vX, rsX, ldsb + L_XT + (ow * 4 + n_) * 1024, (ow * 4 + n_) * 16384); \
    const u4 rsW = mkrs(ws, WS_END); const unsigned sto = WS_MS + (mb2 * 128 + (w & 3) * 32 + np) * 16, sto1 = sto + 256; ALD128(st2[0], sto); ALD128(st2[1], sto1); } while (0)
#define ST2() do { const unsigned sto = WS_MS + (mb2 * 128 + (w & 3) * 32 + np) * 16; st2[0] = LD128(sto); st2[1] = LD128(sto + 256); } while (0)

  if (j < 24) {
    if (w >= 4) {
      LANE();
      unsigned tlo = 0, thi = 0;
      WO_SETUP();
      u2 f = (u2){0u, 0u};
      if (w == 7) {
        const u4 rsW = mkrs(ws, WS_END);
        int nbar = 16;
#define TICK() do { if (nbar) { BAR(); --nbar; } } while (0)
        if (l < 16) ((unsigned*)(lds + L_CNT))[l] = 0u;
        WAITL(0);
        TICK();
        u2 ep; unsigned z0 = 0; PIN(z0);
        ALD64(ep, z0);
        { const u4 pz = (u4){POISON, POISON, POISON, POISON};
          if (isQ) {
            ST128(pz, WS_OFL + ((rg * 2 + (l >> 5)) * 32 + qg * 4) * 128 + (l & 31) * 16);
          } else {
            const unsigned kb = WS_KVP + (hp * 16 + rg) * KVS + l * 16;
            ST128(pz, kb); ST128(pz, kb + 1024); ST128(pz, kb + 8 * KVS); ST128(pz, kb + 8 * KVS + 1024);
            ST128(pz, l < 16 ? WS_KVP + (hp * 16 + rg + (l & 8)) * KVS + 2048 + sl * 16 : WS_OFF);
          } }
        SBAR();
        if (PO && isQ) {
#define PPS 8
          TICK();
#pragma nounroll
          for (int i = 0; i < 16; i += PPS) {
            TICK();
            const unsigned ob = WS_OF + ((rg * 16 + i) * 32 + qg * 4) * 1024 + l * 16; const u4 pz = (u4){POISON, POISON, POISON, POISON};
#pragma unroll
            for (int k = 0; k < PPS; ++k) { ST128(pz, ob + k * 32768); ST128(pz, ob + k * 32768 + 1024); }
          }
        } else TICK();
        TICK(); TICK();
        WAITV(0); PIN(ep);
        const u64 tag = (((u64)RFL(ep[1]) << 32) | RFL(ep[0])) + TAG_STEP;
        tlo = (unsigned)tag; thi = (unsigned)(tag >> 32);
        ST64(((u2){tlo, thi}), l == 0 ? WS_FLAG + gb * 8 : WS_OFF);
        STMP(8);
#pragma nounroll
        while (nbar > 16 - TKP) { TICK();
#define CPF2 10
          if (CPF2 && nbar == 16 - CPF2) { CODE(j * 4); CODE(j * 4 + 1); CODE(j * 4 + 2); CODE(j * 4 + 3); }
        }
        u4 ms[4], cs[8];
        const unsigned a0 = WS_MS + (rg * 256 + l) * 16, a2 = WS_CS + ((g * 96 + hd0) * 16 + np) * 16, a3 = a2 + hd1 * 256, a4 = a2 + 4 * 96 * 256, a5 = a3 + 4 * 96 * 256, a6 = msg;
#pragma nounroll
        for (unsigned it = 0; it < SPIN_CAP; ++it) {
#pragma unroll
          for (int i = 0; i < 4; ++i) asm volatile("buffer_load_dwordx4 %0, %1, %2, 0 offen offset:%3 sc1" : "=v"(ms[i]) : "v"(a0), "s"(rsW), "i"(i * 1024) : "memory");
#pragma unroll
          for (int i = 0; i < 8; ++i) asm volatile("buffer_load_dwordx4 %0, %1, %2, 0 offen offset:%3 sc1" : "=v"(cs[i]) : "v"(i < 2 ? a2 : i < 4 ? a3 : i < 6 ? a4 : a5), "s"(rsW), "i"((i & 1) * 256) : "memory");
          ALD64(f, a6);
          if (nbar > 1) TICK();
          if (nbar > 1 && it == 0) TICK();
          WAITV(0);
          unsigned bad = 0;
#pragma unroll
          for (int i = 0; i < 4; ++i) { PIN(ms[i]); PIN(cs[i]); PIN(cs[4 + i]); bad |= GBAD(ms[i]) | GBAD(cs[i]) | GBAD(cs[4 + i]); }
          PIN(f);
          if (all64(bad == 0u)) break;
        }
        STMP(9);
        float ct[2];
#pragma unroll
        for (int e = 0; e < 2; ++e) {
          float t[4];
#pragma unroll
          for (int i = 0; i < 4; ++i) t[i] = add1(U2F(cs[i][2 * e]), U2F(cs[4 + i][2 * e]));
#pragma unroll
          for (int i = 0; i < 2; ++i) { auto r = __builtin_amdgcn_permlane32_swap(F2U(t[i]), F2U(t[i + 2]), false, false); t[i] = add1(U2F(r[0]), U2F(r[1])); }
          auto r = __builtin_amdgcn_permlane16_swap(F2U(t[0]), F2U(t[1]), false, false);
          ct[e] = add1(U2F(r[0]), U2F(r[1])); PIN(ct[e]);
        }
#pragma unroll
        for (int i = 0; i < 4; ++i) { ms[i][0] = (ms[i][0] & 0xFFFF0000u) ^ 0x80000000u; ms[i][1] = F2U(mul1(U2F(ms[i][2]), 1.44269504f)); PIN(ms[i]); }
#pragma nounroll
        while (nbar) TICK();
#pragma unroll
        for (int i = 0; i < 4; ++i) { ((unsigned*)(lds + L_ROWM))[i * 64 + l] = ms[i][0]; ((unsigned*)(lds + L_ROWS))[i * 64 + l] = ms[i][1]; }
        ((float*)(lds + L_COL))[g * 32 + np] = ct[0]; ((float*)(lds + L_COL))[g * 32 + 16 + np] = ct[1];
        if (l == 8) *(u2*)(lds + L_TAG) = (u2){tlo, thi};
        if (!isQ) lds_arrive((LAS unsigned*)(lds + L_CNT) + 5);
      } else {
        VSET();
        WAITV(8); BAR(); PB(1); PA(2); WAITV(8); BAR(); STMP(44); PB(2); if (QL == 3) so = lim; PA(0); PB(0);
#define LS(s) do { if ((s) < 14) WAITV(16); else WAITV(0); BAR(); STMP(44 + (s)); if ((s) + 3 < 16) { if ((s) + 3 == QL) so = lim; PA(s); PB(s); } } while (0)
        LS(1); LS(2); LS(3); LS(4); LS(5); LS(6); LS(7); LS(8); LS(9); LS(10); LS(11); LS(12); LS(13); LS(14);
      }
#define KEYS() do { if (w == 7) { _Pragma("nounroll") for (unsigned it = 0; it < SPIN_CAP && !MSGOK(f); ++it) { f = LD64(msg); CLOB(); } \
        if (l < 8) ((unsigned*)(lds + L_TAG + 16))[l] = f[0]; \
        lds_arrive((LAS unsigned*)(lds + L_CNT) + 3); } } while (0)
      if (isQ) {
        if (w < 7) { WOL(20 + w - 4, WQ0, 3); }
        LBAR(); STMP(59);
        XOWN(); SBAR(); WOL(WQ0 + w - 4, 128, 4); WOL(w - 4, 20, 4);
        const unsigned tq = (unsigned)__builtin_amdgcn_s_memrealtime();
        WAITV(5); OINIT();
        KEYS();
        STMP(21);
        const unsigned sb = WS_KVP + (qg * 4 + (w & 1)) * 8 * KVS + ((w >> 1) & 1) * 1024 + l * 16;
        const unsigned sz = (w & 2) ? WS_OFF : WS_KVP + ((qg * 4 + (w & 1)) * 8 + (np >> 1)) * KVS + 2048 + (np & 1) * 64 + g * 16;
        u4 prm[2];
#pragma unroll
        for (int e = 0; e < 4; ++e) {
          const unsigned o = (np >> 2) == g ? ((np & 1) ? 0x3F800000u : 0x00003F80u) : 0u;
          prm[0][e] = e == (w & 2) + ((np >> 1) & 1) ? o : 0u; prm[1][e] = e == 2 - (w & 2) + ((np >> 1) & 1) ? o : 0u;
        }
        PIN(prm[0]); PIN(prm[1]);
        __builtin_amdgcn_s_setprio(2);
#pragma nounroll
        while ((unsigned)__builtin_amdgcn_s_memrealtime() - tq < KD) __builtin_amdgcn_s_sleep(1);
        lds_wait<1>((LAS unsigned*)(lds + L_CNT) + 3, 1);
        u4 v[16], zv[2]; f4 kg[4][2];
#define KVLD(gq) do { _Pragma("unroll") for (int p = 4 * (gq); p < 4 * (gq) + 4; ++p) v[p] = __builtin_amdgcn_raw_buffer_load_b128(wrs, sb, ((p & 7) + (p >> 3) * 16) * KVS, 16); } while (0)
#define ZLD(hh) zv[hh] = __builtin_amdgcn_raw_buffer_load_b128(wrs, sz, (hh) * 16 * KVS, 16)
        ZLD(0); KVLD(0); KVLD(1); SBAR(); if (GP1) PACE(GP1); ZLD(1); KVLD(2); KVLD(3); SBAR();
#pragma unroll
        for (int hh = 0; hh < 2; ++hh) {
          unsigned pend = 7;
#pragma nounroll
          for (unsigned it = 0; it < SPIN_CAP; ++it) {
#pragma unroll
            for (int gq = 2 * hh; gq < 2 * hh + 2; ++gq)
              if (pend & (1u << (gq & 1))) {
                f4 t0_ = (f4)(0.f), t1_ = (f4)(0.f); unsigned mx = 0;
#pragma unroll
                for (int p = 4 * gq; p < 4 * gq + 4; ++p) { mx = mx4(mx, v[p]); t0_ = MFMA16(BC8(prm[0]), BC8(v[p]), t0_); t1_ = MFMA16(BC8(prm[1]), BC8(v[p]), t1_); }
                kg[gq][0] = t0_; kg[gq][1] = t1_;
                if (clean(mx)) pend &= ~(1u << (gq & 1));
              }
            if ((pend & 4u) && clean(mx4(0u, zv[hh]))) pend &= ~4u;
            if (!pend) break;
            if (KSL) __builtin_amdgcn_s_sleep(KSL);
            CLOB();
            if (pend & 4u) ZLD(hh);
            if (pend & 1u) KVLD(2 * hh);
            if (pend & 2u) KVLD(2 * hh + 1);
            if (hh == 0) { ZLD(1); KVLD(2); KVLD(3); }
            SBAR();
          }
          if (hh) STMP(25); else STMP(4);
          const f4 k0 = kg[2 * hh][0] + kg[2 * hh + 1][0], k1 = kg[2 * hh][1] + kg[2 * hh + 1][1];
          *(u4*)(lds + L_KX + ((2 * hh + (w & 1)) * 2 + ((w >> 1) & 1)) * 1024 + l * 16) = (u4){pk2(k0[0], k0[1]), pk2(k0[2], k0[3]), pk2(k1[0], k1[1]), pk2(k1[2], k1[3])};
          if (!(w & 2)) {
            f4 z = __builtin_bit_cast(f4, zv[hh]);
#pragma unroll
            for (int e = 0; e < 4; ++e) { z[e] += DPPF(z[e], 0x122, 0xf); z[e] += DPPF(z[e], 0x124, 0xf); z[e] += DPPF(z[e], 0x128, 0xf); }
            if (np < 2) *(u2*)(lds + L_ZX + (2 * hh + (w & 1)) * 64 + g * 16 + np * 8) = (u2){pk2(z[0], z[1]), pk2(z[2], z[3])};
          }
          LBAR();
        }
        __builtin_amdgcn_s_setprio(0);
        CPL(3);
        TPOLL(PDQ);
      } else {
        if (w < 7) { if (WE) { WOL(32 + w, 128, 3); WAITV(0); STMP(35); } lds_arrive((LAS unsigned*)(lds + L_CNT) + 2); }
        KEYS();
        __builtin_amdgcn_s_waitcnt(0x0F70);
      }
    } else {
      const int wm = w >> 1, wn = w & 1;
      if (!isQ) {
        constexpr bool TRN = false;
        unsigned cA = ldsb + L_CNT; PIN(cA);
        unsigned exw, exa, pka, pza, cB = cA + wn * 4, one1 = 1u; bool zsel;
        { LANE(); const unsigned kb = WS_KVP + ((hp * 2 + wn) * 8 + rg) * KVS;
          exw = ldsb + L_EX + l * 16 + w * 4096; exa = exw ^ 8192; pka = kb + wm * 1024 + l * 16; pza = wm == 0 && np < 2 ? kb + 2048 + np * 64 + g * 16 : WS_OFF; zsel = (np ^ wm) & 1;
          PIN(exw); PIN(exa); PIN(pka); PIN(pza); PIN(cB); PIN(one1); SBAR(); }
        KLOOP();
        float cc[4]; f4 rsk[8], mk[8]; unsigned tf;
        unsigned tC1 = tC ^ 64; PIN(tC1);
#define TB1(n) do { if ((n) < 4) RD32(cc[(n) & 3], ((n) & 1) ? tC1 : tC, ((n) >> 1) * 256); else if ((n) & 1) RD128(rsk[(((n) - 4) >> 1) & 3], tR, 1024 + (((n) - 4) >> 1) * 64); else RD128(mk[(((n) - 4) >> 1) & 3], tR, (((n) - 4) >> 1) * 64); } while (0)
#define TB(n) do { if ((n) == 8) RD32(tf, cA, 20); else if ((n) > 8 && (n) < 21) TB1(((n) - 9) % 12); SBAR(); } while (0)
        KLAST();
#undef TB
        PIN(tf);
        if (__builtin_expect(RFL(tf) == 0u, 0)) {
          lds_wait<0>((LAS unsigned*)(lds + L_CNT) + 5, 1);
          TB1(0); TB1(1); TB1(2); TB1(3); TB1(4); TB1(5); TB1(6); TB1(7); TB1(8); TB1(9); TB1(10); TB1(11); WAITL(0);
        }
#pragma unroll
        for (int i = 0; i < 4; ++i) { PIN(mk[i]); PIN(rsk[i]); PIN(cc[i]); }
#pragma unroll
        for (int i = 4; i < 8; ++i) { RD128(mk[i], tR, i * 64); RD128(rsk[i], tR, 1024 + i * 64); }
        SBAR();
        STMP(2); STMC(41);
        f4 kv[2][2], zz[2];
        kv[0][0] = kv[0][1] = kv[1][0] = kv[1][1] = zz[0] = zz[1] = (f4)(0.f);
        const u4 ones = (u4){0x3F803F80u, 0x3F803F80u, 0x3F803F80u, 0x3F803F80u};
        u4 kf[2], vf[2];
#define LN2(i, jj, e0) (__builtin_elementwise_fma(H2(mk[i], e0), (f2)(cc[jj]), H2(c[i][jj], e0)) * H2(rsk[i], e0))
#define KVM(n) do { if ((n) < 4) kv[(n) >> 1][(n) & 1] = MFMA16(BC8(kf[(n) >> 1]), BC8(vf[(n) & 1]), kv[(n) >> 1][(n) & 1]); else zz[(n) & 1] = MFMA16(BC8(kf[(n) & 1]), BC8(ones), zz[(n) & 1]); } while (0)
#pragma unroll
        for (int ip = 0; ip < 4; ++ip) {
          if (ip == 2) { WAITL(0);
#pragma unroll
            for (int i = 4; i < 8; ++i) { PIN(mk[i]); PIN(rsk[i]); } }
          f2 ak[8], av[8], ek[8];
#pragma unroll
          for (int n = 0; n < 8; ++n) { const int i = 2 * ip + ((n >> 1) & 1), e0 = (n & 1) * 2, t = n >> 2; ak[n] = LN2(i, t, e0); av[n] = LN2(i, 2 + t, e0); }
          SBAR();
#pragma unroll
          for (int n = 0; n < 8; ++n) {
            if (ip > 0 && n < 6) KVM(n);
            ek[n] = (f2){__builtin_amdgcn_fmed3f(__builtin_amdgcn_exp2f(ak[n][0]), 0.f, 1.f), __builtin_amdgcn_fmed3f(__builtin_amdgcn_exp2f(ak[n][1]), 0.f, 1.f)};
            ak[n] = (f2){fmaxf(ak[n][0], 1.44269504e-6f), fmaxf(ak[n][1], 1.44269504e-6f)};
            SBAR();
          }
#pragma unroll
          for (int n = 0; n < 8; ++n) { const f2 r = __builtin_elementwise_fma(ak[n], (f2)(0.693147181f), ek[n]); kf[n >> 2][n & 3] = pk2(r[0], r[1]); vf[n >> 2][n & 3] = pk2(av[n][0], av[n][1]); }
          SBAR();
        }
        KVM(1); KVM(3); KVM(4); KVM(5); KVM(0); KVM(2);
        STMP(19);
        const f4 zs = zsel ? zz[1] : zz[0];
        *(LAS f4*)(size_t)(exw + 1024) = kv[0][1]; *(LAS f4*)(size_t)exw = kv[1][1]; *(LAS f4*)(size_t)(exw + 2048) = zs;
        asm volatile("s_mov_b64 exec, 1\n ds_add_u32 %0, %1 offset:24\n s_mov_b64 exec, -1" ::"v"(cB), "v"(one1) : "memory");
        f4 e0, e1, e2;
#pragma nounroll
        do {
          RD32(tf, cB, 24); RD128(e0, exa, 0); RD128(e1, exa, 1024); RD128(e2, exa, 2048);
          WAITL(0); PIN(tf); PIN(e0); PIN(e1); PIN(e2);
        } while (RFL(tf) < 2u);
        e0 += kv[0][0]; e1 += kv[1][0];
        ST128(san4((u4){pk2(e0[0], e0[1]), pk2(e0[2], e0[3]), pk2(e1[0], e1[1]), pk2(e1[2], e1[3])}), pka);
        ST128(san4(__builtin_bit_cast(u4, (zs + e2) * 1.44269504f)), pza);
        SBAR();
        STMP(3);
        LANE();
        TPOLL(PDK);
        if (j == 1 && w == 0) { const u2 t_ = *(const u2*)(lds + L_TAG); ST128(((u4){t_[0], t_[1], tpoll, 0u}), l < 8 ? WS_HU + xcd * 128 + l * 16 : WS_OFF); }
        lds_arrive((LAS unsigned*)(lds + L_CNT) + 8);
        lds_wait<1>((LAS unsigned*)(lds + L_CNT) + 8, 4);
        XOWN(); SBAR();
        { WO_SETUP(); (void)vX; (void)rsX; if (WE) { WOL(w, 36, 4); } else { WOR(w, 128, 4); } }
        WAITV(WE ? 9 : 32); OINIT();
        WAITV(0);
        lds_arrive((LAS unsigned*)(lds + L_CNT) + 2);
        lds_wait<1>((LAS unsigned*)(lds + L_CNT) + 3, 1);
        lds_wait<1>((LAS unsigned*)(lds + L_CNT) + 2, 7);
        CPL(1);
        STMP(20);
      } else {
        constexpr bool TRN = true;
        KLOOP();
        f4 cc[4]; float rs[8], mn[8];
#define TB(n) do { if ((n) < 4) RD128(cc[(n) & 3], tC, ((n) >> 1) * 256 + ((n) & 1) * 64); else if ((n) < 20) { if ((n) & 1) RD32(rs[(((n) - 4) >> 1) & 7], tR, 1024 + (((n) - 4) >> 1) * 64); else RD32(mn[(((n) - 4) >> 1) & 7], tR, (((n) - 4) >> 1) * 64); } SBAR(); } while (0)
        KLAST();
#undef TB
#pragma unroll
        for (int i = 0; i < 8; ++i) { PIN(mn[i]); PIN(rs[i]); PIN(cc[i & 3]); }
        STMP(2); STMC(41);
        LANE();
        u4 pf[8][2];
#pragma unroll
        for (int i = 0; i < 8; ++i)
#pragma unroll
          for (int hh = 0; hh < 2; ++hh) {
#pragma unroll
            for (int e = 0; e < 4; ++e) { const int jj = 2 * hh + (e >> 1), e0 = (e & 1) * 2;
              pf[i][hh][e] = phi2(__builtin_elementwise_fma(H2(cc[jj], e0), (f2)(mn[i]), H2(c[i][jj], e0)) * rs[i]); }
            PIN(pf[i][hh]);
          }
        const u2 tg = *(const u2*)(lds + L_TAG);
        const unsigned ofl = l < 8 ? WS_OFL + ((rg * 2 + wm) * 32 + (qg * 2 + wn) * 2) * 128 + sl * 16 : WS_OFF;
        unsigned oa[8];
#pragma unroll
        for (int i = 0; i < 8; ++i) { oa[i] = WS_OF + ((rg * 16 + wm * 8 + i) * 32 + qg * 4 + wn) * 1024 + l * 16; PIN(oa[i]); }
        STMP(19);
        BAR();
        STMP(22);
        u4 kA[2][2], zA[2];
#define KRD(hh) do { zA[hh] = *(const u4*)(lds + L_ZX + (2 * (hh) + wn) * 64 + g * 16); \
          kA[hh][0] = *(const u4*)(lds + L_KX + (2 * (hh) + wn) * 2048 + l * 16); kA[hh][1] = *(const u4*)(lds + L_KX + (2 * (hh) + wn) * 2048 + 1024 + l * 16); SBAR(); } while (0)
        KRD(0);
        if (FI < 0) { ST128(((u4){tg[0], tg[1], tg[0], tg[1]}), ofl); SBAR(); STMP(24); }
        f4 dn[16], x0[16], x1[16];
#define OM(n) do { dn[n] = MFMA16(BC8(zA[(n) >> 3]), BC8(pf[(n) & 7][(n) >> 3]), (f4)(1.44269504e-6f)); x0[n] = MFMA16(BC8(kA[(n) >> 3][0]), BC8(pf[(n) & 7][(n) >> 3]), (f4)(0.f)); \
          x1[n] = MFMA16(BC8(kA[(n) >> 3][1]), BC8(pf[(n) & 7][(n) >> 3]), (f4)(0.f)); } while (0)
        OM(0); OM(1);
#pragma unroll
        for (int n = 0; n < 16; ++n) {
          SBAR();
          const float inv = __builtin_amdgcn_rcpf(dn[n][0]);
          asm volatile("" :: "v"(dn[n]));
          if (n + 2 < 16 && n != 6 && n != 7) OM(n + 2);
          float y[8];
#pragma unroll
          for (int e = 0; e < 4; ++e) { y[e] = x0[n][e] * inv; y[4 + e] = x1[n][e] * inv; PIN(y[e]); PIN(y[4 + e]); }
          const u4 ov = (u4){pk2(y[0], y[1]), pk2(y[2], y[3]), pk2(y[4], y[5]), pk2(y[6], y[7])};
          ST128(PO && n < 8 ? san4(ov) : ov, oa[n & 7] + (n >> 3) * 2048);
          if (n == FI) { SBAR(); if (!PO) WAITV(FI > 7 ? FI - 7 : 0); ST128(((u4){tg[0], tg[1], tg[0], tg[1]}), ofl); SBAR(); STMP(24); }
          if (n == 7) { SBAR(); BAR(); KRD(1); OM(8); OM(9); }
        }
        SBAR();
        STMP(23);
        WAITV(0);
        ST128(((u4){tg[0], tg[1], tg[0], tg[1]}), ofl + 128);
        SBAR();
        CPL(2);
        STMP(5);
      }
    }
  } else {
    LANE();
    const int jm = j - 24, jsl = 7 - xcd, c0 = 4 - (int)(((size_t)wg >> 7) & 7) + xcd;
    const bool master = jm == 7 && jsl == 7, help = jm == 7 && jsl != 7, half = jm == 7 || jm == jsl;
    const int jc = help ? jsl : jm, hc = jc * 8 + ((c0 + jc) & 7);
    const int row0 = (xcd & 1) * 1024 + (xq * 8 + jm) * 32 + w * 4;
    u4 cw[12], xr[8], wv[2];
    const u4 rsW = mkrs(ws, WS_END), rsR = mkrs((const char*)x, S * D * 2), rsQ = mkrs((const char*)wqkv, D * 6144);
    const unsigned co = ((jm * 32 + w * 4 + g) * 3072 + xcd * 128 + np * 8) * 2;
#define CI(n) (((n) % 3) * 4 + (n) / 3)
#define CL(a, b) do { _Pragma("unroll") for (int n = a; n < b; ++n) \
      asm volatile("buffer_load_dwordx4 %0, %1, %2, %3 offen" : "=v"(cw[CI(n)]) : "v"(co), "s"(rsQ), "s"((unsigned)(((CI(n) & 3) * 768 + ((CI(n) >> 2) + 1) % 3) * 2048)) : "memory"); } while (0)
    if (w == 0 && l < 8) ((unsigned*)(lds + L_CNT))[l] = 0u;
    CL(0, MORD);
    LBAR();
    u2 ep;
    { const unsigned xo = row0 * 2048 + l * 16, l16 = l * 16; unsigned z0 = 0; PIN(z0);
#pragma unroll
      for (int i = 0; i < 8; ++i) { const int n = (i & 3) * 2 + (i >> 2);
        asm volatile("buffer_load_dwordx4 %0, %1, %2, %3 offen offset:%4" : "=v"(xr[n]) : "v"(xo), "s"(rsR), "s"((unsigned)((n >> 2) * 4096)), "i"((n & 3) * 1024) : "memory"); }
      asm volatile("global_load_dwordx4 %0, %2, %3\n global_load_dwordx4 %1, %2, %3 offset:1024" : "=&v"(wv[0]), "=&v"(wv[1]) : "v"(l16), "s"(uptr((const char*)wr)) : "memory");
      ALD64(ep, z0); }
    CL(MORD, CL0);
    WAITV(CL0 - MORD);
#pragma unroll
    for (int i = 0; i < 8; ++i) PIN(xr[i]);
    PIN(wv[0]); PIN(wv[1]); PIN(ep);
    const u64 tag = (((u64)RFL(ep[1]) << 32) | RFL(ep[0])) + TAG_STEP;
    const unsigned tlo = (unsigned)tag, thi = (unsigned)(tag >> 32);
    const u2 tgv = (u2){tlo, thi};
    {
      float a[13];
#pragma unroll
      for (int i = 0; i < 13; ++i) a[i] = 0.f;
      unsigned one2 = 0x3F803F80u; PIN(one2);
#pragma unroll
      for (int jj = 0; jj < 8; ++jj) {
        const unsigned wv_ = wv[jj >> 2][jj & 3];
#pragma unroll
        for (int i = 0; i < 4; ++i) {
          const unsigned xv = xr[2 * i + (jj >> 2)][jj & 3];
          a[3 * i] = DOT2(xv, one2, a[3 * i]); a[3 * i + 1] = DOT2(xv, xv, a[3 * i + 1]); a[3 * i + 2] = DOT2(xv, wv_, a[3 * i + 2]);
        }
        a[12] = DOT2(wv_, one2, a[12]);
      }
#define RSTEP(n, ctrl) _Pragma("unroll") for (int i = 0; i < n; ++i) a[i] += DPPF(a[i], ctrl, 0xf)
      RSTEP(13, 0xB1); RSTEP(13, 0x4E);
#pragma unroll
      for (int i = 0; i < 3; ++i) { const float u0 = (l & 1) ? a[3 + i] : a[i], u1 = (l & 1) ? a[9 + i] : a[6 + i]; a[i] = (l & 2) ? u1 : u0; }
      a[3] = a[12];
      RSTEP(4, 0x124); RSTEP(4, 0x128);
#pragma unroll
      for (int i = 0; i < 4; ++i) {
        auto s16 = __builtin_amdgcn_permlane16_swap(F2U(a[i]), F2U(a[i]), false, false);
        a[i] = U2F(s16[0]) + U2F(s16[1]);
        auto s32 = __builtin_amdgcn_permlane32_swap(F2U(a[i]), F2U(a[i]), false, false);
        a[i] = U2F(s32[0]) + U2F(s32[1]);
      }
      const float mf = a[0] * (1.f / D), m = rbf(mf);
      const float var = rbf(fmaxf(a[1] * (1.f / D) - mf * mf, 0.f));
      const float rs = __builtin_amdgcn_rcpf(rbf(__builtin_amdgcn_sqrtf(rbf(var + 1e-5f))));
      const float dsc = rs * (a[2] - m * a[3]);
      ST128(((u4){pk2(dsc, m), tlo, F2U(rs), thi}), l < 4 ? WS_MS + (row0 + l) * 16 : WS_OFF);
    }
    SBAR();
    STMP(18);
    CL(CL0, 12);
    u4 gw[4][8], gf[4][8], dw[2][8];
    const u16* wp = ((np >> 3) ? wu : wg) + (size_t)(128 * w + (help ? 64 : 0) + 8 * g) * F + 64 * hc + 8 * sl;
#define GL(b, on) GL2(b, on, 0, 8)
#define GL2(b, on, i0, i1) do { const u16* p_ = (on) ? wp + (size_t)(32 * (b)) * F : wr; const unsigned st_ = (on) ? F : 0; \
      _Pragma("unroll") for (int i_ = i0; i_ < i1; ++i_) asm volatile("global_load_dwordx4 %0, %1, off nt" : "=v"(gw[b][i_]) : "v"(p_ + (size_t)(i_ * st_)) : "memory"); } while (0)
#define GT(b, NV) do { WAITV(NV); _Pragma("unroll") for (int c_ = 0; c_ < 8; ++c_) PIN(gw[b][c_]); _Pragma("unroll") for (int c_ = 0; c_ < 8; ++c_) { TRFRAG(gf[b][c_], gw[b], c_); PIN(gf[b][c_]); } SBAR(); } while (0)
    GL2(0, !master, 0, G0A);
    WAITV(G0A);
#pragma unroll
    for (int i = 0; i < 12; ++i) PIN(cw[i]);
    char* scr = lds + L_WO + (((lq ^ (hx >> 3)) & 2) ? 0 : 65536);
    float* red = (float*)scr;
    {
      u4 sel8;
      const unsigned one = (np & 1) ? 0x3F800000u : 0x00003F80u;
#pragma unroll
      for (int e = 0; e < 4; ++e) sel8[e] = ((np >> 1) == e) ? one : 0u;
#pragma unroll
      for (int p = 0; p < 3; ++p) {
        f4 c = (f4)(0.f);
#pragma unroll
        for (int i = 0; i < 4; ++i) c = MFMA16(BC8(sel8), BC8(cw[p * 4 + i]), c);
        if (g < 2) *(f4*)(red + (w * 3 + p) * 128 + 8 * np + 4 * g) = c;
      }
    }
    lds_arrive((LAS unsigned*)(lds + L_CNT) + 2);
    if (w < 3) {
      lds_wait<1>((LAS unsigned*)(lds + L_CNT) + 2, 8);
      if (w == 0) ST64(tgv, l == 0 ? WS_FLAG + gb * 8 : WS_OFF);
      const float* rp = red + w * 128 + g * 32 + np;
      float c0 = rp[0], c1 = rp[16];
#pragma unroll
      for (int i = 1; i < 8; ++i) { c0 += rp[i * 384]; c1 += rp[i * 384 + 16]; }
      ST128(((u4){F2U(c0), tlo, F2U(c1), thi}), WS_CS + ((jm * 96 + (w + 1) % 3 * 32 + xcd * 4) * 16 + l) * 16);
    }
    SBAR();
    STMP(17);
    if (master) {
      u4 rv[4];
      const unsigned ra = WS_MS + (w * 256 + l) * 16;
      unsigned bad = 15;
      if (MD1) PACE(MD1);
#pragma nounroll
      for (unsigned it = 0; it < SPIN_CAP; ++it) {
#pragma unroll
        for (int i = 0; i < 4; ++i) {
          const u4 v = LD128((bad >> i) & 1u ? ra + i * 1024 : WS_OFF);
          rv[i] = (bad >> i) & 1u ? v : rv[i];
        }
        bad = 0;
#pragma unroll
        for (int i = 0; i < 4; ++i) bad |= GBAD(rv[i]) ? 1u << i : 0u;
        if (all64(bad == 0u)) break;
        __builtin_amdgcn_s_sleep(4);
        CLOB();
      }
      STMP(10);
      unsigned key[4];
#pragma unroll
      for (int i = 0; i < 4; ++i) {
        const unsigned b = rv[i][0] << 16;
        key[i] = ((b ^ ((unsigned)((int)b >> 31) | 0x80000000u)) & 0xFFFF0000u) | (unsigned)(0xFFFF - (w * 256 + i * 64 + l));
      }
      unsigned* cand = (unsigned*)(lds + L_ROWM);
      LAS unsigned* cnt = (LAS unsigned*)(lds + L_CNT) + 1;
      { const unsigned m = wave_max(max(mx3(key[0], key[1], key[2]), key[3]));
        cand[l < 8 ? w * 8 + l : 96 + w] = l < 8 ? 0u : m; }
      LBAR();
      { const u4 m0 = *(const u4*)(cand + 96), m1 = *(const u4*)(cand + 100);
        const unsigned T = min(min(min(m0[0], m0[1]), min(m0[2], m0[3])), min(min(m1[0], m1[1]), min(m1[2], m1[3])));
        unsigned pos[4], n = 0, base;
#pragma unroll
        for (int i = 0; i < 4; ++i) { const u64 b = __builtin_amdgcn_ballot_w64(key[i] >= T);
          pos[i] = __builtin_amdgcn_mbcnt_hi((unsigned)(b >> 32), __builtin_amdgcn_mbcnt_lo((unsigned)b, n)); n += __builtin_popcountll(b); }
        asm volatile("s_mov_b64 exec, 1\n ds_add_rtn_u32 %0, %1, %2\n s_mov_b64 exec, -1\n s_waitcnt lgkmcnt(0)" : "=v"(base) : "v"((unsigned)(size_t)cnt), "v"(n) : "memory");
        base = RFL(base);
#pragma unroll
        for (int i = 0; i < 4; ++i) cand[key[i] >= T ? min(base + pos[i], 72u) : 72u + np] = key[i]; }
      LBAR();
      STMP(11);
      if (RFL(*cnt) > 64u || FORCE_SLOW) {
#define CE(i, j) do { const unsigned hi_ = max(key[i], key[j]), lo_ = min(key[i], key[j]); key[i] = hi_; key[j] = lo_; } while (0)
        CE(0, 1); CE(2, 3); CE(0, 2); CE(1, 3); CE(1, 2);
        unsigned cv = 0;
#pragma nounroll
        for (int it = 0; it < 8; ++it) {
          const unsigned m = wave_max(key[0]);
          const bool win = key[0] == m;
          key[0] = win ? key[1] : key[0]; key[1] = win ? key[2] : key[1]; key[2] = win ? key[3] : key[2]; key[3] = win ? 0u : key[3];
          cv = sl == it ? m : cv;
        }
        if (l < 8) cand[w * 8 + l] = cv;
        LBAR();
      }
      {
        const unsigned c = cand[w * 8 + sl];
        const u4 c0 = *(const u4*)(cand + rr * 8), c1 = *(const u4*)(cand + rr * 8 + 4);
        unsigned r = 0;
#pragma unroll
        for (int e = 0; e < 4; ++e) r += (c0[e] > c ? 1u : 0u) + (c1[e] > c ? 1u : 0u);
        r += (unsigned)__builtin_amdgcn_update_dpp(0, (int)r, 0x128, 0xf, 0xf, false);
        auto a16 = __builtin_amdgcn_permlane16_swap(r, r, false, false); r = a16[0] + a16[1];
        auto a32 = __builtin_amdgcn_permlane32_swap(r, r, false, false); r = a32[0] + a32[1];
        ST64(((u2){c, tlo}), r < 8u ? WS_MSG + rr * 256 + 64 + r * 8 : WS_OFF);
        STMP(29); }
    }
    if (master && w != 4) lds_wait<4>((LAS unsigned*)(lds + L_CNT) + 3, 1);
    if (master && w == 4) {
      PACE(MD2);
      bool ok = false;
#pragma nounroll
      for (unsigned it = 0; it < SPIN_CAP; ++it) {
        if (!ok) {
          u4 f0 = LD128(WS_FLAG + l * 32), f1 = LD128(WS_FLAG + l * 32 + 16);
          ok = f0[0] == tlo && f0[1] == thi && f0[2] == tlo && f0[3] == thi && f1[0] == tlo && f1[1] == thi && f1[2] == tlo && f1[3] == thi;
        }
        if (all64(ok)) break;
        __builtin_amdgcn_s_sleep(8);
        CLOB();
      }
      CLOB();
      if (l < 8) ST64(tgv, WS_MSG + l * 256);
      if (l == 0) ST64(tgv, WS_EP);
      STMP(30);
      lds_arrive((LAS unsigned*)(lds + L_CNT) + 3);
    }
    GL2(0, !master, G0A, 8);
    GL(1, !master);
    { WO_SETUP(); (void)vX; WOR(w, 32, 8); }
    GT(0, 12);
    GL(2, !half);
    GT(1, 12);
    u2 f;
    { const unsigned m_ = msg; ALD64(f, m_); }
    GL(3, !half);
    GT(2, 9);
    WAITV(8); PIN(f);
    if (!KEYOK(f)) {
#pragma nounroll
      for (unsigned it = 0; it < SPIN_CAP; ++it) {
        CLOB();
        f = LD64(msg);
        if (KEYOK(f)) break;
        __builtin_amdgcn_s_sleep(16);
      }
    }
    CLOB();
    STMP(9);
    if (w == 0) {
      if (l < 8) ((unsigned*)(lds + L_TAG + 16))[l] = f[0];
      if (l == 8) *(u2*)(lds + L_TAG) = tgv;
    }
    u4 df[2][8];
    u16* hb = (u16*)(lds + L_KX);
#define DT(b) do { _Pragma("unroll") for (int c_ = 0; c_ < 8; ++c_) PIN(dw[b][c_]); _Pragma("unroll") for (int c_ = 0; c_ < 8; ++c_) { TRFRAG(df[b][c_], dw[b], c_); PIN(df[b][c_]); } SBAR(); } while (0)
    {
      const unsigned key_ = (unsigned)__builtin_amdgcn_update_dpp((int)f[0], (int)f[0], 0x128, 0xf, 0xc, false);
      const int idx = (0xFFFF - (int)(key_ & 0xFFFF)) & 2047;
      u4 r0, r1, msx;
      { const u4 rsD = mkrs((const char*)wd + (size_t)hc * 64 * 2048 + w * 256, jm == 7 ? 0 : 63 * 2048 + 256);
        const unsigned xo = (idx * D + 128 * w + (help ? 64 : 0) + 32 * (np >> 3) + 8 * g) * 2, mo = WS_MS + idx * 16, o_ = g * 8 * 2048 + np * 16;
        asm volatile("buffer_load_dwordx4 %0, %2, %3, 0 offen\n buffer_load_dwordx4 %1, %2, %3, 0 offen offset:128" : "=&v"(r0), "=&v"(r1) : "v"(xo), "s"(rsR) : "memory");
        ALD128(msx, mo);
#pragma unroll
        for (int i = 0; i < 16; ++i)
          asm volatile("buffer_load_dwordx4 %0, %1, %2, %3 offen offset:%4 nt" : "=v"(dw[i >> 3][i & 7]) : "v"(o_), "s"(rsD), "s"((unsigned)((i >> 3) * 65536 + (i & 6) * 2048)), "i"((i & 1) * 2048) : "memory"); }
      WAITV(16); PIN(r0); PIN(r1);
#pragma unroll
      for (int i = 0; i < 8; ++i) PIN(gw[3][i]);
      f4 acc[4];
#pragma unroll
      for (int c2 = 0; c2 < 4; ++c2) acc[c2] = (f4)(0.f);
      PIN(msx);
      const float m = hi(msx[0]), rs = U2F(msx[2]);
#pragma unroll
      for (int b2 = 0; b2 < 2; ++b2) {
        if (b2 && half) break;
        u4 xl[2], xh[2];
#pragma unroll
        for (int e = 0; e < 4; ++e) {
          const unsigned r = b2 ? r1[e] : r0[e];
          const unsigned v = pk2((lo(r) - m) * rs, (hi(r) - m) * rs);
          const unsigned o = (unsigned)__builtin_amdgcn_update_dpp(0, (int)v, 0x128, 0xf, 0xf, false);
          xl[0][e] = np < 8 ? v : 0u; xh[0][e] = np < 8 ? 0u : o; xl[1][e] = np < 8 ? o : 0u; xh[1][e] = np < 8 ? 0u : v;
        }
#pragma unroll
        for (int it = 0; it < 2; ++it)
#pragma unroll
          for (int c = 0; c < 8; ++c) {
            if (2 * b2 + it == 3) { TRFRAG(gf[3][c], gw[3], c); }
            acc[c >> 1] = MFMA16(BC8((c & 1) ? xh[it] : xl[it]), BC8(gf[2 * b2 + it][c]), acc[c >> 1]);
          }
        SBAR();
      }
      STMP(12);
      red += 3072;
#pragma unroll
      for (int c2 = 0; c2 < 4; ++c2) *(f4*)(red + (w * 4 + c2) * 256 + l * 4) = acc[c2];
      SBAR();
      if (WM) { WO_SETUP(); (void)vX; WOR(32 + w, 64, 8); }
      WAITV(WM ? 12 : 8);
      DT(0);
      LBAR();
      f2 sv = *(const f2*)(red + (w >> 1) * 256 + l * 4 + (w & 1) * 2);
#pragma unroll
      for (int i = 1; i < 8; ++i) sv += *(const f2*)(red + (i * 4 + (w >> 1)) * 256 + l * 4 + (w & 1) * 2);
      const unsigned gp = WS_GP + (xcd * 512 + w * 64 + l) * 16;
      if (jm == 7) ST128(((u4){F2U(sv[0]), tlo, F2U(sv[1]), thi}), help ? gp : WS_OFF);
      else {
      if (half) {
        u4 pv;
#pragma nounroll
        for (unsigned it = 0; it < SPIN_CAP; ++it) {
          pv = LD128(gp);
          if (all64(GBAD(pv) == 0u)) break;
          __builtin_amdgcn_s_sleep(8);
          CLOB();
        }
        sv += (f2){U2F(pv[0]), U2F(pv[2])};
      }
#pragma unroll
      for (int e = 0; e < 2; ++e) {
        const float gg = rbf(sv[e]), u1 = DPPF(gg, 0x128, 0xf);
        const float hv = rbf(gg * __builtin_amdgcn_rcpf(1.f + __builtin_amdgcn_exp2f(gg * -1.44269504f))) * u1;
        if (np < 8) hb[(4 * (g & 1) + 2 * (w & 1) + e) * 64 + 8 * np + 2 * (w >> 1) + (g >> 1)] = (u16)(pk2(hv, hv) & 0xFFFFu);
      }
      }
      SBAR();
      WAITV(WM ? 4 : 0);
      DT(1);
    }
    LBAR();
    STMP(13);
    u4 hu = LD128(w == 7 ? WS_HU + xcd * 128 : WS_OFF);
    SBAR();
    { WO_SETUP(); (void)vX;
      if (!WM) { WOR(32 + w, 64, 8); }
      if (w < 4) { XOWN(); WOR(64 + w, 88, 4); WAITV(6); OINIT(); } else { WOR(84 + w, 128, 4); } }
    u4 yo[4];
    if (jm < 7) {
      f4 acd[4];
#pragma unroll
      for (int c = 0; c < 4; ++c) acd[c] = (f4)(0.f);
#pragma unroll
      for (int fb = 0; fb < 2; ++fb) {
        const u4 ha = *(const u4*)(hb + sl * 64 + 32 * fb + 8 * g);
        u4 hl, hh;
#pragma unroll
        for (int e = 0; e < 4; ++e) { hl[e] = np < 8 ? ha[e] : 0u; hh[e] = np < 8 ? 0u : ha[e]; }
#pragma unroll
        for (int c = 0; c < 4; ++c) acd[c] = MFMA16(BC8(hh), BC8(df[fb][c + 4]), MFMA16(BC8(hl), BC8(df[fb][c]), acd[c]));
      }
#pragma unroll
      for (int e = 0; e < 4; ++e) { yo[e] = tag4((f4){acd[0][e], acd[1][e], acd[2][e], acd[3][e]}, tlo); PIN(yo[e]); }
    }
    __builtin_amdgcn_s_waitcnt(0x0F70); CLOB(); PIN(hu);
#pragma unroll
    for (int e = 0; e < 4; ++e) ST128(yo[e], jm < 7 ? WS_YP + (hc * 8 + 4 * (g & 1) + e) * 4096 + w * 512 + (g >> 1) * 256 + np * 16 : WS_OFF);
    STMP(14);
    STMP(21);
    if (w == 7) {
#pragma nounroll
      for (unsigned it = 0; it < SPIN_CAP && !all64(hu[0] == tlo && hu[1] == thi); ++it) {
        __builtin_amdgcn_s_sleep(8);
        CLOB();
        hu = LD128(WS_HU + xcd * 128);
      }
      if (l == 0) *(unsigned*)(lds + L_TAG + 8) = hu[2];
#pragma nounroll
      for (unsigned it = 0; PO && it < SPIN_CAP && !MSGOK(f); ++it) { f = LD64(msg); CLOB(); }
    }
    LBAR();
    tpoll = RFL(*(const unsigned*)(lds + L_TAG + 8));
    { const unsigned now = (unsigned)__builtin_amdgcn_s_memrealtime(); if (tpoll - now > PDK) tpoll = now; }
    CPE(4);
    STMP(20);
  }

  LANE();
  LAS unsigned* cnt = (LAS unsigned*)(lds + L_CNT);
  if (ow >= 4) {
#define YD 16
    lds_wait<4>(cnt + 4, 1);
    PACE(YD);
    const unsigned mykey = ((const unsigned*)(lds + L_TAG + 16))[sl], tlo = RFL(*(const unsigned*)(lds + L_TAG));
#pragma unroll 1
    for (int i = ow - 4; i < 8; i += 4) {
      const unsigned kk = (unsigned)__builtin_amdgcn_readlane((int)mykey, i);
      const int ti = 0xFFFF - (int)(kk & 0xFFFF);
      if ((ti >> 7) != mb2) continue;
      const unsigned yb = WS_YP + (g * 14 * 8 + i) * 4096 + (nb >> 1) * 512 + (np >> 3) * 256 + ((nb & 1) * 8 + sl) * 16;
      f4 y;
#pragma nounroll
      for (unsigned it = 0; it < SPIN_CAP; ++it) {
        u4 yv[14];
#pragma unroll
        for (int k = 0; k < 14; ++k) yv[k] = LD128(yb + k * 32768);
        SBAR();
        unsigned bad = 0;
        y = untag4(yv[0], tlo, bad);
#pragma unroll
        for (int k = 1; k < 14; ++k) { const f4 t = untag4(yv[k], tlo, bad); y = (f4){add1(y[0], t[0]), add1(y[1], t[1]), add1(y[2], t[2]), add1(y[3], t[3])}; }
        if (tagok(bad)) break;
        __builtin_amdgcn_s_sleep(32);
        CLOB();
      }
#pragma unroll
      for (int e = 0; e < 4; ++e) {
        auto a16 = __builtin_amdgcn_permlane16_swap(F2U(y[e]), F2U(y[e]), false, false);
        y[e] = add1(U2F(a16[0]), U2F(a16[1]));
        auto a32 = __builtin_amdgcn_permlane32_swap(F2U(y[e]), F2U(y[e]), false, false);
        y[e] = add1(U2F(a32[0]), U2F(a32[1]));
      }
      const unsigned mono = kk >> 16;
      const float rv = U2F(((mono & 0x8000u) ? (mono & 0x7FFFu) : (~mono & 0xFFFFu)) << 16);
      const float sg = rbf(__builtin_amdgcn_rcpf(1.f + __builtin_amdgcn_exp2f(rv * -1.44269504f)));
#pragma unroll
      for (int e = 0; e < 4; ++e) y[e] = rbf(mul1(rbf(y[e]), sg));
      if (g == 0) *(f4*)(lds + L_YS + i * 256 + sl * 32 + (np >> 3) * 16) = y;
    }
    if (ow == 4) ((unsigned*)(lds + L_YS + 2048))[l] = 0u;
    lds_arrive(cnt);
    CPE(5);
    STMP(32);
    STDUMP();
    return;
  }
  const u2 tg = *(const u2*)(lds + L_TAG);
  const int trow = 0xFFFF - (int)(((const unsigned*)(lds + L_TAG + 16))[sl] & 0xFFFFu);
  const unsigned hit = (unsigned)__builtin_amdgcn_ballot_w64((trow >> 5) == mb2 * 4 + ow) & 0xFFu;
  unsigned ya[2] = {8u, 8u}, xw[4], xr_ = ldsb + L_XT + (ow * 32 + rr) * 128 + ((sl ^ rr) * 16), oo[4], cA = ldsb + L_CNT;
  if (hit) {
#pragma unroll 1
    for (int i = 0; i < 8; ++i) { const int row = __builtin_amdgcn_readlane(trow, i) - mb2 * 128 - ow * 32 - np; ya[0] = row == 0 ? i : ya[0]; ya[1] = row == 16 ? i : ya[1]; }
  }
#pragma unroll
  for (int i = 0; i < 4; ++i) { xw[i] = ldsb + L_XT + (ow * 32 + np) * 128 + (g & 1) * 8 + (((2 * i + (g >> 1)) ^ sl) * 16);
    oo[i] = (mb2 * 128 + ow * 32 + 8 * i + rr) * 2048 + nb * 128 + sl * 16; PIN(xw[i]); PIN(oo[i]); }
  ya[0] = ldsb + L_YS + ya[0] * 256 + g * 16; ya[1] = ldsb + L_YS + ya[1] * 256 + g * 16;
  PIN(ya[0]); PIN(ya[1]); PIN(xr_); PIN(cA);
  const __amdgpu_buffer_rsrc_t ors = __builtin_amdgcn_make_buffer_rsrc(out, 0, S * D * 2, 0x00020000);
  unsigned woad[2][4];
  { const int q = np >> 2, p = l & 3, row = 4 * g + q;
#pragma unroll
    for (int ct = 0; ct < 4; ++ct) { woad[0][ct] = ldsb + L_WO + row * 128 + ((ct ^ ((row >> 1) & 3)) * 32) + p * 8; woad[1][ct] = woad[0][ct] + 65536; } }
  s4 b[2][16];
#define K3RD(hc, i) RDTR(b[(hc) & 1][i], woad[((hc) >> 2) & 1][((i) >> 1) & 3], ((((hc) & 3) * 4 + ((hc) >> 3) * 2 + ((i) >> 3)) * 4096 + ((i) & 1) * 2048))
  K3RD(0, 0); K3RD(0, 1); K3RD(0, 2); K3RD(0, 3); K3RD(0, 4); K3RD(0, 5); K3RD(0, 6); K3RD(0, 7);
  K3RD(0, 8); K3RD(0, 9); K3RD(0, 10); K3RD(0, 11); K3RD(0, 12); K3RD(0, 13); K3RD(0, 14); K3RD(0, 15);
  const unsigned flg = l < 32 ? WS_OFL + (mb2 * 32 + (np ^ (hx >> 1)) * 2 + g) * 128 : WS_OFF;
#pragma nounroll
  while ((int)((unsigned)__builtin_amdgcn_s_memrealtime() - tpoll) < 0) __builtin_amdgcn_s_sleep(1);
  STMP(26);
  unsigned rdy = 0;
#define OPOLL(MASK, SL) _Pragma("nounroll") for (unsigned it = 0; it < SPIN_CAP && (rdy & (MASK)) != (MASK); ++it) { \
    u2 f_ = LD64((((PM ? (MASK) & ~rdy : ~rdy) >> (l & 31)) & 1u) ? flg : WS_OFF); \
    rdy |= (unsigned)__builtin_amdgcn_ballot_w64(f_[0] == tg[0] && f_[1] == tg[1]); \
    if ((SL) && rdy == 0) __builtin_amdgcn_s_sleep(SL); \
    CLOB(); }
#define OG 16
  OPOLL((1u << (OG / 2)) - 1, OSL);
  CLOB();
  STMP(6);
#define OPF 3
#define OW0 6
#define OW 12
  u4 a[32]; u2 fl;
  const unsigned abase = WS_OF + (mb2 * 8 + ow * 2) * 32768 + l * 16, abase1 = abase + 32768;
#define OL1(f) a[(f) & 31] = __builtin_amdgcn_raw_buffer_load_b128(wrs, (((f) & 1) ? abase1 : abase) + (((f) >> 5) * 2 + (((f) >> 1) & 1)) * 1024, ((((f) >> 2) & 7) * 4096) ^ (hx * 1024), 16)
  unsigned mxo;
#define OMX(hc, k) mxo = mx3((k) ? mxo : 0u, a[(hc) * 4 + ((k) >> 1)][((k) & 1) * 2], a[(hc) * 4 + ((k) >> 1)][((k) & 1) * 2 + 1])
#define OFIX(hc) do { if (__builtin_expect(!clean(mxo), 0)) { NFIX(); _Pragma("nounroll") for (unsigned it = 0; it < SPIN_CAP; ++it) { CLOB(); \
      OL1((hc) * 4); OL1((hc) * 4 + 1); OL1((hc) * 4 + 2); OL1((hc) * 4 + 3); \
      if (clean(mx4(mx4(mx4(mx4(0u, a[(hc) * 4]), a[(hc) * 4 + 1]), a[(hc) * 4 + 2]), a[(hc) * 4 + 3]))) break; } } } while (0)
#define NFIX()
#define OMK(f) ((unsigned)((1ull << (((f) + OG) / 2)) - 1))
#define OLD(f) do { if ((f) < 64) { if ((f) && !((f) % OG) && (rdy & OMK(f)) != OMK(f)) { rdy |= (unsigned)__builtin_amdgcn_ballot_w64(fl[0] == tg[0] && fl[1] == tg[1]); OPOLL(OMK(f), 0); CLOB(); } \
    OL1(f); \
    if (((f) % OG) == OPF && (f) < 64 - OG) fl = LD64((((PM == 1 ? OMK((f) - OPF + OG) & ~rdy : ~rdy) >> (l & 31)) & 1u) ? flg : WS_OFF); } } while (0)
#pragma unroll
  for (int f = 0; f < OW0; ++f) { OLD(f); SBAR(); }
  if (ow == 0) { WAITL(0); lds_arrive(cnt + 4); WAITL(0); }
#define OHI(c) ((OW0 + 2 * (c)) < (OW + (c)) ? (OW0 + 2 * (c)) : (OW + (c)))
#define K3R(i) (((i) ^ ((i) >> 1)) & 1)
#define K3C(hc, i) ((hc) * 4 + ((i) >> 2))
#define K3M(hc, i) do { if (!((i) & 1)) WAITL((hc) < 15 ? 14 : 14 - (i)); SBAR(); \
    oc[K3R(i)][((i) >> 1) & 3] = MFMA16(CAT(b[(hc) & 1][(i) & ~1], b[(hc) & 1][(i) | 1]), BC8(a[((hc) * 4 + ((i) >> 3) * 2 + K3R(i)) & 31]), oc[K3R(i)][((i) >> 1) & 3]); SBAR(); \
    if ((hc) < 15) { K3RD((hc) + 1, i); } \
    if (PO && (hc) < 7 && (i) >= 6 && (i) < 14) { OMX(((hc) + 1) & 7, ((i) - 6) & 7); PIN(mxo); } \
    if (PO && (hc) < 7 && (i) == 15) { SBAR(); OFIX(((hc) + 1) & 7); } \
    if (((i) & 3) == 3) { OLD(OHI(K3C(hc, i) + 1) - 1); } else if (((i) & 3) == 1 && OHI(K3C(hc, i) + 1) - OHI(K3C(hc, i)) == 2) { OLD(OHI(K3C(hc, i))); } \
    SBAR(); } while (0)
#define K3H(hc) do { K3M(hc, 0); K3M(hc, 1); K3M(hc, 2); K3M(hc, 3); K3M(hc, 4); K3M(hc, 5); K3M(hc, 6); K3M(hc, 7); \
    K3M(hc, 8); K3M(hc, 9); K3M(hc, 10); K3M(hc, 11); K3M(hc, 12); K3M(hc, 13); K3M(hc, 14); K3M(hc, 15); } while (0)
  if (PO) { OMX(0, 0); OMX(0, 1); OMX(0, 2); OMX(0, 3); OMX(0, 4); OMX(0, 5); OMX(0, 6); OMX(0, 7); OFIX(0); }
  STMP(28);
  K3H(0); STMP(27); K3H(1); K3H(2); K3H(3); K3H(4); K3H(5); K3H(6); K3H(7); STMP(31); STMC(42); K3H(8); K3H(9); K3H(10); K3H(11); K3H(12); K3H(13); K3H(14);
  f4 yv[2][4]; unsigned yf = 4u;
#define K3L(r, n) do { oc[r][(n) & 3] = MFMA16(CAT(b[1][2 * (n)], b[1][2 * (n) + 1]), BC8(a[28 + ((n) >> 2) * 2 + (r)]), oc[r][(n) & 3]); SBAR(); } while (0)
#define YRD() do { _Pragma("unroll") for (int i = 0; i < 8; ++i) RD128(yv[i >> 2][i & 3], ya[i >> 2], (i & 3) * 64); } while (0)
#define OWR(r, ct) do { const u2 v_ = (u2){pk2(oc[r][ct][0], oc[r][ct][1]), pk2(oc[r][ct][2], oc[r][ct][3])}; \
    asm volatile("ds_write_b64 %0, %1 offset:%2" :: "v"(xw[ct]), "v"(v_), "i"((r) * 2048) : "memory"); SBAR(); } while (0)
  u4 ov[4];
  WAITL(8); SBAR(); K3L(0, 0); K3L(0, 1); K3L(0, 2); K3L(0, 3); WAITL(0);
  if (hit) { RD32(yf, cA, 0); YRD(); }
  SBAR();
  K3L(0, 4); K3L(0, 5); K3L(0, 6); K3L(0, 7); K3L(1, 0); K3L(1, 1); K3L(1, 2); K3L(1, 3);
  if (hit) {
    K3L(1, 4); K3L(1, 5); K3L(1, 6); K3L(1, 7);
    WAITL(0); PIN(yf);
    if (RFL(yf) < 4u) { lds_wait<0>(cnt, 4); YRD(); WAITL(0); }
#pragma unroll
    for (int i = 0; i < 8; ++i) { PIN(yv[i >> 2][i & 3]); oc[i >> 2][i & 3] += yv[i >> 2][i & 3]; }
    OWR(0, 0); OWR(0, 1); OWR(0, 2); OWR(0, 3);
  } else {
    K3L(1, 4); OWR(0, 0); K3L(1, 5); OWR(0, 1); K3L(1, 6); OWR(0, 2); K3L(1, 7); OWR(0, 3);
  }
  STMP(7); STMC(43);
  RD128(ov[0], xr_, 0); RD128(ov[1], xr_, 1024);
  OWR(1, 0); OWR(1, 1); OWR(1, 2); OWR(1, 3);
  RD128(ov[2], xr_, 2048); RD128(ov[3], xr_, 3072);
  WAITL(6); PIN(ov[0]); PIN(ov[1]);
  __builtin_amdgcn_raw_buffer_store_b128(ov[0], ors, oo[0], 0, 16); __builtin_amdgcn_raw_buffer_store_b128(ov[1], ors, oo[1], 0, 16);
  SBAR();
  WAITL(0); PIN(ov[2]); PIN(ov[3]);
  __builtin_amdgcn_raw_buffer_store_b128(ov[2], ors, oo[2], 0, 16); __builtin_amdgcn_raw_buffer_store_b128(ov[3], ors, oo[3], 0, 16);
  CPE(6);
  STMP(16);
  STDUMP();
}

extern "C" void kernel_launch(void* const* d_in, const int* in_sizes, int n_in, void* d_out, int out_size,
                              void* d_ws, size_t ws_size, hipStream_t stream) {
  const u16* x = (const u16*)d_in[0];
  const u16* wqkv = (const u16*)d_in[1];
  const u16* wo = (const u16*)d_in[2];
  const u16* wr = (const u16*)d_in[4];
  const u16* wg = (const u16*)d_in[5];
  const u16* wu = (const u16*)d_in[6];
  const u16* wd = (const u16*)d_in[7];
  (void)hipFuncSetAttribute((const void*)fused, hipFuncAttributeMaxDynamicSharedMemorySize, K_LDS);
  fused<<<256, 512, K_LDS, stream>>>(x, wqkv, wo, wr, wg, wu, wd, (char*)d_ws, (u16*)d_out);
}
```

```cpp
#include <hip/hip_runtime.h>
#include <hip/hip_bf16.h>
#include <stdint.h>

typedef unsigned short u16;
typedef unsigned long long u64;
typedef short s4 __attribute__((ext_vector_type(4)));
typedef short s8 __attribute__((ext_vector_type(8)));
typedef float f4 __attribute__((ext_vector_type(4)));
typedef float f2 __attribute__((ext_vector_type(2)));
typedef unsigned u4 __attribute__((ext_vector_type(4)));
typedef unsigned u2 __attribute__((ext_vector_type(2)));
typedef __bf16 bf2 __attribute__((ext_vector_type(2)));
#define LAS __attribute__((address_space(3)))
#define DEV __device__ __forceinline__

DEV float U2F(unsigned u) { return __builtin_bit_cast(float, u); }
DEV unsigned F2U(float f) { return __builtin_bit_cast(unsigned, f); }
DEV float I2F(int u) { return __builtin_bit_cast(float, u); }
DEV int F2I(float f) { return __builtin_bit_cast(int, f); }

constexpr int S = 2048, D = 1024, F = 3584;

constexpr unsigned WS_EP = 0;
constexpr unsigned WS_FLAG = 4096;
constexpr unsigned WS_MSG = 6144;
constexpr unsigned WS_HU = 8192;
constexpr unsigned WS_ETA = 12288;
constexpr unsigned WS_MS = 32768;
constexpr unsigned WS_CS = 256u << 10;
constexpr unsigned WS_GP = 512u << 10;
constexpr unsigned WS_YP = 1280u << 10;
constexpr unsigned WS_KVP = 3328u << 10;
constexpr unsigned WS_OF = 6u << 20;
constexpr unsigned WS_OFL = 10u << 20;
constexpr unsigned WS_END = (10u << 20) + (128u << 10);
constexpr unsigned WS_OFF = 0x7FFF0000u;
constexpr unsigned KVS = 2304;
constexpr unsigned POISON = 0xFFFFFFFFu;

DEV unsigned pk2(float a, float b) { bf2 v; v[0] = (__bf16)a; v[1] = (__bf16)b; return __builtin_bit_cast(unsigned, v); }
DEV float lo(unsigned u) { return U2F(u << 16); }
DEV float hi(unsigned u) { return U2F(u & 0xFFFF0000u); }
DEV float rbf(float a) { return lo(pk2(a, a)); }
DEV float add1(float a, float b) { float r; asm("v_add_f32 %0, %1, %2" : "=v"(r) : "v"(a), "v"(b)); return r; }
DEV float mul1(float a, float b) { float r; asm("v_mul_f32 %0, %1, %2" : "=v"(r) : "v"(a), "v"(b)); return r; }
DEV unsigned san(unsigned u) { return min(u, 0xFFFFFFFEu); }
DEV u4 san4(u4 v) { v[0] = san(v[0]); v[1] = san(v[1]); v[2] = san(v[2]); v[3] = san(v[3]); return v; }
DEV unsigned mx3(unsigned a, unsigned b, unsigned c) { return max(max(a, b), c); }
DEV unsigned mx4(unsigned m, u4 v) { return mx3(mx3(m, v[0], v[1]), v[2], v[3]); }
DEV u4 tag4(f4 v, unsigned t) { u4 r; _Pragma("unroll") for (int e = 0; e < 4; ++e) r[e] = (F2U(v[e]) & 0xFFFFFF00u) | ((t >> (8 * e)) & 0xFFu); return r; }
DEV f4 untag4(u4 v, unsigned t, unsigned& bad) {
  f4 r; _Pragma("unroll") for (int e = 0; e < 4; ++e) { const unsigned u = v[e] ^ ((t >> (8 * e)) & 0xFFu); bad |= u; r[e] = U2F(u); } return r; }
DEV bool tagok(unsigned bad) { return __builtin_amdgcn_ballot_w64((bad & 0xFFu) != 0u) == 0ull; }
DEV bool clean(unsigned m) { return __builtin_amdgcn_ballot_w64(m == POISON) == 0ull; }
DEV bool all64(bool c) { return __builtin_amdgcn_ballot_w64(!c) == 0ull; }
DEV float phi1(float a) { return __builtin_fmaf(fmaxf(a, 1.44269504e-6f), 0.693147181f, __builtin_amdgcn_fmed3f(__builtin_amdgcn_exp2f(a), 0.f, 1.f)); }
DEV unsigned phi2(f2 a) {
  const f2 e = (f2){__builtin_amdgcn_fmed3f(__builtin_amdgcn_exp2f(a[0]), 0.f, 1.f), __builtin_amdgcn_fmed3f(__builtin_amdgcn_exp2f(a[1]), 0.f, 1.f)};
  const f2 r = __builtin_elementwise_fma((f2){fmaxf(a[0], 1.44269504e-6f), fmaxf(a[1], 1.44269504e-6f)}, (f2)(0.693147181f), e);
  return pk2(r[0], r[1]);
}
#define RD128(dst, addr, OFF) asm volatile("ds_read_b128 %0, %1 offset:%2" : "=v"(dst) : "v"(addr), "i"(OFF) : "memory")
#define RD64(dst, addr, OFF) asm volatile("ds_read_b64 %0, %1 offset:%2" : "=v"(dst) : "v"(addr), "i"(OFF) : "memory")
#define RD32(dst, addr, OFF) asm volatile("ds_read_b32 %0, %1 offset:%2" : "=v"(dst) : "v"(addr), "i"(OFF) : "memory")
#define RDTR(dst, addr, OFF) asm volatile("ds_read_b64_tr_b16 %0, %1 offset:%2" : "=v"(dst) : "v"(addr), "i"(OFF) : "memory")
#define SBAR() __builtin_amdgcn_sched_barrier(0)
#define CAT(x, y) __builtin_shufflevector(x, y, 0, 1, 2, 3, 4, 5, 6, 7)
#define WAITV(n) asm volatile("s_waitcnt vmcnt(%0)" ::"i"(n) : "memory")
#define WAITL(n) asm volatile("s_waitcnt lgkmcnt(%0)" ::"i"(n) : "memory")
#define CLOB() asm volatile("" ::: "memory")
#define BAR() asm volatile("s_barrier" ::: "memory")
#define LBAR() asm volatile("s_waitcnt lgkmcnt(0)\n s_barrier" ::: "memory")
#define PIN(v) asm volatile("" : "+v"(v))
#define PACE(n) do { SBAR(); __builtin_amdgcn_s_sleep(n); SBAR(); } while (0)
#define RFL(v) ((unsigned)__builtin_amdgcn_readfirstlane(v))
DEV const char* uptr(const char* p) {
  u64 v = (u64)p;
  return (const char*)(((u64)RFL((unsigned)(v >> 32)) << 32) | RFL((unsigned)v));
}
DEV u4 mkrs(const char* p, unsigned bytes) {
  const u64 v = (u64)uptr(p);
  u4 r; r[0] = (unsigned)v; r[1] = (unsigned)(v >> 32) & 0xFFFFu; r[2] = RFL(bytes); r[3] = 0x00020000u;
  return r;
}
#define BLDS(voff, rs, so, OFF) asm volatile("buffer_load_dwordx4 %0, %1, %2 offen offset:%3 lds" ::"v"(voff), "s"(rs), "s"(so), "i"(OFF) : "memory")
#define MFMA16(a, b, c) __builtin_amdgcn_mfma_f32_16x16x32_bf16(a, b, c, 0, 0, 0)
#define MFMAH(a, b, c) __builtin_amdgcn_mfma_f32_16x16x16bf16_1k(__builtin_bit_cast(s4, (u2){(a), 0u}), __builtin_bit_cast(s4, (u2){(b), 0u}), c, 0, 0, 0)
#define DOT2(a, b, c) __builtin_amdgcn_fdot2_f32_bf16(__builtin_bit_cast(bf2, (unsigned)(a)), __builtin_bit_cast(bf2, (unsigned)(b)), c, false)
#define BC8(v) __builtin_bit_cast(s8, v)
#define LD128(off) __builtin_amdgcn_raw_buffer_load_b128(wrs, (off), 0, 16)
#define LD64(off) __builtin_amdgcn_raw_buffer_load_b64(wrs, (off), 0, 16)
#define LD32(off) __builtin_amdgcn_raw_buffer_load_b32(wrs, (off), 0, 16)
#define ST128(v, off) __builtin_amdgcn_raw_buffer_store_b128((v), wrs, (off), 0, 16)
#define ST64(v, off) __builtin_amdgcn_raw_buffer_store_b64((v), wrs, (off), 0, 16)
#define ST32(v, off) __builtin_amdgcn_raw_buffer_store_b32((v), wrs, (off), 0, 16)
#define ALD128(dst, off) asm volatile("buffer_load_dwordx4 %0, %1, %2, 0 offen sc1" : "=v"(dst) : "v"(off), "s"(rsW) : "memory")
#define ALD64(dst, off) asm volatile("buffer_load_dwordx2 %0, %1, %2, 0 offen sc1" : "=v"(dst) : "v"(off), "s"(rsW) : "memory")
#define ALD32(dst, off) asm volatile("buffer_load_dword %0, %1, %2, 0 offen sc1" : "=v"(dst) : "v"(off), "s"(rsW) : "memory")
constexpr unsigned SPIN_CAP = 1u << 20;

DEV void lds_arrive(LAS unsigned* c) {
  asm volatile("s_mov_b64 exec, 1\n ds_add_u32 %0, %1\n s_mov_b64 exec, -1" ::"v"((unsigned)(size_t)c), "v"(1u) : "memory");
}
template <int SL> DEV void lds_wait(LAS unsigned* c, unsigned target) {
#pragma nounroll
  while (RFL(__hip_atomic_load(c, __ATOMIC_RELAXED, __HIP_MEMORY_SCOPE_WORKGROUP)) < target) { if (SL) __builtin_amdgcn_s_sleep(SL); }
  CLOB();
}

#define STMP(idx)
#define STMC(idx)
#define STDUMP()
#define DPPF(v, ctrl, rm) I2F(__builtin_amdgcn_update_dpp(0, F2I(v), ctrl, rm, 0xf, false))
DEV unsigned wave_max(unsigned v) {
  v = max(v, (unsigned)__builtin_amdgcn_update_dpp(0, (int)v, 0x111, 0xf, 0xf, false));
  v = max(v, (unsigned)__builtin_amdgcn_update_dpp(0, (int)v, 0x112, 0xf, 0xf, false));
  v = max(v, (unsigned)__builtin_amdgcn_update_dpp(0, (int)v, 0x114, 0xf, 0xf, false));
  v = max(v, (unsigned)__builtin_amdgcn_update_dpp(0, (int)v, 0x118, 0xf, 0xf, false));
  v = max(v, (unsigned)__builtin_amdgcn_update_dpp(0, (int)v, 0x142, 0xa, 0xf, false));
  v = max(v, (unsigned)__builtin_amdgcn_update_dpp(0, (int)v, 0x143, 0xc, 0xf, false));
  return (unsigned)__builtin_amdgcn_readlane((int)v, 63);
}

constexpr int SLOT = 49152, L_B = 32768;
constexpr int L_EX = SLOT;
constexpr int L_ROWM = 159744;
constexpr int L_ROWS = L_ROWM + 1024;
constexpr int L_COL = L_ROWS + 1024;
constexpr int L_TAG = L_COL + 512;
constexpr int L_CNT = L_TAG + 64;
constexpr int L_TOP = L_CNT + 64;
constexpr int L_XT = 0;
constexpr int L_KX = 16384;
constexpr int L_ZX = L_KX + 8192;
constexpr int L_YS = L_ZX + 256;
constexpr int L_WO = 28672;
constexpr int K_LDS = 163840;

#define TRFRAG(f, W, c) do { _Pragma("unroll") for (int q_ = 0; q_ < 4; ++q_) { unsigned e_ = W[2 * q_][(c) >> 1], o_ = W[2 * q_ + 1][(c) >> 1]; \
    f[q_] = ((c) & 1) ? __builtin_amdgcn_perm(o_, e_, 0x07060302u) : __builtin_amdgcn_perm(o_, e_, 0x05040100u); } } while (0)
constexpr u64 TAG_STEP = 0x9E3779B97F4A7C15ull;

struct FW { s8 a[8]; s4 b[8]; };
#define RA(G, s, u, i) RD128(G.a[i], aad[((s) % 3) == 2][u], (((s) % 3) & 1) * SLOT + (i) * 2048)
#define RB(G, s, u, n) RDTR(G.b[n], bad[((s) % 3) == 2][((n) >> 1) & 1], (((s) % 3) & 1) * SLOT + ((n) >> 2) * 8192 + (u) * 4096 + ((n) & 1) * 512)
#define FB(G, j) CAT(G.b[2 * (j)], G.b[2 * (j) + 1])
#define RW(G, s, u, n) do { if ((n) < 8) RB(G, s, u, (n) & 7); else RA(G, s, u, ((n) - 8) & 7); SBAR(); } while (0)
#define SNK(n) ((((n) >> 2) & 1) ? 3 - ((n) & 3) : (n) & 3)
#define MW(G, n) do { if (TRN) c[(n) >> 2][SNK(n)] = MFMA16(FB(G, SNK(n)), G.a[(n) >> 2], c[(n) >> 2][SNK(n)]); \
    else c[(n) >> 2][SNK(n)] = MFMA16(G.a[(n) >> 2], FB(G, SNK(n)), c[(n) >> 2][SNK(n)]); SBAR(); } while (0)
#define MFA(c_, a_, b_) asm volatile("v_mfma_f32_16x16x32_bf16 %0, %1, %2, %0" : "+v"(c_) : "v"(a_), "v"(b_))
#define MA(G, n) do { if (TRN) MFA(c[(n) >> 2][SNK(n)], FB(G, SNK(n)), G.a[(n) >> 2]); else MFA(c[(n) >> 2][SNK(n)], G.a[(n) >> 2], FB(G, SNK(n))); SBAR(); } while (0)
#define MW2(Fq, G, sn, un, n) do { MA(Fq, 2 * (n)); if ((n) < 12) RW(G, sn, un, (n) + ((n) + 2) / 3); MA(Fq, 2 * (n) + 1); if ((n) < 12 && (n) % 3 == 0) RW(G, sn, un, (n) + (n) / 3 + 1); } while (0)
#define HALFW(Fq, G, sn, un) do { MW2(Fq, G, sn, un, 0); MW2(Fq, G, sn, un, 1); MW2(Fq, G, sn, un, 2); MW2(Fq, G, sn, un, 3); MW2(Fq, G, sn, un, 4); MW2(Fq, G, sn, un, 5); MW2(Fq, G, sn, un, 6); MW2(Fq, G, sn, un, 7); \
    MW2(Fq, G, sn, un, 8); MW2(Fq, G, sn, un, 9); MW2(Fq, G, sn, un, 10); MW2(Fq, G, sn, un, 11); MW2(Fq, G, sn, un, 12); MW2(Fq, G, sn, un, 13); MW2(Fq, G, sn, un, 14); MW2(Fq, G, sn, un, 15); WAITL(0); SBAR(); } while (0)
#define STAGEW(s) do { HALFW(f0, f1, s, 1); BAR(); SBAR(); HALFW(f1, f0, (s) + 1, 0); } while (0)
#define KLOOP() \
      unsigned aad[2][2], bad[2][2]; \
      { LANE(); const int q = np >> 2, p = l & 3, f = ((q >> 1) & 1) | ((g & 1) << 1); \
        _Pragma("unroll") for (int u = 0; u < 2; ++u) { aad[0][u] = ldsb + (wm * 128 + np) * 128 + (((4 * u + g) ^ (np >> 1)) * 16); aad[1][u] = aad[0][u] + 2 * SLOT; \
          bad[0][u] = ldsb + L_B + (8 * g + q) * 128 + (((2 * wn + (TRN ? u : u ^ wm)) ^ f) * 32) + p * 8; bad[1][u] = bad[0][u] + 2 * SLOT; \
          PIN(aad[0][u]); PIN(aad[1][u]); PIN(bad[0][u]); PIN(bad[1][u]); } } \
      unsigned tR, tC; \
      { LANE(); tR = ldsb + L_ROWM + (wm * 128 + (TRN ? np : 4 * g)) * 4; tC = ldsb + L_COL + (wn * 32 + (TRN ? 4 * g : wm * 16 + np)) * 4; PIN(tR); PIN(tC); SBAR(); } \
      f4 c[8][4]; \
      _Pragma("unroll") for (int i = 0; i < 8; ++i) _Pragma("unroll") for (int jj = 0; jj < 4; ++jj) { c[i][jj] = (f4)(0.f); PIN(c[i][jj]); } \
      FW f0, f1; \
      BAR(); \
      STMP(1); STMC(40); \
      RW(f0, 0, 0, 0); RW(f0, 0, 0, 1); RW(f0, 0, 0, 8); RW(f0, 0, 0, 2); RW(f0, 0, 0, 3); RW(f0, 0, 0, 4); RW(f0, 0, 0, 5); RW(f0, 0, 0, 6); RW(f0, 0, 0, 7); \
      RW(f0, 0, 0, 9); RW(f0, 0, 0, 10); RW(f0, 0, 0, 11); RW(f0, 0, 0, 12); RW(f0, 0, 0, 13); RW(f0, 0, 0, 14); RW(f0, 0, 0, 15); \
      WAITL(0); SBAR(); \
      STAGEW(0); STAGEW(1); STAGEW(2); STAGEW(3); STAGEW(4); STAGEW(5); STAGEW(6); STAGEW(7); \
      STAGEW(8); STAGEW(9); STAGEW(10); STAGEW(11); STAGEW(12); STAGEW(13); STAGEW(14); \
      HALFW(f0, f1, 15, 1); if (TRN) BAR(); SBAR()
#define MT(n) do { MW(f1, n); TB(n); } while (0)
#define KLAST() do { MT(0); MT(1); MT(2); MT(3); MT(4); MT(5); MT(6); MT(7); MT(8); MT(9); MT(10); MT(11); MT(12); MT(13); MT(14); MT(15); \
      MT(16); MT(17); MT(18); MT(19); MT(20); MT(21); MT(22); MT(23); MT(24); MT(25); MT(26); MT(27); MT(28); MT(29); MT(30); MT(31); WAITL(0); } while (0)
#define H2(v, e0) ((f2){(v)[e0], (v)[(e0) + 1]})

#define WQ0 56
#define HXM 0
#define QROT2 0
#define MORD 3
#define PO 0
#define FI (PO ? 3 : 9)
#define PM 2
#define WE 1
#define FORCE_SLOW 0
#define GP1 3
#define WM 0
#define G0A 4
#define OI 1
#define KE 190
#define KD 210
#define KSL 2
#define PD5 8
#define OSL 1
#define MD1 0
#define MD2 1
#define TKP 13
#define CL0 6

__global__ __launch_bounds__(512, 2) void fused(const u16* __restrict__ x, const u16* __restrict__ wqkv, const u16* __restrict__ wo, const u16* __restrict__ wr,
                                                const u16* __restrict__ wg, const u16* __restrict__ wu, const u16* __restrict__ wd, char* ws,
                                                u16* __restrict__ out) {
  extern __shared__ __attribute__((aligned(16))) char lds[];
  const int w = __builtin_amdgcn_readfirstlane(threadIdx.x >> 6);
#define LANE() int l; asm volatile("v_mbcnt_lo_u32_b32 %0, -1, 0\n v_mbcnt_hi_u32_b32 %0, -1, %0" : "=v"(l)); \
  const int np = l & 15, g = l >> 4, rr = l >> 3, sl = l & 7; (void)np; (void)g; (void)rr; (void)sl
  STMP(0);
  const int gb = blockIdx.x, xcd = gb & 7, j = gb >> 3, xq = xcd >> 1;
  const unsigned ldsb = (unsigned)(size_t)(LAS char*)lds;
  const bool isQ = j >= 16;
#define XM 1
  const int rg = XM ? j & 7 : (xcd & 1) * 4 + (j & 3), hp = XM ? xcd * 2 + (j >> 3) : xq * 4 + (j >> 2), qg = XM ? xcd : xq * 2 + ((j >> 2) & 1);
  const int hd0 = isQ ? qg * 4 : 32 + hp * 2, hd1 = isQ ? 2 : 32;
  const bool isB = w == 6;
  unsigned mb_, step, d1, d2, d3, so, lim; u4 rs;
#define VSET() unsigned v[4]; { const unsigned pitch = isB ? 6144 : 2048, sm = isB ? 2 : 7; \
    _Pragma("unroll") for (int i = 0; i < 4; ++i) v[i] = (8 * i + rr) * pitch + ((sl ^ ((isB ? rr : rr >> 1) & sm) ^ ((i & 1) << 2)) * 16) - i * 1024; }
#define QL 16
#define QROT 1
#define G4(s, q, dq) do { unsigned t_; asm volatile("s_add_u32 m0, %1, %2\n s_add_u32 %0, %3, %4" : "=s"(t_) : "s"(mb_), "i"(((s) % 3) * SLOT + (q) * 4096), "s"(so), "s"(dq) : "scc", "memory"); \
        BLDS(v[0], rs, t_, 0); BLDS(v[1], rs, t_, 1024); BLDS(v[2], rs, t_, 2048); BLDS(v[3], rs, t_, 3072); } while (0)
#define PA(s) do { G4(s, 0, 0u); G4(s, 1, d1); } while (0)
#define PB(s) do { G4(s, 2, d2); G4(s, 3, d3); asm volatile("s_add_u32 %0, %0, %1\n s_cmp_eq_u32 %0, %2\n s_cselect_b32 %0, 0, %0" : "+s"(so) : "s"(step), "s"(lim) : "scc"); } while (0)
  if (__builtin_expect(j < 24 && w >= 4 && w < 7, 1)) {
    LANE();
    rs = isB ? mkrs((const char*)wqkv + hd0 * 64, 1024 * 6144 - hd0 * 64) : mkrs((const char*)x + (size_t)(rg * 256 + (w - 4) * 128) * 2048, 128 * 2048);
    mb_ = RFL(ldsb + (w - 4) * 16384); step = isB ? 64 * 6144 : 128;
    d1 = isB ? 32 * 6144 : 65536; d2 = isB ? hd1 * 64 : 131072; d3 = d1 + d2;
    so = isQ ? (QROT + QROT2 * (qg & 1)) * step : 0; lim = QL * step;
    VSET();
    STMP(37);
    PA(0); PB(0); PA(1);
    STMP(38);
  }
#define CPF 1
  u4 rsC;
  { u64 pc; unsigned len;
    asm volatile("s_getpc_b64 %0\n.Lcp0:\n s_mov_b32 %1, .Lcp1-.Lcp0\n s_max_i32 %1, %1, .Lcp2-.Lcp0\n s_max_i32 %1, %1, .Lcp3-.Lcp0\n s_max_i32 %1, %1, .Lcp4-.Lcp0\n s_max_i32 %1, %1, .Lcp5-.Lcp0\n s_max_i32 %1, %1, .Lcp6-.Lcp0\n s_max_i32 %1, %1, 0" : "=&s"(pc), "=&s"(len) :: "scc");
    rsC = (u4){(unsigned)pc & ~127u, (unsigned)(pc >> 32) & 0xFFFFu, len, 0x00020000u}; }
#define CODE(n) asm volatile("s_mov_b32 m0, %2\n s_nop 0\n buffer_load_dwordx4 %0, %1, 0 offen lds" :: "v"((n) * 1024 + l * 16), "s"(rsC), "s"(RFL(ldsb + L_ROWM + 3072)) : "memory")
  if (CPF && j < 24 && w < 4) { LANE(); CODE(j * 4 + w); }
#define CPL(n) do { __builtin_amdgcn_s_waitcnt(0x0F70); asm volatile(".Lcp" #n ":"); } while (0)
#define CPE(n) asm volatile(".Lcp" #n ":")
  const __amdgpu_buffer_rsrc_t wrs = __builtin_amdgcn_make_buffer_rsrc(ws, 0, WS_END, 0x00020000);
  const int mb2 = (xcd & 1) * 8 + (xcd >> 2) * 4 + (j >> 3), nb = ((xcd >> 1) & 1) * 8 + (j & 7), lq = (mb2 + (mb2 >> 2)) & 3, hx = (nb * 4) & HXM;
#define msg (WS_MSG + xcd * 256 + ((l & 8) ? 0 : 64 + sl * 8))
#define MSGOK(f) all64((((l & 8) ? ((f)[0] ^ tlo) | ((f)[1] ^ thi) : (f)[1] ^ tlo)) == 0u)
#define KEYOK(f) all64((l & 8) || (f)[1] == tlo)
#define GBAD(v) (((v)[1] ^ tlo) | ((v)[3] ^ thi))
#define WO_SETUP() const u4 rsO = mkrs((const char*)wo + nb * 128, 1024 * 2048 - nb * 128), rsX = mkrs((const char*)x + (size_t)mb2 * 128 * 2048 + nb * 128, 128 * 2048 - nb * 128); \
  const unsigned vW = rr * 2048 + ((sl ^ (((rr >> 1) & 3) << 1)) * 16), vX = rr * 2048 + ((sl ^ rr) * 16)
#define DMA1(v, rs, m, so) asm volatile("s_mov_b32 m0, %2\n s_nop 0\n buffer_load_dwordx4 %0, %1, %3 offen lds" ::"v"(v), "s"(rs), "s"(RFL(m)), "s"(RFL(so)) : "memory")
#define WOL(n0, n1, st) _Pragma("nounroll") for (int n_ = (n0); n_ < (n1); n_ += (st)) DMA1(vW, rsO, ldsb + L_WO + n_ * 1024, (n_ * 16384) ^ (hx * 65536))
#define WOR(n0, n1, st) _Pragma("nounroll") for (int n_ = (n0); n_ < (n1); n_ += (st)) DMA1(vW, rsO, ldsb + L_WO + (n_ ^ (lq * 32) ^ (hx * 4)) * 1024, (n_ ^ (lq * 32)) * 16384)
#define XTL(n0, st) _Pragma("nounroll") for (int n_ = (n0); n_ < 16; n_ += (st)) DMA1(vX, rsX, ldsb + L_XT + n_ * 1024, n_ * 16384)
  const int ow = isQ && j < 24 ? w ^ 4 : w;
  f4 oc[2][4];
  u4 st2[2];
  unsigned tpoll = 0;
#define TPOLL(d) tpoll = (unsigned)__builtin_amdgcn_s_memrealtime() + (d)
#define PDK 280
#define PDQ 80
#define XT(r, ct) (xt + (r) * 2048 + (((2 * (ct) + (g >> 1)) ^ sl) * 16))
#define S2OK() all64(((st2[0][1] ^ t_[0]) | (st2[0][3] ^ t_[1]) | (st2[1][1] ^ t_[0]) | (st2[1][3] ^ t_[1])) == 0u)
#define OINIT() do { PIN(st2[0]); PIN(st2[1]); const char* xt = lds + L_XT + (ow * 32 + np) * 128 + (g & 1) * 8; \
    { const u2 t_ = *(const u2*)(lds + L_TAG); if (__builtin_expect(!S2OK(), 0)) { _Pragma("nounroll") for (unsigned it = 0; it < SPIN_CAP; ++it) { CLOB(); ST2(); if (S2OK()) break; } } } \
    _Pragma("unroll") for (int r = 0; r < 2; ++r) { \
      const float c1 = 1.f + U2F(st2[r][2]), c0 = -hi(st2[r][0]) * U2F(st2[r][2]); \
      _Pragma("unroll") for (int ct = 0; ct < 4; ++ct) { const u2 v = *(const u2*)XT(r, ct); \
        oc[r][ct] = (f4){__builtin_fmaf(lo(v[0]), c1, c0), __builtin_fmaf(hi(v[0]), c1, c0), __builtin_fmaf(lo(v[1]), c1, c0), __builtin_fmaf(hi(v[1]), c1, c0)}; PIN(oc[r][ct]); } \
      } } while (0)
#define XOWN() do { WO_SETUP(); (void)vW; (void)rsO; _Pragma("unroll") for (int n_ = 0; n_ < 4; ++n_) DMA1(vX, rsX, ldsb + L_XT + (ow * 4 + n_) * 1024, (ow * 4 + n_) * 16384); \
    const u4 rsW = mkrs(ws, WS_END); const unsigned sto = WS_MS + (mb2 * 128 + (w & 3) * 32 + np) * 16, sto1 = sto + 256; ALD128(st2[0], sto); ALD128(st2[1], sto1); } while (0)
#define ST2() do { const unsigned sto = WS_MS + (mb2 * 128 + (w & 3) * 32 + np) * 16; st2[0] = LD128(sto); st2[1] = LD128(sto + 256); } while (0)

  if (j < 24) {
    if (w >= 4) {
      LANE();
      unsigned tlo = 0, thi = 0;
      WO_SETUP();
      u2 f = (u2){0u, 0u};
      if (w == 7) {
        const u4 rsW = mkrs(ws, WS_END);
        int nbar = 16;
#define TICK() do { if (nbar) { BAR(); --nbar; } } while (0)
        if (l < 16) ((unsigned*)(lds + L_CNT))[l] = 0u;
        WAITL(0);
        TICK();
        u2 ep; unsigned z0 = 0; PIN(z0);
        ALD64(ep, z0);
        { const u4 pz = (u4){POISON, POISON, POISON, POISON};
          if (isQ) {
            ST128(pz, WS_OFL + ((rg * 2 + (l >> 5)) * 32 + qg * 4) * 128 + (l & 31) * 16);
          } else {
            const unsigned kb = WS_KVP + (hp * 16 + rg) * KVS + l * 16;
            ST128(pz, kb); ST128(pz, kb + 1024); ST128(pz, kb + 8 * KVS); ST128(pz, kb + 8 * KVS + 1024);
            ST128(pz, l < 16 ? WS_KVP + (hp * 16 + rg + (l & 8)) * KVS + 2048 + sl * 16 : WS_OFF);
          } }
        SBAR();
        if (PO && isQ) {
#define PPS 8
          TICK();
#pragma nounroll
          for (int i = 0; i < 16; i += PPS) {
            TICK();
            const unsigned ob = WS_OF + ((rg * 16 + i) * 32 + qg * 4) * 1024 + l * 16; const u4 pz = (u4){POISON, POISON, POISON, POISON};
#pragma unroll
            for (int k = 0; k < PPS; ++k) { ST128(pz, ob + k * 32768); ST128(pz, ob + k * 32768 + 1024); }
          }
        } else TICK();
        TICK(); TICK();
        WAITV(0); PIN(ep);
        const u64 tag = (((u64)RFL(ep[1]) << 32) | RFL(ep[0])) + TAG_STEP;
        tlo = (unsigned)tag; thi = (unsigned)(tag >> 32);
        ST64(((u2){tlo, thi}), l == 0 ? WS_FLAG + gb * 8 : WS_OFF);
        STMP(8);
#pragma nounroll
        while (nbar > 16 - TKP) { TICK();
#define CPF2 10
          if (CPF2 && nbar == 16 - CPF2) { CODE(j * 4); CODE(j * 4 + 1); CODE(j * 4 + 2); CODE(j * 4 + 3); }
        }
        u4 ms[4], cs[8];
        const unsigned a0 = WS_MS + (rg * 256 + l) * 16, a2 = WS_CS + ((g * 96 + hd0) * 16 + np) * 16, a3 = a2 + hd1 * 256, a4 = a2 + 4 * 96 * 256, a5 = a3 + 4 * 96 * 256, a6 = msg;
#pragma nounroll
        for (unsigned it = 0; it < SPIN_CAP; ++it) {
#pragma unroll
          for (int i = 0; i < 4; ++i) asm volatile("buffer_load_dwordx4 %0, %1, %2, 0 offen offset:%3 sc1" : "=v"(ms[i]) : "v"(a0), "s"(rsW), "i"(i * 1024) : "memory");
#pragma unroll
          for (int i = 0; i < 8; ++i) asm volatile("buffer_load_dwordx4 %0, %1, %2, 0 offen offset:%3 sc1" : "=v"(cs[i]) : "v"(i < 2 ? a2 : i < 4 ? a3 : i < 6 ? a4 : a5), "s"(rsW), "i"((i & 1) * 256) : "memory");
          ALD64(f, a6);
          if (nbar > 1) TICK();
          if (nbar > 1 && it == 0) TICK();
          WAITV(0);
          unsigned bad = 0;
#pragma unroll
          for (int i = 0; i < 4; ++i) { PIN(ms[i]); PIN(cs[i]); PIN(cs[4 + i]); bad |= GBAD(ms[i]) | GBAD(cs[i]) | GBAD(cs[4 + i]); }
          PIN(f);
          if (all64(bad == 0u)) break;
        }
        STMP(9);
        float ct[2];
#pragma unroll
        for (int e = 0; e < 2; ++e) {
          float t[4];
#pragma unroll
          for (int i = 0; i < 4; ++i) t[i] = add1(U2F(cs[i][2 * e]), U2F(cs[4 + i][2 * e]));
#pragma unroll
          for (int i = 0; i < 2; ++i) { auto r = __builtin_amdgcn_permlane32_swap(F2U(t[i]), F2U(t[i + 2]), false, false); t[i] = add1(U2F(r[0]), U2F(r[1])); }
          auto r = __builtin_amdgcn_permlane16_swap(F2U(t[0]), F2U(t[1]), false, false);
          ct[e] = add1(U2F(r[0]), U2F(r[1])); PIN(ct[e]);
        }
#pragma unroll
        for (int i = 0; i < 4; ++i) { ms[i][0] = (ms[i][0] & 0xFFFF0000u) ^ 0x80000000u; ms[i][1] = F2U(mul1(U2F(ms[i][2]), 1.44269504f)); PIN(ms[i]); }
#pragma nounroll
        while (nbar) TICK();
#pragma unroll
        for (int i = 0; i < 4; ++i) { ((unsigned*)(lds + L_ROWM))[i * 64 + l] = ms[i][0]; ((unsigned*)(lds + L_ROWS))[i * 64 + l] = ms[i][1]; }
        ((float*)(lds + L_COL))[g * 32 + np] = ct[0]; ((float*)(lds + L_COL))[g * 32 + 16 + np] = ct[1];
        if (l == 8) *(u2*)(lds + L_TAG) = (u2){tlo, thi};
        if (!isQ) { lds_arrive((LAS unsigned*)(lds + L_CNT) + 5);
          if (KE) ST128(((u4){tlo, thi, (unsigned)__builtin_amdgcn_s_memrealtime(), 0u}), l == 0 ? WS_ETA + (hp * 8 + rg) * 16 : WS_OFF); }
      } else {
        VSET();
        WAITV(8); BAR(); PB(1); PA(2); WAITV(8); BAR(); STMP(44); PB(2); if (QL == 3) so = lim; PA(0); PB(0);
#define LS(s) do { if ((s) < 14) WAITV(16); else WAITV(0); BAR(); STMP(44 + (s)); if ((s) + 3 < 16) { if ((s) + 3 == QL) so = lim; PA(s); PB(s); } } while (0)
        LS(1); LS(2); LS(3); LS(4); LS(5); LS(6); LS(7); LS(8); LS(9); LS(10); LS(11); LS(12); LS(13); LS(14);
      }
#define KEYS() do { if (w == 7) { _Pragma("nounroll") for (unsigned it = 0; it < SPIN_CAP && !MSGOK(f); ++it) { f = LD64(msg); CLOB(); } \
        if (l < 8) ((unsigned*)(lds + L_TAG + 16))[l] = f[0]; \
        lds_arrive((LAS unsigned*)(lds + L_CNT) + 3); } } while (0)
      if (isQ) {
        if (w < 7) { WOL(20 + w - 4, WQ0, 3); }
        LBAR(); STMP(59);
        unsigned tq = (unsigned)__builtin_amdgcn_s_memrealtime();
        XOWN(); SBAR(); WOL(WQ0 + w - 4, 128, 4);
        u4 eta;
        { const u4 rsW = mkrs(ws, WS_END); const unsigned eo = l < 16 ? WS_ETA + qg * 256 + l * 16 : WS_OFF; ALD128(eta, eo); }
        WOL(w - 4, 20, 4);
        WAITV(5); PIN(st2[0]); PIN(st2[1]); PIN(eta);
        if (KE) { const u2 t_ = *(const u2*)(lds + L_TAG);
          const unsigned d = wave_max(eta[0] == t_[0] && eta[1] == t_[1] ? (unsigned)min(max((int)(eta[2] + 200u - tq), 1), 400) : 0u);
          if (d) tq += d + KE - KD - 200u; }
        if (OI == 0) OINIT();
        KEYS();
        STMP(21);
        const unsigned sb = WS_KVP + (qg * 4 + (w & 1)) * 8 * KVS + ((w >> 1) & 1) * 1024 + l * 16;
        const unsigned sz = (w & 2) ? WS_OFF : WS_KVP + ((qg * 4 + (w & 1)) * 8 + (np >> 1)) * KVS + 2048 + (np & 1) * 64 + g * 16;
        u4 prm[2];
#pragma unroll
        for (int e = 0; e < 4; ++e) {
          const unsigned o = (np >> 2) == g ? ((np & 1) ? 0x3F800000u : 0x00003F80u) : 0u;
          prm[0][e] = e == (w & 2) + ((np >> 1) & 1) ? o : 0u; prm[1][e] = e == 2 - (w & 2) + ((np >> 1) & 1) ? o : 0u;
        }
        PIN(prm[0]); PIN(prm[1]);
        __builtin_amdgcn_s_setprio(2);
#pragma nounroll
        while ((int)((unsigned)__builtin_amdgcn_s_memrealtime() - tq) < KD) __builtin_amdgcn_s_sleep(1);
        STMP(39);
        lds_wait<1>((LAS unsigned*)(lds + L_CNT) + 3, 1);
        u4 v[16], zv[2]; f4 kg[4][2];
#define KVLD(gq) do { _Pragma("unroll") for (int p = 4 * (gq); p < 4 * (gq) + 4; ++p) v[p] = __builtin_amdgcn_raw_buffer_load_b128(wrs, sb, ((p & 7) + (p >> 3) * 16) * KVS, 16); } while (0)
#define ZLD(hh) zv[hh] = __builtin_amdgcn_raw_buffer_load_b128(wrs, sz, (hh) * 16 * KVS, 16)
        ZLD(0); KVLD(0); KVLD(1); SBAR(); if (GP1) PACE(GP1); ZLD(1); KVLD(2); KVLD(3); SBAR();
#pragma unroll
        for (int hh = 0; hh < 2; ++hh) {
          unsigned pend = 7;
#pragma nounroll
          for (unsigned it = 0; it < SPIN_CAP; ++it) {
#pragma unroll
            for (int gq = 2 * hh; gq < 2 * hh + 2; ++gq)
              if (pend & (1u << (gq & 1))) {
                f4 t0_ = (f4)(0.f), t1_ = (f4)(0.f); unsigned mx = 0;
#pragma unroll
                for (int p = 4 * gq; p < 4 * gq + 4; ++p) { mx = mx4(mx, v[p]); t0_ = MFMA16(BC8(prm[0]), BC8(v[p]), t0_); t1_ = MFMA16(BC8(prm[1]), BC8(v[p]), t1_); }
                kg[gq][0] = t0_; kg[gq][1] = t1_;
                if (clean(mx)) pend &= ~(1u << (gq & 1));
              }
            if ((pend & 4u) && clean(mx4(0u, zv[hh]))) pend &= ~4u;
            if (!pend) break;
            if (KSL) __builtin_amdgcn_s_sleep(KSL);
            CLOB();
            if (pend & 4u) ZLD(hh);
            if (pend & 1u) KVLD(2 * hh);
            if (pend & 2u) KVLD(2 * hh + 1);
            if (hh == 0) { ZLD(1); KVLD(2); KVLD(3); }
            SBAR();
          }
          if (hh) STMP(25); else STMP(4);
          const f4 k0 = kg[2 * hh][0] + kg[2 * hh + 1][0], k1 = kg[2 * hh][1] + kg[2 * hh + 1][1];
          *(u4*)(lds + L_KX + ((2 * hh + (w & 1)) * 2 + ((w >> 1) & 1)) * 1024 + l * 16) = (u4){pk2(k0[0], k0[1]), pk2(k0[2], k0[3]), pk2(k1[0], k1[1]), pk2(k1[2], k1[3])};
          if (!(w & 2)) {
            f4 z = __builtin_bit_cast(f4, zv[hh]);
#pragma unroll
            for (int e = 0; e < 4; ++e) { z[e] += DPPF(z[e], 0x122, 0xf); z[e] += DPPF(z[e], 0x124, 0xf); z[e] += DPPF(z[e], 0x128, 0xf); }
            if (np < 2) *(u2*)(lds + L_ZX + (2 * hh + (w & 1)) * 64 + g * 16 + np * 8) = (u2){pk2(z[0], z[1]), pk2(z[2], z[3])};
          }
          LBAR();
        }
        __builtin_amdgcn_s_setprio(0);
        TPOLL(PDQ);
        if (OI == 1) OINIT();
        CPL(3);
      } else {
        if (w < 7) { if (WE) { WOL(32 + w, 128, 3); WAITV(0); STMP(35); } lds_arrive((LAS unsigned*)(lds + L_CNT) + 2); }
        KEYS();
        __builtin_amdgcn_s_waitcnt(0x0F70);
      }
    } else {
      const int wm = w >> 1, wn = w & 1;
      if (!isQ) {
        constexpr bool TRN = false;
        unsigned cA = ldsb + L_CNT; PIN(cA);
        unsigned exw, exa, pka, pza, cB = cA + wn * 4, one1 = 1u; bool zsel;
        { LANE(); const unsigned kb = WS_KVP + ((hp * 2 + wn) * 8 + rg) * KVS;
          exw = ldsb + L_EX + l * 16 + w * 4096; exa = exw ^ 8192; pka = kb + wm * 1024 + l * 16; pza = wm == 0 && np < 2 ? kb + 2048 + np * 64 + g * 16 : WS_OFF; zsel = (np ^ wm) & 1;
          PIN(exw); PIN(exa); PIN(pka); PIN(pza); PIN(cB); PIN(one1); SBAR(); }
        KLOOP();
        float cc[4]; f4 rsk[8], mk[8]; unsigned tf;
        unsigned tC1 = tC ^ 64; PIN(tC1);
#define TB1(n) do { if ((n) < 4) RD32(cc[(n) & 3], ((n) & 1) ? tC1 : tC, ((n) >> 1) * 256); else if ((n) & 1) RD128(rsk[(((n) - 4) >> 1) & 3], tR, 1024 + (((n) - 4) >> 1) * 64); else RD128(mk[(((n) - 4) >> 1) & 3], tR, (((n) - 4) >> 1) * 64); } while (0)
#define TB(n) do { if ((n) == 8) RD32(tf, cA, 20); else if ((n) > 8 && (n) < 21) TB1(((n) - 9) % 12); SBAR(); } while (0)
        KLAST();
#undef TB
        PIN(tf);
        if (__builtin_expect(RFL(tf) == 0u, 0)) {
          lds_wait<0>((LAS unsigned*)(lds + L_CNT) + 5, 1);
          TB1(0); TB1(1); TB1(2); TB1(3); TB1(4); TB1(5); TB1(6); TB1(7); TB1(8); TB1(9); TB1(10); TB1(11); WAITL(0);
        }
#pragma unroll
        for (int i = 0; i < 4; ++i) { PIN(mk[i]); PIN(rsk[i]); PIN(cc[i]); }
#pragma unroll
        for (int i = 4; i < 8; ++i) { RD128(mk[i], tR, i * 64); RD128(rsk[i], tR, 1024 + i * 64); }
        SBAR();
        STMP(2); STMC(41);
        f4 kv[2][2], zz[2];
        kv[0][0] = kv[0][1] = kv[1][0] = kv[1][1] = zz[0] = zz[1] = (f4)(0.f);
        const u4 ones = (u4){0x3F803F80u, 0x3F803F80u, 0x3F803F80u, 0x3F803F80u};
        u4 kf[2], vf[2];
#define LN2(i, jj, e0) (__builtin_elementwise_fma(H2(mk[i], e0), (f2)(cc[jj]), H2(c[i][jj], e0)) * H2(rsk[i], e0))
#define KVM(n) do { if ((n) < 4) kv[(n) >> 1][(n) & 1] = MFMA16(BC8(kf[(n) >> 1]), BC8(vf[(n) & 1]), kv[(n) >> 1][(n) & 1]); else zz[(n) & 1] = MFMA16(BC8(kf[(n) & 1]), BC8(ones), zz[(n) & 1]); } while (0)
#pragma unroll
        for (int ip = 0; ip < 4; ++ip) {
          if (ip == 2) { WAITL(0);
#pragma unroll
            for (int i = 4; i < 8; ++i) { PIN(mk[i]); PIN(rsk[i]); } }
          f2 ak[8], av[8], ek[8];
#pragma unroll
          for (int n = 0; n < 8; ++n) { const int i = 2 * ip + ((n >> 1) & 1), e0 = (n & 1) * 2, t = n >> 2; ak[n] = LN2(i, t, e0); av[n] = LN2(i, 2 + t, e0); }
          SBAR();
#pragma unroll
          for (int n = 0; n < 8; ++n) {
            if (ip > 0 && n < 6) KVM(n);
            ek[n] = (f2){__builtin_amdgcn_fmed3f(__builtin_amdgcn_exp2f(ak[n][0]), 0.f, 1.f), __builtin_amdgcn_fmed3f(__builtin_amdgcn_exp2f(ak[n][1]), 0.f, 1.f)};
            ak[n] = (f2){fmaxf(ak[n][0], 1.44269504e-6f), fmaxf(ak[n][1], 1.44269504e-6f)};
            SBAR();
          }
#pragma unroll
          for (int n = 0; n < 8; ++n) { const f2 r = __builtin_elementwise_fma(ak[n], (f2)(0.693147181f), ek[n]); kf[n >> 2][n & 3] = pk2(r[0], r[1]); vf[n >> 2][n & 3] = pk2(av[n][0], av[n][1]); }
          SBAR();
        }
        KVM(1); KVM(3); KVM(4); KVM(5); KVM(0); KVM(2);
        STMP(19);
        const f4 zs = zsel ? zz[1] : zz[0];
        *(LAS f4*)(size_t)(exw + 1024) = kv[0][1]; *(LAS f4*)(size_t)exw = kv[1][1]; *(LAS f4*)(size_t)(exw + 2048) = zs;
        asm volatile("s_mov_b64 exec, 1\n ds_add_u32 %0, %1 offset:24\n s_mov_b64 exec, -1" ::"v"(cB), "v"(one1) : "memory");
        f4 e0, e1, e2;
#pragma nounroll
        do {
          RD32(tf, cB, 24); RD128(e0, exa, 0); RD128(e1, exa, 1024); RD128(e2, exa, 2048);
          WAITL(0); PIN(tf); PIN(e0); PIN(e1); PIN(e2);
        } while (RFL(tf) < 2u);
        e0 += kv[0][0]; e1 += kv[1][0];
        ST128(san4((u4){pk2(e0[0], e0[1]), pk2(e0[2], e0[3]), pk2(e1[0], e1[1]), pk2(e1[2], e1[3])}), pka);
        ST128(san4(__builtin_bit_cast(u4, (zs + e2) * 1.44269504f)), pza);
        SBAR();
        STMP(3);
        LANE();
        TPOLL(PDK);
        if (j == 1 && w == 0) { const u2 t_ = *(const u2*)(lds + L_TAG); ST128(((u4){t_[0], t_[1], tpoll, 0u}), l < 8 ? WS_HU + xcd * 128 + l * 16 : WS_OFF); }
        lds_arrive((LAS unsigned*)(lds + L_CNT) + 8);
        lds_wait<1>((LAS unsigned*)(lds + L_CNT) + 8, 4);
        XOWN(); SBAR();
        { WO_SETUP(); (void)vX; (void)rsX; if (WE) { WOL(w, 36, 4); } else { WOR(w, 128, 4); } }
        WAITV(WE ? 9 : 32); OINIT();
        WAITV(0);
        lds_arrive((LAS unsigned*)(lds + L_CNT) + 2);
        lds_wait<1>((LAS unsigned*)(lds + L_CNT) + 3, 1);
        lds_wait<1>((LAS unsigned*)(lds + L_CNT) + 2, 7);
        CPL(1);
        STMP(20);
      } else {
        constexpr bool TRN = true;
        KLOOP();
        f4 cc[4]; float rs[8], mn[8];
#define TB(n) do { if ((n) < 4) RD128(cc[(n) & 3], tC, ((n) >> 1) * 256 + ((n) & 1) * 64); else if ((n) < 20) { if ((n) & 1) RD32(rs[(((n) - 4) >> 1) & 7], tR, 1024 + (((n) - 4) >> 1) * 64); else RD32(mn[(((n) - 4) >> 1) & 7], tR, (((n) - 4) >> 1) * 64); } SBAR(); } while (0)
        KLAST();
#undef TB
#pragma unroll
        for (int i = 0; i < 8; ++i) { PIN(mn[i]); PIN(rs[i]); PIN(cc[i & 3]); }
        STMP(2); STMC(41);
        LANE();
        u4 pf[8][2];
#define PH 6
#define PHQ(hh, i0, i1) _Pragma("unroll") for (int i = i0; i < i1; ++i) { _Pragma("unroll") for (int e = 0; e < 4; ++e) { const int jj = 2 * (hh) + (e >> 1), e0 = (e & 1) * 2; \
            pf[i][hh][e] = phi2(__builtin_elementwise_fma(H2(cc[jj], e0), (f2)(mn[i]), H2(c[i][jj], e0)) * rs[i]); } PIN(pf[i][hh]); }
        PHQ(0, 0, 8); PHQ(1, 0, PH);
        const u2 tg = *(const u2*)(lds + L_TAG);
        const unsigned ofl = l < 8 ? WS_OFL + ((rg * 2 + wm) * 32 + (qg * 2 + wn) * 2) * 128 + sl * 16 : WS_OFF;
        unsigned oa[8];
#pragma unroll
        for (int i = 0; i < 8; ++i) { oa[i] = WS_OF + ((rg * 16 + wm * 8 + i) * 32 + qg * 4 + wn) * 1024 + l * 16; PIN(oa[i]); }
        STMP(19);
        __builtin_amdgcn_s_setprio(1);
        BAR();
        STMP(22);
        u4 kA[2][2], zA[2];
#define KRD(hh) do { zA[hh] = *(const u4*)(lds + L_ZX + (2 * (hh) + wn) * 64 + g * 16); \
          kA[hh][0] = *(const u4*)(lds + L_KX + (2 * (hh) + wn) * 2048 + l * 16); kA[hh][1] = *(const u4*)(lds + L_KX + (2 * (hh) + wn) * 2048 + 1024 + l * 16); SBAR(); } while (0)
        KRD(0);
        if (FI < 0) { ST128(((u4){tg[0], tg[1], tg[0], tg[1]}), ofl); SBAR(); STMP(24); }
        f4 dn[16], x0[16], x1[16];
#define OM(n) do { dn[n] = MFMA16(BC8(zA[(n) >> 3]), BC8(pf[(n) & 7][(n) >> 3]), (f4)(1.44269504e-6f)); x0[n] = MFMA16(BC8(kA[(n) >> 3][0]), BC8(pf[(n) & 7][(n) >> 3]), (f4)(0.f)); \
          x1[n] = MFMA16(BC8(kA[(n) >> 3][1]), BC8(pf[(n) & 7][(n) >> 3]), (f4)(0.f)); } while (0)
        OM(0); OM(1);
#pragma unroll
        for (int n = 0; n < 16; ++n) {
          SBAR();
          const float inv = __builtin_amdgcn_rcpf(dn[n][0]);
          asm volatile("" :: "v"(dn[n]));
          if (n + 2 < 16 && n != 6 && n != 7) OM(n + 2);
          float y[8];
#pragma unroll
          for (int e = 0; e < 4; ++e) { y[e] = x0[n][e] * inv; y[4 + e] = x1[n][e] * inv; PIN(y[e]); PIN(y[4 + e]); }
          const u4 ov = (u4){pk2(y[0], y[1]), pk2(y[2], y[3]), pk2(y[4], y[5]), pk2(y[6], y[7])};
          ST128(PO && n < 8 ? san4(ov) : ov, oa[n & 7] + (n >> 3) * 2048);
          if (n == FI && FI != 7) { SBAR(); if (!PO) WAITV(FI > 7 ? FI - 7 : 0); ST128(((u4){tg[0], tg[1], tg[0], tg[1]}), ofl); SBAR(); STMP(24); }
          if (n == 7) { SBAR(); BAR(); KRD(1); PHQ(1, PH, 8); SBAR();
            if (FI == 7) { WAITV(0); ST128(((u4){tg[0], tg[1], tg[0], tg[1]}), ofl); SBAR(); STMP(24); }
            OM(8); OM(9); }
        }
        SBAR();
        STMP(23);
        WAITV(0);
        ST128(((u4){tg[0], tg[1], tg[0], tg[1]}), ofl + 128);
        SBAR();
        __builtin_amdgcn_s_setprio(0);
        CPL(2);
        STMP(5);
      }
    }
  } else {
    LANE();
    const int jm = j - 24, jsl = 7 - xcd, c0 = 4 - (int)(((size_t)wg >> 7) & 7) + xcd;
    const bool master = jm == 7 && jsl == 7, help = jm == 7 && jsl != 7, half = jm == 7 || jm == jsl;
    const int jc = help ? jsl : jm, hc = jc * 8 + ((c0 + jc) & 7);
    const int row0 = (xcd & 1) * 1024 + (xq * 8 + jm) * 32 + w * 4;
    u4 cw[12], xr[8], wv[2];
    const u4 rsW = mkrs(ws, WS_END), rsR = mkrs((const char*)x, S * D * 2), rsQ = mkrs((const char*)wqkv, D * 6144);
    const unsigned co = ((jm * 32 + w * 4 + g) * 3072 + xcd * 128 + np * 8) * 2;
#define CI(n) (((n) % 3) * 4 + (n) / 3)
#define CL(a, b) do { _Pragma("unroll") for (int n = a; n < b; ++n) \
      asm volatile("buffer_load_dwordx4 %0, %1, %2, %3 offen" : "=v"(cw[CI(n)]) : "v"(co), "s"(rsQ), "s"((unsigned)(((CI(n) & 3) * 768 + ((CI(n) >> 2) + 1) % 3) * 2048)) : "memory"); } while (0)
    if (w == 0 && l < 8) ((unsigned*)(lds + L_CNT))[l] = 0u;
    CL(0, MORD);
    LBAR();
    u2 ep;
    { const unsigned xo = row0 * 2048 + l * 16, l16 = l * 16; unsigned z0 = 0; PIN(z0);
#pragma unroll
      for (int i = 0; i < 8; ++i) { const int n = (i & 3) * 2 + (i >> 2);
        asm volatile("buffer_load_dwordx4 %0, %1, %2, %3 offen offset:%4" : "=v"(xr[n]) : "v"(xo), "s"(rsR), "s"((unsigned)((n >> 2) * 4096)), "i"((n & 3) * 1024) : "memory"); }
      asm volatile("global_load_dwordx4 %0, %2, %3\n global_load_dwordx4 %1, %2, %3 offset:1024" : "=&v"(wv[0]), "=&v"(wv[1]) : "v"(l16), "s"(uptr((const char*)wr)) : "memory");
      ALD64(ep, z0); }
    CL(MORD, CL0);
    WAITV(CL0 - MORD);
#pragma unroll
    for (int i = 0; i < 8; ++i) PIN(xr[i]);
    PIN(wv[0]); PIN(wv[1]); PIN(ep);
    const u64 tag = (((u64)RFL(ep[1]) << 32) | RFL(ep[0])) + TAG_STEP;
    const unsigned tlo = (unsigned)tag, thi = (unsigned)(tag >> 32);
    const u2 tgv = (u2){tlo, thi};
    {
      float a[13];
#pragma unroll
      for (int i = 0; i < 13; ++i) a[i] = 0.f;
      unsigned one2 = 0x3F803F80u; PIN(one2);
#pragma unroll
      for (int jj = 0; jj < 8; ++jj) {
        const unsigned wv_ = wv[jj >> 2][jj & 3];
#pragma unroll
        for (int i = 0; i < 4; ++i) {
          const unsigned xv = xr[2 * i + (jj >> 2)][jj & 3];
          a[3 * i] = DOT2(xv, one2, a[3 * i]); a[3 * i + 1] = DOT2(xv, xv, a[3 * i + 1]); a[3 * i + 2] = DOT2(xv, wv_, a[3 * i + 2]);
        }
        a[12] = DOT2(wv_, one2, a[12]);
      }
#define RSTEP(n, ctrl) _Pragma("unroll") for (int i = 0; i < n; ++i) a[i] += DPPF(a[i], ctrl, 0xf)
      RSTEP(13, 0xB1); RSTEP(13, 0x4E);
#pragma unroll
      for (int i = 0; i < 3; ++i) { const float u0 = (l & 1) ? a[3 + i] : a[i], u1 = (l & 1) ? a[9 + i] : a[6 + i]; a[i] = (l & 2) ? u1 : u0; }
      a[3] = a[12];
      RSTEP(4, 0x124); RSTEP(4, 0x128);
#pragma unroll
      for (int i = 0; i < 4; ++i) {
        auto s16 = __builtin_amdgcn_permlane16_swap(F2U(a[i]), F2U(a[i]), false, false);
        a[i] = U2F(s16[0]) + U2F(s16[1]);
        auto s32 = __builtin_amdgcn_permlane32_swap(F2U(a[i]), F2U(a[i]), false, false);
        a[i] = U2F(s32[0]) + U2F(s32[1]);
      }
      const float mf = a[0] * (1.f / D), m = rbf(mf);
      const float var = rbf(fmaxf(a[1] * (1.f / D) - mf * mf, 0.f));
      const float rs = __builtin_amdgcn_rcpf(rbf(__builtin_amdgcn_sqrtf(rbf(var + 1e-5f))));
      const float dsc = rs * (a[2] - m * a[3]);
      ST128(((u4){pk2(dsc, m), tlo, F2U(rs), thi}), l < 4 ? WS_MS + (row0 + l) * 16 : WS_OFF);
    }
    SBAR();
    STMP(18);
    CL(CL0, 12);
    u4 gw[4][8], gf[4][8], dw[2][8];
    const u16* wp = ((np >> 3) ? wu : wg) + (size_t)(128 * w + (help ? 64 : 0) + 8 * g) * F + 64 * hc + 8 * sl;
#define GL(b, on) GL2(b, on, 0, 8)
#define GL2(b, on, i0, i1) do { const u16* p_ = (on) ? wp + (size_t)(32 * (b)) * F : wr; const unsigned st_ = (on) ? F : 0; \
      _Pragma("unroll") for (int i_ = i0; i_ < i1; ++i_) asm volatile("global_load_dwordx4 %0, %1, off nt" : "=v"(gw[b][i_]) : "v"(p_ + (size_t)(i_ * st_)) : "memory"); } while (0)
#define GT(b, NV) do { WAITV(NV); _Pragma("unroll") for (int c_ = 0; c_ < 8; ++c_) PIN(gw[b][c_]); _Pragma("unroll") for (int c_ = 0; c_ < 8; ++c_) { TRFRAG(gf[b][c_], gw[b], c_); PIN(gf[b][c_]); } SBAR(); } while (0)
    GL2(0, !master, 0, G0A);
    WAITV(G0A);
#pragma unroll
    for (int i = 0; i < 12; ++i) PIN(cw[i]);
    char* scr = lds + L_WO + (((lq ^ (hx >> 3)) & 2) ? 0 : 65536);
    float* red = (float*)scr;
    {
      u4 sel8;
      const unsigned one = (np & 1) ? 0x3F800000u : 0x00003F80u;
#pragma unroll
      for (int e = 0; e < 4; ++e) sel8[e] = ((np >> 1) == e) ? one : 0u;
#pragma unroll
      for (int p = 0; p < 3; ++p) {
        f4 c = (f4)(0.f);
#pragma unroll
        for (int i = 0; i < 4; ++i) c = MFMA16(BC8(sel8), BC8(cw[p * 4 + i]), c);
        if (g < 2) *(f4*)(red + (w * 3 + p) * 128 + 8 * np + 4 * g) = c;
      }
    }
    lds_arrive((LAS unsigned*)(lds + L_CNT) + 2);
    if (w < 3) {
      lds_wait<1>((LAS unsigned*)(lds + L_CNT) + 2, 8);
      if (w == 0) ST64(tgv, l == 0 ? WS_FLAG + gb * 8 : WS_OFF);
      const float* rp = red + w * 128 + g * 32 + np;
      float c0 = rp[0], c1 = rp[16];
#pragma unroll
      for (int i = 1; i < 8; ++i) { c0 += rp[i * 384]; c1 += rp[i * 384 + 16]; }
      ST128(((u4){F2U(c0), tlo, F2U(c1), thi}), WS_CS + ((jm * 96 + (w + 1) % 3 * 32 + xcd * 4) * 16 + l) * 16);
    }
    SBAR();
    STMP(17);
    if (master) {
      u4 rv[4];
      const unsigned ra = WS_MS + (w * 256 + l) * 16;
      unsigned bad = 15;
      if (MD1) PACE(MD1);
#pragma nounroll
      for (unsigned it = 0; it < SPIN_CAP; ++it) {
#pragma unroll
        for (int i = 0; i < 4; ++i) {
          const u4 v = LD128((bad >> i) & 1u ? ra + i * 1024 : WS_OFF);
          rv[i] = (bad >> i) & 1u ? v : rv[i];
        }
        bad = 0;
#pragma unroll
        for (int i = 0; i < 4; ++i) bad |= GBAD(rv[i]) ? 1u << i : 0u;
        if (all64(bad == 0u)) break;
        __builtin_amdgcn_s_sleep(4);
        CLOB();
      }
      STMP(10);
      unsigned key[4];
#pragma unroll
      for (int i = 0; i < 4; ++i) {
        const unsigned b = rv[i][0] << 16;
        key[i] = ((b ^ ((unsigned)((int)b >> 31) | 0x80000000u)) & 0xFFFF0000u) | (unsigned)(0xFFFF - (w * 256 + i * 64 + l));
      }
      unsigned* cand = (unsigned*)(lds + L_ROWM);
      LAS unsigned* cnt = (LAS unsigned*)(lds + L_CNT) + 1;
      { const unsigned m = wave_max(max(mx3(key[0], key[1], key[2]), key[3]));
        cand[l < 8 ? w * 8 + l : 96 + w] = l < 8 ? 0u : m; }
      LBAR();
      { const u4 m0 = *(const u4*)(cand + 96), m1 = *(const u4*)(cand + 100);
        const unsigned T = min(min(min(m0[0], m0[1]), min(m0[2], m0[3])), min(min(m1[0], m1[1]), min(m1[2], m1[3])));
        unsigned pos[4], n = 0, base;
#pragma unroll
        for (int i = 0; i < 4; ++i) { const u64 b = __builtin_amdgcn_ballot_w64(key[i] >= T);
          pos[i] = __builtin_amdgcn_mbcnt_hi((unsigned)(b >> 32), __builtin_amdgcn_mbcnt_lo((unsigned)b, n)); n += __builtin_popcountll(b); }
        asm volatile("s_mov_b64 exec, 1\n ds_add_rtn_u32 %0, %1, %2\n s_mov_b64 exec, -1\n s_waitcnt lgkmcnt(0)" : "=v"(base) : "v"((unsigned)(size_t)cnt), "v"(n) : "memory");
        base = RFL(base);
#pragma unroll
        for (int i = 0; i < 4; ++i) cand[key[i] >= T ? min(base + pos[i], 72u) : 72u + np] = key[i]; }
      LBAR();
      STMP(11);
      if (RFL(*cnt) > 64u || FORCE_SLOW) {
#define CE(i, j) do { const unsigned hi_ = max(key[i], key[j]), lo_ = min(key[i], key[j]); key[i] = hi_; key[j] = lo_; } while (0)
        CE(0, 1); CE(2, 3); CE(0, 2); CE(1, 3); CE(1, 2);
        unsigned cv = 0;
#pragma nounroll
        for (int it = 0; it < 8; ++it) {
          const unsigned m = wave_max(key[0]);
          const bool win = key[0] == m;
          key[0] = win ? key[1] : key[0]; key[1] = win ? key[2] : key[1]; key[2] = win ? key[3] : key[2]; key[3] = win ? 0u : key[3];
          cv = sl == it ? m : cv;
        }
        if (l < 8) cand[w * 8 + l] = cv;
        LBAR();
      }
      {
        const unsigned c = cand[w * 8 + sl];
        const u4 c0 = *(const u4*)(cand + rr * 8), c1 = *(const u4*)(cand + rr * 8 + 4);
        unsigned r = 0;
#pragma unroll
        for (int e = 0; e < 4; ++e) r += (c0[e] > c ? 1u : 0u) + (c1[e] > c ? 1u : 0u);
        r += (unsigned)__builtin_amdgcn_update_dpp(0, (int)r, 0x128, 0xf, 0xf, false);
        auto a16 = __builtin_amdgcn_permlane16_swap(r, r, false, false); r = a16[0] + a16[1];
        auto a32 = __builtin_amdgcn_permlane32_swap(r, r, false, false); r = a32[0] + a32[1];
        ST64(((u2){c, tlo}), r < 8u ? WS_MSG + rr * 256 + 64 + r * 8 : WS_OFF);
        STMP(29); }
    }
    if (master && w != 4) lds_wait<4>((LAS unsigned*)(lds + L_CNT) + 3, 1);
    if (master && w == 4) {
      PACE(MD2);
      bool ok = false;
#pragma nounroll
      for (unsigned it = 0; it < SPIN_CAP; ++it) {
        if (!ok) {
          u4 f0 = LD128(WS_FLAG + l * 32), f1 = LD128(WS_FLAG + l * 32 + 16);
          ok = f0[0] == tlo && f0[1] == thi && f0[2] == tlo && f0[3] == thi && f1[0] == tlo && f1[1] == thi && f1[2] == tlo && f1[3] == thi;
        }
        if (all64(ok)) break;
        __builtin_amdgcn_s_sleep(8);
        CLOB();
      }
      CLOB();
      if (l < 8) ST64(tgv, WS_MSG + l * 256);
      if (l == 0) ST64(tgv, WS_EP);
      STMP(30);
      lds_arrive((LAS unsigned*)(lds + L_CNT) + 3);
    }
    GL2(0, !master, G0A, 8);
    GL(1, !master);
    { WO_SETUP(); (void)vX; WOR(w, 32, 8); }
    GT(0, 12);
    GL(2, !half);
    GT(1, 12);
    u2 f;
    { const unsigned m_ = msg; ALD64(f, m_); }
    GL(3, !half);
    GT(2, 9);
    WAITV(8); PIN(f);
    if (!KEYOK(f)) {
#pragma nounroll
      for (unsigned it = 0; it < SPIN_CAP; ++it) {
        CLOB();
        f = LD64(msg);
        if (KEYOK(f)) break;
        __builtin_amdgcn_s_sleep(16);
      }
    }
    CLOB();
    STMP(9);
    if (w == 0) {
      if (l < 8) ((unsigned*)(lds + L_TAG + 16))[l] = f[0];
      if (l == 8) *(u2*)(lds + L_TAG) = tgv;
    }
    u4 df[2][8];
    u16* hb = (u16*)(lds + L_KX);
#define DT(b) do { _Pragma("unroll") for (int c_ = 0; c_ < 8; ++c_) PIN(dw[b][c_]); _Pragma("unroll") for (int c_ = 0; c_ < 8; ++c_) { TRFRAG(df[b][c_], dw[b], c_); PIN(df[b][c_]); } SBAR(); } while (0)
    {
      const unsigned key_ = (unsigned)__builtin_amdgcn_update_dpp((int)f[0], (int)f[0], 0x128, 0xf, 0xc, false);
      const int idx = (0xFFFF - (int)(key_ & 0xFFFF)) & 2047;
      u4 r0, r1, msx;
      { const u4 rsD = mkrs((const char*)wd + (size_t)hc * 64 * 2048 + w * 256, jm == 7 ? 0 : 63 * 2048 + 256);
        const unsigned xo = (idx * D + 128 * w + (help ? 64 : 0) + 32 * (np >> 3) + 8 * g) * 2, mo = WS_MS + idx * 16, o_ = g * 8 * 2048 + np * 16;
        asm volatile("buffer_load_dwordx4 %0, %2, %3, 0 offen\n buffer_load_dwordx4 %1, %2, %3, 0 offen offset:128" : "=&v"(r0), "=&v"(r1) : "v"(xo), "s"(rsR) : "memory");
        ALD128(msx, mo);
#pragma unroll
        for (int i = 0; i < 16; ++i)
          asm volatile("buffer_load_dwordx4 %0, %1, %2, %3 offen offset:%4 nt" : "=v"(dw[i >> 3][i & 7]) : "v"(o_), "s"(rsD), "s"((unsigned)((i >> 3) * 65536 + (i & 6) * 2048)), "i"((i & 1) * 2048) : "memory"); }
      WAITV(16); PIN(r0); PIN(r1);
#pragma unroll
      for (int i = 0; i < 8; ++i) PIN(gw[3][i]);
      f4 acc[4];
#pragma unroll
      for (int c2 = 0; c2 < 4; ++c2) acc[c2] = (f4)(0.f);
      PIN(msx);
      const float m = hi(msx[0]), rs = U2F(msx[2]);
#pragma unroll
      for (int b2 = 0; b2 < 2; ++b2) {
        if (b2 && half) break;
        u4 xl[2], xh[2];
#pragma unroll
        for (int e = 0; e < 4; ++e) {
          const unsigned r = b2 ? r1[e] : r0[e];
          const unsigned v = pk2((lo(r) - m) * rs, (hi(r) - m) * rs);
          const unsigned o = (unsigned)__builtin_amdgcn_update_dpp(0, (int)v, 0x128, 0xf, 0xf, false);
          xl[0][e] = np < 8 ? v : 0u; xh[0][e] = np < 8 ? 0u : o; xl[1][e] = np < 8 ? o : 0u; xh[1][e] = np < 8 ? 0u : v;
        }
#pragma unroll
        for (int it = 0; it < 2; ++it)
#pragma unroll
          for (int c = 0; c < 8; ++c) {
            if (2 * b2 + it == 3) { TRFRAG(gf[3][c], gw[3], c); }
            acc[c >> 1] = MFMA16(BC8((c & 1) ? xh[it] : xl[it]), BC8(gf[2 * b2 + it][c]), acc[c >> 1]);
          }
        SBAR();
      }
      STMP(12);
      red += 3072;
#pragma unroll
      for (int c2 = 0; c2 < 4; ++c2) *(f4*)(red + (w * 4 + c2) * 256 + l * 4) = acc[c2];
      SBAR();
      if (WM) { WO_SETUP(); (void)vX; WOR(32 + w, 64, 8); }
      WAITV(WM ? 12 : 8);
      DT(0);
      LBAR();
      f2 sv = *(const f2*)(red + (w >> 1) * 256 + l * 4 + (w & 1) * 2);
#pragma unroll
      for (int i = 1; i < 8; ++i) sv += *(const f2*)(red + (i * 4 + (w >> 1)) * 256 + l * 4 + (w & 1) * 2);
      const unsigned gp = WS_GP + (xcd * 512 + w * 64 + l) * 16;
      if (jm == 7) ST128(((u4){F2U(sv[0]), tlo, F2U(sv[1]), thi}), help ? gp : WS_OFF);
      else {
      if (half) {
        u4 pv;
#pragma nounroll
        for (unsigned it = 0; it < SPIN_CAP; ++it) {
          pv = LD128(gp);
          if (all64(GBAD(pv) == 0u)) break;
          __builtin_amdgcn_s_sleep(8);
          CLOB();
        }
        sv += (f2){U2F(pv[0]), U2F(pv[2])};
      }
#pragma unroll
      for (int e = 0; e < 2; ++e) {
        const float gg = rbf(sv[e]), u1 = DPPF(gg, 0x128, 0xf);
        const float hv = rbf(gg * __builtin_amdgcn_rcpf(1.f + __builtin_amdgcn_exp2f(gg * -1.44269504f))) * u1;
        if (np < 8) hb[(4 * (g & 1) + 2 * (w & 1) + e) * 64 + 8 * np + 2 * (w >> 1) + (g >> 1)] = (u16)(pk2(hv, hv) & 0xFFFFu);
      }
      }
      SBAR();
      WAITV(WM ? 4 : 0);
      DT(1);
    }
    LBAR();
    STMP(13);
    u4 hu = LD128(w == 7 ? WS_HU + xcd * 128 : WS_OFF);
    SBAR();
    { WO_SETUP(); (void)vX;
      if (!WM) { WOR(32 + w, 64, 8); }
      if (w < 4) { XOWN(); WOR(64 + w, 88, 4); WAITV(6); OINIT(); } else { WOR(84 + w, 128, 4); } }
    u4 yo[4];
    if (jm < 7) {
      f4 acd[4];
#pragma unroll
      for (int c = 0; c < 4; ++c) acd[c] = (f4)(0.f);
#pragma unroll
      for (int fb = 0; fb < 2; ++fb) {
        const u4 ha = *(const u4*)(hb + sl * 64 + 32 * fb + 8 * g);
        u4 hl, hh;
#pragma unroll
        for (int e = 0; e < 4; ++e) { hl[e] = np < 8 ? ha[e] : 0u; hh[e] = np < 8 ? 0u : ha[e]; }
#pragma unroll
        for (int c = 0; c < 4; ++c) acd[c] = MFMA16(BC8(hh), BC8(df[fb][c + 4]), MFMA16(BC8(hl), BC8(df[fb][c]), acd[c]));
      }
#pragma unroll
      for (int e = 0; e < 4; ++e) { yo[e] = tag4((f4){acd[0][e], acd[1][e], acd[2][e], acd[3][e]}, tlo); PIN(yo[e]); }
    }
    __builtin_amdgcn_s_waitcnt(0x0F70); CLOB(); PIN(hu);
#pragma unroll
    for (int e = 0; e < 4; ++e) ST128(yo[e], jm < 7 ? WS_YP + (hc * 8 + 4 * (g & 1) + e) * 4096 + w * 512 + (g >> 1) * 256 + np * 16 : WS_OFF);
    STMP(14);
    STMP(21);
    if (w == 7) {
#pragma nounroll
      for (unsigned it = 0; it < SPIN_CAP && !all64(hu[0] == tlo && hu[1] == thi); ++it) {
        __builtin_amdgcn_s_sleep(8);
        CLOB();
        hu = LD128(WS_HU + xcd * 128);
      }
      if (l == 0) *(unsigned*)(lds + L_TAG + 8) = hu[2];
#pragma nounroll
      for (unsigned it = 0; PO && it < SPIN_CAP && !MSGOK(f); ++it) { f = LD64(msg); CLOB(); }
    }
    LBAR();
    tpoll = RFL(*(const unsigned*)(lds + L_TAG + 8));
    { const unsigned now = (unsigned)__builtin_amdgcn_s_memrealtime(); if (tpoll - now > PDK) tpoll = now; }
    CPE(4);
    STMP(20);
  }

  LANE();
  LAS unsigned* cnt = (LAS unsigned*)(lds + L_CNT);
  if (ow >= 4) {
#define YD 16
    lds_wait<4>(cnt + 4, 1);
    PACE(YD);
    const unsigned mykey = ((const unsigned*)(lds + L_TAG + 16))[sl], tlo = RFL(*(const unsigned*)(lds + L_TAG));
#pragma unroll 1
    for (int i = ow - 4; i < 8; i += 4) {
      const unsigned kk = (unsigned)__builtin_amdgcn_readlane((int)mykey, i);
      const int ti = 0xFFFF - (int)(kk & 0xFFFF);
      if ((ti >> 7) != mb2) continue;
      const unsigned yb = WS_YP + (g * 14 * 8 + i) * 4096 + (nb >> 1) * 512 + (np >> 3) * 256 + ((nb & 1) * 8 + sl) * 16;
      f4 y;
#pragma nounroll
      for (unsigned it = 0; it < SPIN_CAP; ++it) {
        u4 yv[14];
#pragma unroll
        for (int k = 0; k < 14; ++k) yv[k] = LD128(yb + k * 32768);
        SBAR();
        unsigned bad = 0;
        y = untag4(yv[0], tlo, bad);
#pragma unroll
        for (int k = 1; k < 14; ++k) { const f4 t = untag4(yv[k], tlo, bad); y = (f4){add1(y[0], t[0]), add1(y[1], t[1]), add1(y[2], t[2]), add1(y[3], t[3])}; }
        if (tagok(bad)) break;
        __builtin_amdgcn_s_sleep(32);
        CLOB();
      }
#pragma unroll
      for (int e = 0; e < 4; ++e) {
        auto a16 = __builtin_amdgcn_permlane16_swap(F2U(y[e]), F2U(y[e]), false, false);
        y[e] = add1(U2F(a16[0]), U2F(a16[1]));
        auto a32 = __builtin_amdgcn_permlane32_swap(F2U(y[e]), F2U(y[e]), false, false);
        y[e] = add1(U2F(a32[0]), U2F(a32[1]));
      }
      const unsigned mono = kk >> 16;
      const float rv = U2F(((mono & 0x8000u) ? (mono & 0x7FFFu) : (~mono & 0xFFFFu)) << 16);
      const float sg = rbf(__builtin_amdgcn_rcpf(1.f + __builtin_amdgcn_exp2f(rv * -1.44269504f)));
#pragma unroll
      for (int e = 0; e < 4; ++e) y[e] = rbf(mul1(rbf(y[e]), sg));
      if (g == 0) *(f4*)(lds + L_YS + i * 256 + sl * 32 + (np >> 3) * 16) = y;
    }
    if (ow == 4) ((unsigned*)(lds + L_YS + 2048))[l] = 0u;
    lds_arrive(cnt);
    CPE(5);
    STMP(32);
    STDUMP();
    return;
  }
  const u2 tg = *(const u2*)(lds + L_TAG);
  const int trow = 0xFFFF - (int)(((const unsigned*)(lds + L_TAG + 16))[sl] & 0xFFFFu);
  const unsigned hit = (unsigned)__builtin_amdgcn_ballot_w64((trow >> 5) == mb2 * 4 + ow) & 0xFFu;
  unsigned ya[2] = {8u, 8u}, xw[4], xr_ = ldsb + L_XT + (ow * 32 + rr) * 128 + ((sl ^ rr) * 16), oo[4], cA = ldsb + L_CNT;
  if (hit) {
#pragma unroll 1
    for (int i = 0; i < 8; ++i) { const int row = __builtin_amdgcn_readlane(trow, i) - mb2 * 128 - ow * 32 - np; ya[0] = row == 0 ? i : ya[0]; ya[1] = row == 16 ? i : ya[1]; }
  }
#pragma unroll
  for (int i = 0; i < 4; ++i) { xw[i] = ldsb + L_XT + (ow * 32 + np) * 128 + (g & 1) * 8 + (((2 * i + (g >> 1)) ^ sl) * 16);
    oo[i] = (mb2 * 128 + ow * 32 + 8 * i + rr) * 2048 + nb * 128 + sl * 16; PIN(xw[i]); PIN(oo[i]); }
  ya[0] = ldsb + L_YS + ya[0] * 256 + g * 16; ya[1] = ldsb + L_YS + ya[1] * 256 + g * 16;
  PIN(ya[0]); PIN(ya[1]); PIN(xr_); PIN(cA);
  const __amdgpu_buffer_rsrc_t ors = __builtin_amdgcn_make_buffer_rsrc(out, 0, S * D * 2, 0x00020000);
  unsigned woad[2][4];
  { const int q = np >> 2, p = l & 3, row = 4 * g + q;
#pragma unroll
    for (int ct = 0; ct < 4; ++ct) { woad[0][ct] = ldsb + L_WO + row * 128 + ((ct ^ ((row >> 1) & 3)) * 32) + p * 8; woad[1][ct] = woad[0][ct] + 65536; } }
  s4 b[2][16];
#define K3RD(hc, i) RDTR(b[(hc) & 1][i], woad[((hc) >> 2) & 1][((i) >> 1) & 3], ((((hc) & 3) * 4 + ((hc) >> 3) * 2 + ((i) >> 3)) * 4096 + ((i) & 1) * 2048))
  K3RD(0, 0); K3RD(0, 1); K3RD(0, 2); K3RD(0, 3); K3RD(0, 4); K3RD(0, 5); K3RD(0, 6); K3RD(0, 7);
  K3RD(0, 8); K3RD(0, 9); K3RD(0, 10); K3RD(0, 11); K3RD(0, 12); K3RD(0, 13); K3RD(0, 14); K3RD(0, 15);
  const unsigned flg = l < 32 ? WS_OFL + (mb2 * 32 + (np ^ (hx >> 1)) * 2 + g) * 128 : WS_OFF;
#pragma nounroll
  while ((int)((unsigned)__builtin_amdgcn_s_memrealtime() - tpoll) < 0) __builtin_amdgcn_s_sleep(1);
  STMP(26);
  unsigned rdy = 0;
#define OPOLL(MASK, SL) _Pragma("nounroll") for (unsigned it = 0; it < SPIN_CAP && (rdy & (MASK)) != (MASK); ++it) { \
    u2 f_ = LD64((((PM ? (MASK) & ~rdy : ~rdy) >> (l & 31)) & 1u) ? flg : WS_OFF); \
    rdy |= (unsigned)__builtin_amdgcn_ballot_w64(f_[0] == tg[0] && f_[1] == tg[1]); \
    if ((SL) && rdy == 0) __builtin_amdgcn_s_sleep(SL); \
    CLOB(); }
#define OG 16
  OPOLL((1u << (OG / 2)) - 1, OSL);
  CLOB();
  STMP(6);
#define OPF 3
#define OW0 6
#define OW 12
  u4 a[32]; u2 fl;
  const unsigned abase = WS_OF + (mb2 * 8 + ow * 2) * 32768 + l * 16, abase1 = abase + 32768;
#define OL1(f) a[(f) & 31] = __builtin_amdgcn_raw_buffer_load_b128(wrs, (((f) & 1) ? abase1 : abase) + (((f) >> 5) * 2 + (((f) >> 1) & 1)) * 1024, ((((f) >> 2) & 7) * 4096) ^ (hx * 1024), 16)
  unsigned mxo;
#define OMX(hc, k) mxo = mx3((k) ? mxo : 0u, a[(hc) * 4 + ((k) >> 1)][((k) & 1) * 2], a[(hc) * 4 + ((k) >> 1)][((k) & 1) * 2 + 1])
#define OFIX(hc) do { if (__builtin_expect(!clean(mxo), 0)) { NFIX(); _Pragma("nounroll") for (unsigned it = 0; it < SPIN_CAP; ++it) { CLOB(); \
      OL1((hc) * 4); OL1((hc) * 4 + 1); OL1((hc) * 4 + 2); OL1((hc) * 4 + 3); \
      if (clean(mx4(mx4(mx4(mx4(0u, a[(hc) * 4]), a[(hc) * 4 + 1]), a[(hc) * 4 + 2]), a[(hc) * 4 + 3]))) break; } } } while (0)
#define NFIX()
#define OMK(f) ((unsigned)((1ull << (((f) + OG) / 2)) - 1))
#define OLD(f) do { if ((f) < 64) { if ((f) && !((f) % OG) && (rdy & OMK(f)) != OMK(f)) { rdy |= (unsigned)__builtin_amdgcn_ballot_w64(fl[0] == tg[0] && fl[1] == tg[1]); OPOLL(OMK(f), 0); CLOB(); } \
    OL1(f); \
    if (((f) % OG) == OPF && (f) < 64 - OG) fl = LD64((((PM == 1 ? OMK((f) - OPF + OG) & ~rdy : ~rdy) >> (l & 31)) & 1u) ? flg : WS_OFF); } } while (0)
#pragma unroll
  for (int f = 0; f < OW0; ++f) { OLD(f); SBAR(); }
  if (ow == 0) { WAITL(0); lds_arrive(cnt + 4); WAITL(0); }
#define OHI(c) ((OW0 + 2 * (c)) < (OW + (c)) ? (OW0 + 2 * (c)) : (OW + (c)))
#define K3R(i) (((i) ^ ((i) >> 1)) & 1)
#define K3C(hc, i) ((hc) * 4 + ((i) >> 2))
#define K3M(hc, i) do { if (!((i) & 1)) WAITL((hc) < 15 ? 14 : 14 - (i)); SBAR(); \
    oc[K3R(i)][((i) >> 1) & 3] = MFMA16(CAT(b[(hc) & 1][(i) & ~1], b[(hc) & 1][(i) | 1]), BC8(a[((hc) * 4 + ((i) >> 3) * 2 + K3R(i)) & 31]), oc[K3R(i)][((i) >> 1) & 3]); SBAR(); \
    if ((hc) < 15) { K3RD((hc) + 1, i); } \
    if (PO && (hc) < 7 && (i) >= 6 && (i) < 14) { OMX(((hc) + 1) & 7, ((i) - 6) & 7); PIN(mxo); } \
    if (PO && (hc) < 7 && (i) == 15) { SBAR(); OFIX(((hc) + 1) & 7); } \
    if (((i) & 3) == 3) { OLD(OHI(K3C(hc, i) + 1) - 1); } else if (((i) & 3) == 1 && OHI(K3C(hc, i) + 1) - OHI(K3C(hc, i)) == 2) { OLD(OHI(K3C(hc, i))); } \
    SBAR(); } while (0)
#define K3H(hc) do { K3M(hc, 0); K3M(hc, 1); K3M(hc, 2); K3M(hc, 3); K3M(hc, 4); K3M(hc, 5); K3M(hc, 6); K3M(hc, 7); \
    K3M(hc, 8); K3M(hc, 9); K3M(hc, 10); K3M(hc, 11); K3M(hc, 12); K3M(hc, 13); K3M(hc, 14); K3M(hc, 15); } while (0)
  if (PO) { OMX(0, 0); OMX(0, 1); OMX(0, 2); OMX(0, 3); OMX(0, 4); OMX(0, 5); OMX(0, 6); OMX(0, 7); OFIX(0); }
  STMP(28);
  K3H(0); STMP(27); K3H(1); K3H(2); K3H(3); K3H(4); K3H(5); K3H(6); K3H(7); STMP(31); STMC(42); K3H(8); K3H(9); K3H(10); K3H(11); K3H(12); K3H(13); K3H(14);
  f4 yv[2][4]; unsigned yf = 4u;
#define K3L(r, n) do { oc[r][(n) & 3] = MFMA16(CAT(b[1][2 * (n)], b[1][2 * (n) + 1]), BC8(a[28 + ((n) >> 2) * 2 + (r)]), oc[r][(n) & 3]); SBAR(); } while (0)
#define YRD() do { _Pragma("unroll") for (int i = 0; i < 8; ++i) RD128(yv[i >> 2][i & 3], ya[i >> 2], (i & 3) * 64); } while (0)
#define OWR(r, ct) do { const u2 v_ = (u2){pk2(oc[r][ct][0], oc[r][ct][1]), pk2(oc[r][ct][2], oc[r][ct][3])}; \
    asm volatile("ds_write_b64 %0, %1 offset:%2" :: "v"(xw[ct]), "v"(v_), "i"((r) * 2048) : "memory"); SBAR(); } while (0)
  u4 ov[4];
  WAITL(8); SBAR(); K3L(0, 0); K3L(0, 1); K3L(0, 2); K3L(0, 3); WAITL(0);
  if (hit) { RD32(yf, cA, 0); YRD(); }
  SBAR();
  K3L(0, 4); K3L(0, 5); K3L(0, 6); K3L(0, 7); K3L(1, 0); K3L(1, 1); K3L(1, 2); K3L(1, 3);
  if (hit) {
    K3L(1, 4); K3L(1, 5); K3L(1, 6); K3L(1, 7);
    WAITL(0); PIN(yf);
    if (RFL(yf) < 4u) { lds_wait<0>(cnt, 4); YRD(); WAITL(0); }
#pragma unroll
    for (int i = 0; i < 8; ++i) { PIN(yv[i >> 2][i & 3]); oc[i >> 2][i & 3] += yv[i >> 2][i & 3]; }
    OWR(0, 0); OWR(0, 1); OWR(0, 2); OWR(0, 3);
  } else {
    K3L(1, 4); OWR(0, 0); K3L(1, 5); OWR(0, 1); K3L(1, 6); OWR(0, 2); K3L(1, 7); OWR(0, 3);
  }
  STMP(7); STMC(43);
  RD128(ov[0], xr_, 0); RD128(ov[1], xr_, 1024);
  OWR(1, 0); OWR(1, 1); OWR(1, 2); OWR(1, 3);
  RD128(ov[2], xr_, 2048); RD128(ov[3], xr_, 3072);
  WAITL(6); PIN(ov[0]); PIN(ov[1]);
  __builtin_amdgcn_raw_buffer_store_b128(ov[0], ors, oo[0], 0, 16); __builtin_amdgcn_raw_buffer_store_b128(ov[1], ors, oo[1], 0, 16);
  SBAR();
  WAITL(0); PIN(ov[2]); PIN(ov[3]);
  __builtin_amdgcn_raw_buffer_store_b128(ov[2], ors, oo[2], 0, 16); __builtin_amdgcn_raw_buffer_store_b128(ov[3], ors, oo[3], 0, 16);
  CPE(6);
  STMP(16);
  STDUMP();
}

extern "C" void kernel_launch(void* const* d_in, const int* in_sizes, int n_in, void* d_out, int out_size,
                              void* d_ws, size_t ws_size, hipStream_t stream) {
  const u16* x = (const u16*)d_in[0];
  const u16* wqkv = (const u16*)d_in[1];
  const u16* wo = (const u16*)d_in[2];
  const u16* wr = (const u16*)d_in[4];
  const u16* wg = (const u16*)d_in[5];
  const u16* wu = (const u16*)d_in[6];
  const u16* wd = (const u16*)d_in[7];
  (void)hipFuncSetAttribute((const void*)fused, hipFuncAttributeMaxDynamicSharedMemorySize, K_LDS);
  fused<<<256, 512, K_LDS, stream>>>(x, wqkv, wo, wr, wg, wu, wd, (char*)d_ws, (u16*)d_out);
}
```

```cpp
#include <hip/hip_runtime.h>
#include <hip/hip_bf16.h>
#include <stdint.h>

typedef unsigned short u16;
typedef unsigned long long u64;
typedef short s4 __attribute__((ext_vector_type(4)));
typedef short s8 __attribute__((ext_vector_type(8)));
typedef float f4 __attribute__((ext_vector_type(4)));
typedef float f2 __attribute__((ext_vector_type(2)));
typedef unsigned u4 __attribute__((ext_vector_type(4)));
typedef unsigned u2 __attribute__((ext_vector_type(2)));
typedef __bf16 bf2 __attribute__((ext_vector_type(2)));
#define LAS __attribute__((address_space(3)))
#define DEV __device__ __forceinline__

DEV float U2F(unsigned u) { return __builtin_bit_cast(float, u); }
DEV unsigned F2U(float f) { return __builtin_bit_cast(unsigned, f); }
DEV float I2F(int u) { return __builtin_bit_cast(float, u); }
DEV int F2I(float f) { return __builtin_bit_cast(int, f); }

constexpr int S = 2048, D = 1024, F = 3584;

constexpr unsigned WS_EP = 0;
constexpr unsigned WS_FLAG = 4096;
constexpr unsigned WS_MSG = 6144;
constexpr unsigned WS_HU = 8192;
constexpr unsigned WS_MS = 32768;
constexpr unsigned WS_CS = 256u << 10;
constexpr unsigned WS_GP = 512u << 10;
constexpr unsigned WS_YP = 1280u << 10;
constexpr unsigned WS_KVP = 3328u << 10;
constexpr unsigned WS_OF = 6u << 20;
constexpr unsigned WS_OFL = 10u << 20;
constexpr unsigned WS_END = (10u << 20) + (128u << 10);
constexpr unsigned WS_OFF = 0x7FFF0000u;
constexpr unsigned KVS = 2304;
constexpr unsigned POISON = 0xFFFFFFFFu;

DEV unsigned pk2(float a, float b) { bf2 v; v[0] = (__bf16)a; v[1] = (__bf16)b; return __builtin_bit_cast(unsigned, v); }
DEV float lo(unsigned u) { return U2F(u << 16); }
DEV float hi(unsigned u) { return U2F(u & 0xFFFF0000u); }
DEV float rbf(float a) { return lo(pk2(a, a)); }
DEV float add1(float a, float b) { float r; asm("v_add_f32 %0, %1, %2" : "=v"(r) : "v"(a), "v"(b)); return r; }
DEV float mul1(float a, float b) { float r; asm("v_mul_f32 %0, %1, %2" : "=v"(r) : "v"(a), "v"(b)); return r; }
DEV unsigned san(unsigned u) { return min(u, 0xFFFFFFFEu); }
DEV u4 san4(u4 v) { v[0] = san(v[0]); v[1] = san(v[1]); v[2] = san(v[2]); v[3] = san(v[3]); return v; }
DEV unsigned mx3(unsigned a, unsigned b, unsigned c) { return max(max(a, b), c); }
DEV unsigned mx4(unsigned m, u4 v) { return mx3(mx3(m, v[0], v[1]), v[2], v[3]); }
DEV u4 tag4(f4 v, unsigned t) { u4 r; _Pragma("unroll") for (int e = 0; e < 4; ++e) r[e] = (F2U(v[e]) & 0xFFFFFF00u) | ((t >> (8 * e)) & 0xFFu); return r; }
DEV f4 untag4(u4 v, unsigned t, unsigned& bad) {
  f4 r; _Pragma("unroll") for (int e = 0; e < 4; ++e) { const unsigned u = v[e] ^ ((t >> (8 * e)) & 0xFFu); bad |= u; r[e] = U2F(u); } return r; }
DEV bool tagok(unsigned bad) { return __builtin_amdgcn_ballot_w64((bad & 0xFFu) != 0u) == 0ull; }
DEV bool clean(unsigned m) { return __builtin_amdgcn_ballot_w64(m == POISON) == 0ull; }
DEV bool all64(bool c) { return __builtin_amdgcn_ballot_w64(!c) == 0ull; }
DEV float phi1(float a) { return __builtin_fmaf(fmaxf(a, 1.44269504e-6f), 0.693147181f, __builtin_amdgcn_fmed3f(__builtin_amdgcn_exp2f(a), 0.f, 1.f)); }
DEV unsigned phi2(f2 a) {
  const f2 e = (f2){__builtin_amdgcn_fmed3f(__builtin_amdgcn_exp2f(a[0]), 0.f, 1.f), __builtin_amdgcn_fmed3f(__builtin_amdgcn_exp2f(a[1]), 0.f, 1.f)};
  const f2 r = __builtin_elementwise_fma((f2){fmaxf(a[0], 1.44269504e-6f), fmaxf(a[1], 1.44269504e-6f)}, (f2)(0.693147181f), e);
  return pk2(r[0], r[1]);
}
#define RD128(dst, addr, OFF) asm volatile("ds_read_b128 %0, %1 offset:%2" : "=v"(dst) : "v"(addr), "i"(OFF) : "memory")
#define RD64(dst, addr, OFF) asm volatile("ds_read_b64 %0, %1 offset:%2" : "=v"(dst) : "v"(addr), "i"(OFF) : "memory")
#define RD32(dst, addr, OFF) asm volatile("ds_read_b32 %0, %1 offset:%2" : "=v"(dst) : "v"(addr), "i"(OFF) : "memory")
#define RDTR(dst, addr, OFF) asm volatile("ds_read_b64_tr_b16 %0, %1 offset:%2" : "=v"(dst) : "v"(addr), "i"(OFF) : "memory")
#define SBAR() __builtin_amdgcn_sched_barrier(0)
#define CAT(x, y) __builtin_shufflevector(x, y, 0, 1, 2, 3, 4, 5, 6, 7)
#define WAITV(n) asm volatile("s_waitcnt vmcnt(%0)" ::"i"(n) : "memory")
#define WAITL(n) asm volatile("s_waitcnt lgkmcnt(%0)" ::"i"(n) : "memory")
#define CLOB() asm volatile("" ::: "memory")
#define BAR() asm volatile("s_barrier" ::: "memory")
#define LBAR() asm volatile("s_waitcnt lgkmcnt(0)\n s_barrier" ::: "memory")
#define PIN(v) asm volatile("" : "+v"(v))
#define PACE(n) do { SBAR(); __builtin_amdgcn_s_sleep(n); SBAR(); } while (0)
#define RFL(v) ((unsigned)__builtin_amdgcn_readfirstlane(v))
DEV const char* uptr(const char* p) {
  u64 v = (u64)p;
  return (const char*)(((u64)RFL((unsigned)(v >> 32)) << 32) | RFL((unsigned)v));
}
DEV u4 mkrs(const char* p, unsigned bytes) {
  const u64 v = (u64)uptr(p);
  u4 r; r[0] = (unsigned)v; r[1] = (unsigned)(v >> 32) & 0xFFFFu; r[2] = RFL(bytes); r[3] = 0x00020000u;
  return r;
}
#define BLDS(voff, rs, so, OFF) asm volatile("buffer_load_dwordx4 %0, %1, %2 offen offset:%3 lds" ::"v"(voff), "s"(rs), "s"(so), "i"(OFF) : "memory")
#define MFMA16(a, b, c) __builtin_amdgcn_mfma_f32_16x16x32_bf16(a, b, c, 0, 0, 0)
#define MFMAH(a, b, c) __builtin_amdgcn_mfma_f32_16x16x16bf16_1k(__builtin_bit_cast(s4, (u2){(a), 0u}), __builtin_bit_cast(s4, (u2){(b), 0u}), c, 0, 0, 0)
#define DOT2(a, b, c) __builtin_amdgcn_fdot2_f32_bf16(__builtin_bit_cast(bf2, (unsigned)(a)), __builtin_bit_cast(bf2, (unsigned)(b)), c, false)
#define BC8(v) __builtin_bit_cast(s8, v)
#define LD128(off) __builtin_amdgcn_raw_buffer_load_b128(wrs, (off), 0, 16)
#define LD64(off) __builtin_amdgcn_raw_buffer_load_b64(wrs, (off), 0, 16)
#define LD32(off) __builtin_amdgcn_raw_buffer_load_b32(wrs, (off), 0, 16)
#define ST128(v, off) __builtin_amdgcn_raw_buffer_store_b128((v), wrs, (off), 0, 16)
#define ST64(v, off) __builtin_amdgcn_raw_buffer_store_b64((v), wrs, (off), 0, 16)
#define ST32(v, off) __builtin_amdgcn_raw_buffer_store_b32((v), wrs, (off), 0, 16)
#define ALD128(dst, off) asm volatile("buffer_load_dwordx4 %0, %1, %2, 0 offen sc1" : "=v"(dst) : "v"(off), "s"(rsW) : "memory")
#define ALD64(dst, off) asm volatile("buffer_load_dwordx2 %0, %1, %2, 0 offen sc1" : "=v"(dst) : "v"(off), "s"(rsW) : "memory")
#define ALD32(dst, off) asm volatile("buffer_load_dword %0, %1, %2, 0 offen sc1" : "=v"(dst) : "v"(off), "s"(rsW) : "memory")
constexpr unsigned SPIN_CAP = 1u << 20;

DEV void lds_arrive(LAS unsigned* c) {
  asm volatile("s_mov_b64 exec, 1\n ds_add_u32 %0, %1\n s_mov_b64 exec, -1" ::"v"((unsigned)(size_t)c), "v"(1u) : "memory");
}
template <int SL> DEV void lds_wait(LAS unsigned* c, unsigned target) {
#pragma nounroll
  while (RFL(__hip_atomic_load(c, __ATOMIC_RELAXED, __HIP_MEMORY_SCOPE_WORKGROUP)) < target) { if (SL) __builtin_amdgcn_s_sleep(SL); }
  CLOB();
}

#define STMP(idx)
#define STMC(idx)
#define STDUMP()
#define DPPF(v, ctrl, rm) I2F(__builtin_amdgcn_update_dpp(0, F2I(v), ctrl, rm, 0xf, false))
DEV unsigned wave_max(unsigned v) {
  v = max(v, (unsigned)__builtin_amdgcn_update_dpp(0, (int)v, 0x111, 0xf, 0xf, false));
  v = max(v, (unsigned)__builtin_amdgcn_update_dpp(0, (int)v, 0x112, 0xf, 0xf, false));
  v = max(v, (unsigned)__builtin_amdgcn_update_dpp(0, (int)v, 0x114, 0xf, 0xf, false));
  v = max(v, (unsigned)__builtin_amdgcn_update_dpp(0, (int)v, 0x118, 0xf, 0xf, false));
  v = max(v, (unsigned)__builtin_amdgcn_update_dpp(0, (int)v, 0x142, 0xa, 0xf, false));
  v = max(v, (unsigned)__builtin_amdgcn_update_dpp(0, (int)v, 0x143, 0xc, 0xf, false));
  return (unsigned)__builtin_amdgcn_readlane((int)v, 63);
}

constexpr int SLOT = 49152, L_B = 32768;
constexpr int L_EX = SLOT;
constexpr int L_ROWM = 159744;
constexpr int L_ROWS = L_ROWM + 1024;
constexpr int L_COL = L_ROWS + 1024;
constexpr int L_TAG = L_COL + 512;
constexpr int L_CNT = L_TAG + 64;
constexpr int L_TOP = L_CNT + 64;
constexpr int L_XT = 0;
constexpr int L_KX = 16384;
constexpr int L_ZX = L_KX + 8192;
constexpr int L_YS = L_ZX + 256;
constexpr int L_WO = 28672;
constexpr int K_LDS = 163840;

#define TRFRAG(f, W, c) do { _Pragma("unroll") for (int q_ = 0; q_ < 4; ++q_) { unsigned e_ = W[2 * q_][(c) >> 1], o_ = W[2 * q_ + 1][(c) >> 1]; \
    f[q_] = ((c) & 1) ? __builtin_amdgcn_perm(o_, e_, 0x07060302u) : __builtin_amdgcn_perm(o_, e_, 0x05040100u); } } while (0)
constexpr u64 TAG_STEP = 0x9E3779B97F4A7C15ull;

struct FW { s8 a[8]; s4 b[8]; };
#define RA(G, s, u, i) RD128(G.a[i], aad[((s) % 3) == 2][u], (((s) % 3) & 1) * SLOT + (i) * 2048)
#define RB(G, s, u, n) RDTR(G.b[n], bad[((s) % 3) == 2][((n) >> 1) & 1], (((s) % 3) & 1) * SLOT + ((n) >> 2) * 8192 + (u) * 4096 + ((n) & 1) * 512)
#define FB(G, j) CAT(G.b[2 * (j)], G.b[2 * (j) + 1])
#define RW(G, s, u, n) do { if ((n) < 8) RB(G, s, u, (n) & 7); else RA(G, s, u, ((n) - 8) & 7); SBAR(); } while (0)
#define SNK(n) ((((n) >> 2) & 1) ? 3 - ((n) & 3) : (n) & 3)
#define MW(G, n) do { if (TRN) c[(n) >> 2][SNK(n)] = MFMA16(FB(G, SNK(n)), G.a[(n) >> 2], c[(n) >> 2][SNK(n)]); \
    else c[(n) >> 2][SNK(n)] = MFMA16(G.a[(n) >> 2], FB(G, SNK(n)), c[(n) >> 2][SNK(n)]); SBAR(); } while (0)
#define MFA(c_, a_, b_) asm volatile("v_mfma_f32_16x16x32_bf16 %0, %1, %2, %0" : "+v"(c_) : "v"(a_), "v"(b_))
#define MA(G, n) do { if (TRN) MFA(c[(n) >> 2][SNK(n)], FB(G, SNK(n)), G.a[(n) >> 2]); else MFA(c[(n) >> 2][SNK(n)], G.a[(n) >> 2], FB(G, SNK(n))); SBAR(); } while (0)
#define MW2(Fq, G, sn, un, n) do { MA(Fq, 2 * (n)); if ((n) < 12) RW(G, sn, un, (n) + ((n) + 2) / 3); MA(Fq, 2 * (n) + 1); if ((n) < 12 && (n) % 3 == 0) RW(G, sn, un, (n) + (n) / 3 + 1); } while (0)
#define HALFW(Fq, G, sn, un) do { MW2(Fq, G, sn, un, 0); MW2(Fq, G, sn, un, 1); MW2(Fq, G, sn, un, 2); MW2(Fq, G, sn, un, 3); MW2(Fq, G, sn, un, 4); MW2(Fq, G, sn, un, 5); MW2(Fq, G, sn, un, 6); MW2(Fq, G, sn, un, 7); \
    MW2(Fq, G, sn, un, 8); MW2(Fq, G, sn, un, 9); MW2(Fq, G, sn, un, 10); MW2(Fq, G, sn, un, 11); MW2(Fq, G, sn, un, 12); MW2(Fq, G, sn, un, 13); MW2(Fq, G, sn, un, 14); MW2(Fq, G, sn, un, 15); WAITL(0); SBAR(); } while (0)
#define STAGEW(s) do { HALFW(f0, f1, s, 1); BAR(); SBAR(); HALFW(f1, f0, (s) + 1, 0); } while (0)
#define KLOOP() \
      unsigned aad[2][2], bad[2][2]; \
      { LANE(); const int q = np >> 2, p = l & 3, f = ((q >> 1) & 1) | ((g & 1) << 1); \
        _Pragma("unroll") for (int u = 0; u < 2; ++u) { aad[0][u] = ldsb + (wm * 128 + np) * 128 + (((4 * u + g) ^ (np >> 1)) * 16); aad[1][u] = aad[0][u] + 2 * SLOT; \
          bad[0][u] = ldsb + L_B + (8 * g + q) * 128 + (((2 * wn + (TRN ? u : u ^ wm)) ^ f) * 32) + p * 8; bad[1][u] = bad[0][u] + 2 * SLOT; \
          PIN(aad[0][u]); PIN(aad[1][u]); PIN(bad[0][u]); PIN(bad[1][u]); } } \
      unsigned tR, tC; \
      { LANE(); tR = ldsb + L_ROWM + (wm * 128 + (TRN ? np : 4 * g)) * 4; tC = ldsb + L_COL + (wn * 32 + (TRN ? 4 * g : wm * 16 + np)) * 4; PIN(tR); PIN(tC); SBAR(); } \
      f4 c[8][4]; \
      _Pragma("unroll") for (int i = 0; i < 8; ++i) _Pragma("unroll") for (int jj = 0; jj < 4; ++jj) { c[i][jj] = (f4)(0.f); PIN(c[i][jj]); } \
      FW f0, f1; \
      BAR(); \
      STMP(1); STMC(40); \
      RW(f0, 0, 0, 0); RW(f0, 0, 0, 1); RW(f0, 0, 0, 8); RW(f0, 0, 0, 2); RW(f0, 0, 0, 3); RW(f0, 0, 0, 4); RW(f0, 0, 0, 5); RW(f0, 0, 0, 6); RW(f0, 0, 0, 7); \
      RW(f0, 0, 0, 9); RW(f0, 0, 0, 10); RW(f0, 0, 0, 11); RW(f0, 0, 0, 12); RW(f0, 0, 0, 13); RW(f0, 0, 0, 14); RW(f0, 0, 0, 15); \
      WAITL(0); SBAR(); \
      STAGEW(0); STAGEW(1); STAGEW(2); STAGEW(3); STAGEW(4); STAGEW(5); STAGEW(6); STAGEW(7); \
      STAGEW(8); STAGEW(9); STAGEW(10); STAGEW(11); STAGEW(12); STAGEW(13); STAGEW(14); \
      HALFW(f0, f1, 15, 1); if (TRN) BAR(); SBAR()
#define MT(n) do { MW(f1, n); TB(n); } while (0)
#define KLAST() do { MT(0); MT(1); MT(2); MT(3); MT(4); MT(5); MT(6); MT(7); MT(8); MT(9); MT(10); MT(11); MT(12); MT(13); MT(14); MT(15); \
      MT(16); MT(17); MT(18); MT(19); MT(20); MT(21); MT(22); MT(23); MT(24); MT(25); MT(26); MT(27); MT(28); MT(29); MT(30); MT(31); WAITL(0); } while (0)
#define H2(v, e0) ((f2){(v)[e0], (v)[(e0) + 1]})

#define WQ0 56
#define HXM 0
#define QROT2 1
#define MORD 3
#define PO 0
#define FI (PO ? 3 : 11)
#define PM 2
#define WE 1
#define FORCE_SLOW 0
#define GP1 3
#define G0A 4
#define KD 90
#define KSL 2
#define PD5 8
#define OSL 1
#define MD1 0
#define MD2 1
#define TKP 13
#define CL0 6

__global__ __launch_bounds__(512, 2) void fused(const u16* __restrict__ x, const u16* __restrict__ wqkv, const u16* __restrict__ wo, const u16* __restrict__ wr,
                                                const u16* __restrict__ wg, const u16* __restrict__ wu, const u16* __restrict__ wd, char* ws,
                                                u16* __restrict__ out) {
  extern __shared__ __attribute__((aligned(16))) char lds[];
  const int w = __builtin_amdgcn_readfirstlane(threadIdx.x >> 6);
#define LANE() int l; asm volatile("v_mbcnt_lo_u32_b32 %0, -1, 0\n v_mbcnt_hi_u32_b32 %0, -1, %0" : "=v"(l)); \
  const int np = l & 15, g = l >> 4, rr = l >> 3, sl = l & 7; (void)np; (void)g; (void)rr; (void)sl
  STMP(0);
  const int gb = blockIdx.x, xcd = gb & 7, j = gb >> 3, xq = xcd >> 1;
  const unsigned ldsb = (unsigned)(size_t)(LAS char*)lds;
  const bool isQ = j >= 16;
  const int rg = (xcd & 1) * 4 + (j & 3), hp = xq * 4 + (j >> 2), qg = xq * 2 + ((j >> 2) & 1);
  const int hd0 = isQ ? qg * 4 : 32 + hp * 2, hd1 = isQ ? 2 : 32;
  const bool isB = w == 6;
  unsigned mb_, step, d1, d2, d3, so, lim; u4 rs;
#define VSET() unsigned v[4]; { const unsigned pitch = isB ? 6144 : 2048, sm = isB ? 2 : 7; \
    _Pragma("unroll") for (int i = 0; i < 4; ++i) v[i] = (8 * i + rr) * pitch + ((sl ^ ((isB ? rr : rr >> 1) & sm) ^ ((i & 1) << 2)) * 16) - i * 1024; }
#define QL 4
#define QROT 1
#define G4(s, q, dq) do { unsigned t_; asm volatile("s_add_u32 m0, %1, %2\n s_add_u32 %0, %3, %4" : "=s"(t_) : "s"(mb_), "i"(((s) % 3) * SLOT + (q) * 4096), "s"(so), "s"(dq) : "scc", "memory"); \
        BLDS(v[0], rs, t_, 0); BLDS(v[1], rs, t_, 1024); BLDS(v[2], rs, t_, 2048); BLDS(v[3], rs, t_, 3072); } while (0)
#define PA(s) do { G4(s, 0, 0u); G4(s, 1, d1); } while (0)
#define PB(s) do { G4(s, 2, d2); G4(s, 3, d3); asm volatile("s_add_u32 %0, %0, %1\n s_cmp_eq_u32 %0, %2\n s_cselect_b32 %0, 0, %0" : "+s"(so) : "s"(step), "s"(lim) : "scc"); } while (0)
  if (__builtin_expect(j < 24 && w >= 4 && w < 7, 1)) {
    LANE();
    rs = isB ? mkrs((const char*)wqkv + hd0 * 64, 1024 * 6144 - hd0 * 64) : mkrs((const char*)x + (size_t)(rg * 256 + (w - 4) * 128) * 2048, 128 * 2048);
    mb_ = RFL(ldsb + (w - 4) * 16384); step = isB ? 64 * 6144 : 128;
    d1 = isB ? 32 * 6144 : 65536; d2 = isB ? hd1 * 64 : 131072; d3 = d1 + d2;
    so = isQ ? (QROT + QROT2 * (qg & 1)) * step : 0; lim = QL * step;
    VSET();
    STMP(37);
    PA(0); PB(0); PA(1);
    STMP(38);
  }
#define CPF 1
  u4 rsC;
  { u64 pc; unsigned len;
    asm volatile("s_getpc_b64 %0\n.Lcp0:\n s_mov_b32 %1, .Lcp1-.Lcp0\n s_max_i32 %1, %1, .Lcp2-.Lcp0\n s_max_i32 %1, %1, .Lcp3-.Lcp0\n s_max_i32 %1, %1, .Lcp4-.Lcp0\n s_max_i32 %1, %1, .Lcp5-.Lcp0\n s_max_i32 %1, %1, .Lcp6-.Lcp0\n s_max_i32 %1, %1, 0" : "=&s"(pc), "=&s"(len) :: "scc");
    rsC = (u4){(unsigned)pc & ~127u, (unsigned)(pc >> 32) & 0xFFFFu, len, 0x00020000u}; }
#define CODE(n) asm volatile("s_mov_b32 m0, %2\n s_nop 0\n buffer_load_dwordx4 %0, %1, 0 offen lds" :: "v"((n) * 1024 + l * 16), "s"(rsC), "s"(RFL(ldsb + L_ROWM + 3072)) : "memory")
  if (CPF && j < 24 && w < 4) { LANE(); CODE(j * 4 + w); }
#define CPL(n) do { __builtin_amdgcn_s_waitcnt(0x0F70); asm volatile(".Lcp" #n ":"); } while (0)
#define CPE(n) asm volatile(".Lcp" #n ":")
  const __amdgpu_buffer_rsrc_t wrs = __builtin_amdgcn_make_buffer_rsrc(ws, 0, WS_END, 0x00020000);
  const int mb2 = (xcd & 1) * 8 + (xcd >> 2) * 4 + (j >> 3), nb = ((xcd >> 1) & 1) * 8 + (j & 7), lq = (mb2 + (mb2 >> 2)) & 3, hx = (nb * 4) & HXM;
#define msg (WS_MSG + xcd * 256 + ((l & 8) ? 0 : 64 + sl * 8))
#define MSGOK(f) all64((((l & 8) ? ((f)[0] ^ tlo) | ((f)[1] ^ thi) : (f)[1] ^ tlo)) == 0u)
#define KEYOK(f) all64((l & 8) || (f)[1] == tlo)
#define GBAD(v) (((v)[1] ^ tlo) | ((v)[3] ^ thi))
#define WO_SETUP() const u4 rsO = mkrs((const char*)wo + nb * 128, 1024 * 2048 - nb * 128), rsX = mkrs((const char*)x + (size_t)mb2 * 128 * 2048 + nb * 128, 128 * 2048 - nb * 128); \
  const unsigned vW = rr * 2048 + ((sl ^ (((rr >> 1) & 3) << 1)) * 16), vX = rr * 2048 + ((sl ^ rr) * 16)
#define DMA1(v, rs, m, so) asm volatile("s_mov_b32 m0, %2\n s_nop 0\n buffer_load_dwordx4 %0, %1, %3 offen lds" ::"v"(v), "s"(rs), "s"(RFL(m)), "s"(RFL(so)) : "memory")
#define WOL(n0, n1, st) _Pragma("nounroll") for (int n_ = (n0); n_ < (n1); n_ += (st)) DMA1(vW, rsO, ldsb + L_WO + n_ * 1024, (n_ * 16384) ^ (hx * 65536))
#define WOR(n0, n1, st) _Pragma("nounroll") for (int n_ = (n0); n_ < (n1); n_ += (st)) DMA1(vW, rsO, ldsb + L_WO + (n_ ^ (lq * 32) ^ (hx * 4)) * 1024, (n_ ^ (lq * 32)) * 16384)
#define XTL(n0, st) _Pragma("nounroll") for (int n_ = (n0); n_ < 16; n_ += (st)) DMA1(vX, rsX, ldsb + L_XT + n_ * 1024, n_ * 16384)
  const int ow = isQ && j < 24 ? w ^ 4 : w;
  f4 oc[2][4];
  u4 st2[2];
  unsigned tpoll = 0;
#define TPOLL(d) tpoll = (unsigned)__builtin_amdgcn_s_memrealtime() + (d)
#define PDK 280
#define PDQ 80
#define XT(r, ct) (xt + (r) * 2048 + (((2 * (ct) + (g >> 1)) ^ sl) * 16))
#define S2OK() all64(((st2[0][1] ^ t_[0]) | (st2[0][3] ^ t_[1]) | (st2[1][1] ^ t_[0]) | (st2[1][3] ^ t_[1])) == 0u)
#define OINIT() do { PIN(st2[0]); PIN(st2[1]); const char* xt = lds + L_XT + (ow * 32 + np) * 128 + (g & 1) * 8; \
    { const u2 t_ = *(const u2*)(lds + L_TAG); if (__builtin_expect(!S2OK(), 0)) { _Pragma("nounroll") for (unsigned it = 0; it < SPIN_CAP; ++it) { CLOB(); ST2(); if (S2OK()) break; } } } \
    _Pragma("unroll") for (int r = 0; r < 2; ++r) { \
      const float c1 = 1.f + U2F(st2[r][2]), c0 = -hi(st2[r][0]) * U2F(st2[r][2]); \
      _Pragma("unroll") for (int ct = 0; ct < 4; ++ct) { const u2 v = *(const u2*)XT(r, ct); \
        oc[r][ct] = (f4){__builtin_fmaf(lo(v[0]), c1, c0), __builtin_fmaf(hi(v[0]), c1, c0), __builtin_fmaf(lo(v[1]), c1, c0), __builtin_fmaf(hi(v[1]), c1, c0)}; PIN(oc[r][ct]); } \
      } } while (0)
#define XOWN() do { WO_SETUP(); (void)vW; (void)rsO; if (ow < 4) _Pragma("unroll") for (int n_ = 0; n_ < 4; ++n_) DMA1(vX, rsX, ldsb + L_XT + (ow * 4 + n_) * 1024, (ow * 4 + n_) * 16384); \
    const u4 rsW = mkrs(ws, WS_END); const unsigned sto = WS_MS + (mb2 * 128 + (w & 3) * 32 + np) * 16, sto1 = sto + 256; ALD128(st2[0], sto); ALD128(st2[1], sto1); } while (0)
#define ST2() do { const unsigned sto = WS_MS + (mb2 * 128 + (w & 3) * 32 + np) * 16; st2[0] = LD128(sto); st2[1] = LD128(sto + 256); } while (0)

  if (j < 24) {
    if (w >= 4) {
      LANE();
      unsigned tlo = 0, thi = 0;
      WO_SETUP();
      u2 f = (u2){0u, 0u};
      if (w == 7) {
        const u4 rsW = mkrs(ws, WS_END);
        int nbar = 16;
#define TICK() do { if (nbar) { BAR(); --nbar; } } while (0)
        if (l < 16) ((unsigned*)(lds + L_CNT))[l] = 0u;
        WAITL(0);
        TICK();
        u2 ep; unsigned z0 = 0; PIN(z0);
        ALD64(ep, z0);
        { const u4 pz = (u4){POISON, POISON, POISON, POISON};
          if (isQ) {
            ST128(pz, WS_OFL + ((rg * 2 + (l >> 5)) * 32 + qg * 4) * 128 + (l & 31) * 16);
          } else {
            const unsigned kb = WS_KVP + (hp * 16 + rg) * KVS + l * 16;
            ST128(pz, kb); ST128(pz, kb + 1024); ST128(pz, kb + 8 * KVS); ST128(pz, kb + 8 * KVS + 1024);
            ST128(pz, l < 16 ? WS_KVP + (hp * 16 + rg + (l & 8)) * KVS + 2048 + sl * 16 : WS_OFF);
          } }
        SBAR();
        if (PO && isQ) {
#define PPS 8
          TICK();
#pragma nounroll
          for (int i = 0; i < 16; i += PPS) {
            TICK();
            const unsigned ob = WS_OF + ((rg * 16 + i) * 32 + qg * 4) * 1024 + l * 16; const u4 pz = (u4){POISON, POISON, POISON, POISON};
#pragma unroll
            for (int k = 0; k < PPS; ++k) { ST128(pz, ob + k * 32768); ST128(pz, ob + k * 32768 + 1024); }
          }
        } else TICK();
        TICK(); TICK();
        WAITV(0); PIN(ep);
        const u64 tag = (((u64)RFL(ep[1]) << 32) | RFL(ep[0])) + TAG_STEP;
        tlo = (unsigned)tag; thi = (unsigned)(tag >> 32);
        ST64(((u2){tlo, thi}), l == 0 ? WS_FLAG + gb * 8 : WS_OFF);
        STMP(8);
#pragma nounroll
        while (nbar > 16 - TKP) { TICK();
#define CPF2 10
          if (CPF2 && nbar == 16 - CPF2) { CODE(j * 4); CODE(j * 4 + 1); CODE(j * 4 + 2); CODE(j * 4 + 3); }
        }
        u4 ms[4], cs[8];
        const unsigned a0 = WS_MS + (rg * 256 + l) * 16, a2 = WS_CS + ((g * 96 + hd0) * 16 + np) * 16, a3 = a2 + hd1 * 256, a4 = a2 + 4 * 96 * 256, a5 = a3 + 4 * 96 * 256, a6 = msg;
#pragma nounroll
        for (unsigned it = 0; it < SPIN_CAP; ++it) {
#pragma unroll
          for (int i = 0; i < 4; ++i) asm volatile("buffer_load_dwordx4 %0, %1, %2, 0 offen offset:%3 sc1" : "=v"(ms[i]) : "v"(a0), "s"(rsW), "i"(i * 1024) : "memory");
#pragma unroll
          for (int i = 0; i < 8; ++i) asm volatile("buffer_load_dwordx4 %0, %1, %2, 0 offen offset:%3 sc1" : "=v"(cs[i]) : "v"(i < 2 ? a2 : i < 4 ? a3 : i < 6 ? a4 : a5), "s"(rsW), "i"((i & 1) * 256) : "memory");
          ALD64(f, a6);
          if (nbar > 1) TICK();
          if (nbar > 1 && it == 0) TICK();
          WAITV(0);
          unsigned bad = 0;
#pragma unroll
          for (int i = 0; i < 4; ++i) { PIN(ms[i]); PIN(cs[i]); PIN(cs[4 + i]); bad |= GBAD(ms[i]) | GBAD(cs[i]) | GBAD(cs[4 + i]); }
          PIN(f);
          if (all64(bad == 0u)) break;
        }
        STMP(9);
        float ct[2];
#pragma unroll
        for (int e = 0; e < 2; ++e) {
          float t[4];
#pragma unroll
          for (int i = 0; i < 4; ++i) t[i] = add1(U2F(cs[i][2 * e]), U2F(cs[4 + i][2 * e]));
#pragma unroll
          for (int i = 0; i < 2; ++i) { auto r = __builtin_amdgcn_permlane32_swap(F2U(t[i]), F2U(t[i + 2]), false, false); t[i] = add1(U2F(r[0]), U2F(r[1])); }
          auto r = __builtin_amdgcn_permlane16_swap(F2U(t[0]), F2U(t[1]), false, false);
          ct[e] = add1(U2F(r[0]), U2F(r[1])); PIN(ct[e]);
        }
#pragma unroll
        for (int i = 0; i < 4; ++i) { ms[i][0] = (ms[i][0] & 0xFFFF0000u) ^ 0x80000000u; ms[i][1] = F2U(mul1(U2F(ms[i][2]), 1.44269504f)); PIN(ms[i]); }
#pragma nounroll
        while (nbar) TICK();
#pragma unroll
        for (int i = 0; i < 4; ++i) { ((unsigned*)(lds + L_ROWM))[i * 64 + l] = ms[i][0]; ((unsigned*)(lds + L_ROWS))[i * 64 + l] = ms[i][1]; }
        ((float*)(lds + L_COL))[g * 32 + np] = ct[0]; ((float*)(lds + L_COL))[g * 32 + 16 + np] = ct[1];
        if (l == 8) *(u2*)(lds + L_TAG) = (u2){tlo, thi};
        if (!isQ) lds_arrive((LAS unsigned*)(lds + L_CNT) + 5);
      } else {
        VSET();
        WAITV(8); BAR(); PB(1); PA(2); WAITV(8); BAR(); STMP(44); PB(2); if (QL == 3) so = lim; PA(0); PB(0);
#define LS(s) do { if ((s) < 14) WAITV(16); else WAITV(0); BAR(); STMP(44 + (s)); if ((s) + 3 < 16) { if ((s) + 3 == QL) so = lim; PA(s); PB(s); } } while (0)
        LS(1); LS(2); LS(3); LS(4); LS(5); LS(6); LS(7); LS(8); LS(9); LS(10); LS(11); LS(12); LS(13); LS(14);
      }
#define KEYS() do { if (w == 7) { _Pragma("nounroll") for (unsigned it = 0; it < SPIN_CAP && !MSGOK(f); ++it) { f = LD64(msg); CLOB(); } \
        if (l < 8) ((unsigned*)(lds + L_TAG + 16))[l] = f[0]; \
        lds_arrive((LAS unsigned*)(lds + L_CNT) + 3); } } while (0)
      if (isQ) {
        if (w < 7) { WOL(20 + w - 4, WQ0, 3); }
        LBAR(); STMP(59);
        XOWN(); SBAR(); WOL(WQ0 + w - 4, 128, 4); WOL(w - 4, 20, 4);
        const unsigned tq = (unsigned)__builtin_amdgcn_s_memrealtime();
        WAITV(5); OINIT();
        KEYS();
        STMP(21);
        const unsigned sb = WS_KVP + (qg * 4 + (w & 1)) * 8 * KVS + ((w >> 1) & 1) * 1024 + l * 16;
        const unsigned sz = (w & 2) ? WS_OFF : WS_KVP + ((qg * 4 + (w & 1)) * 8 + (np >> 1)) * KVS + 2048 + (np & 1) * 64 + g * 16;
        u4 prm[2];
#pragma unroll
        for (int e = 0; e < 4; ++e) {
          const unsigned o = (np >> 2) == g ? ((np & 1) ? 0x3F800000u : 0x00003F80u) : 0u;
          prm[0][e] = e == (w & 2) + ((np >> 1) & 1) ? o : 0u; prm[1][e] = e == 2 - (w & 2) + ((np >> 1) & 1) ? o : 0u;
        }
        PIN(prm[0]); PIN(prm[1]);
        __builtin_amdgcn_s_setprio(2);
#pragma nounroll
        while ((unsigned)__builtin_amdgcn_s_memrealtime() - tq < KD) __builtin_amdgcn_s_sleep(1);
        lds_wait<1>((LAS unsigned*)(lds + L_CNT) + 3, 1);
        u4 v[16], zv[2]; f4 kg[4][2];
#define KVLD(gq) do { _Pragma("unroll") for (int p = 4 * (gq); p < 4 * (gq) + 4; ++p) v[p] = __builtin_amdgcn_raw_buffer_load_b128(wrs, sb, ((p & 7) + (p >> 3) * 16) * KVS, 16); } while (0)
#define ZLD(hh) zv[hh] = __builtin_amdgcn_raw_buffer_load_b128(wrs, sz, (hh) * 16 * KVS, 16)
        ZLD(0); KVLD(0); KVLD(1); SBAR(); if (GP1) PACE(GP1); ZLD(1); KVLD(2); KVLD(3); SBAR();
#pragma unroll
        for (int hh = 0; hh < 2; ++hh) {
          unsigned pend = 7;
#pragma nounroll
          for (unsigned it = 0; it < SPIN_CAP; ++it) {
#pragma unroll
            for (int gq = 2 * hh; gq < 2 * hh + 2; ++gq)
              if (pend & (1u << (gq & 1))) {
                f4 t0_ = (f4)(0.f), t1_ = (f4)(0.f); unsigned mx = 0;
#pragma unroll
                for (int p = 4 * gq; p < 4 * gq + 4; ++p) { mx = mx4(mx, v[p]); t0_ = MFMA16(BC8(prm[0]), BC8(v[p]), t0_); t1_ = MFMA16(BC8(prm[1]), BC8(v[p]), t1_); }
                kg[gq][0] = t0_; kg[gq][1] = t1_;
                if (clean(mx)) pend &= ~(1u << (gq & 1));
              }
            if ((pend & 4u) && clean(mx4(0u, zv[hh]))) pend &= ~4u;
            if (!pend) break;
            if (KSL) __builtin_amdgcn_s_sleep(KSL);
            CLOB();
            if (pend & 4u) ZLD(hh);
            if (pend & 1u) KVLD(2 * hh);
            if (pend & 2u) KVLD(2 * hh + 1);
            if (hh == 0) { ZLD(1); KVLD(2); KVLD(3); }
            SBAR();
          }
          if (hh) STMP(25); else STMP(4);
          const f4 k0 = kg[2 * hh][0] + kg[2 * hh + 1][0], k1 = kg[2 * hh][1] + kg[2 * hh + 1][1];
          *(u4*)(lds + L_KX + ((2 * hh + (w & 1)) * 2 + ((w >> 1) & 1)) * 1024 + l * 16) = (u4){pk2(k0[0], k0[1]), pk2(k0[2], k0[3]), pk2(k1[0], k1[1]), pk2(k1[2], k1[3])};
          if (!(w & 2)) {
            f4 z = __builtin_bit_cast(f4, zv[hh]);
#pragma unroll
            for (int e = 0; e < 4; ++e) { z[e] += DPPF(z[e], 0x122, 0xf); z[e] += DPPF(z[e], 0x124, 0xf); z[e] += DPPF(z[e], 0x128, 0xf); }
            if (np < 2) *(u2*)(lds + L_ZX + (2 * hh + (w & 1)) * 64 + g * 16 + np * 8) = (u2){pk2(z[0], z[1]), pk2(z[2], z[3])};
          }
          LBAR();
        }
        __builtin_amdgcn_s_setprio(0);
        CPL(3);
        TPOLL(PDQ);
      } else {
        if (w < 7) { if (WE) { WOL(32 + w, 128, 3); WAITV(0); STMP(35); } lds_arrive((LAS unsigned*)(lds + L_CNT) + 2); }
        KEYS();
        __builtin_amdgcn_s_waitcnt(0x0F70);
      }
    } else {
      const int wm = w >> 1, wn = w & 1;
      if (!isQ) {
        constexpr bool TRN = false;
        unsigned cA = ldsb + L_CNT; PIN(cA);
        unsigned exw, exa, pka, pza, cB = cA + wn * 4, one1 = 1u; bool zsel;
        { LANE(); const unsigned kb = WS_KVP + ((hp * 2 + wn) * 8 + rg) * KVS;
          exw = ldsb + L_EX + l * 16 + w * 4096; exa = exw ^ 8192; pka = kb + wm * 1024 + l * 16; pza = wm == 0 && np < 2 ? kb + 2048 + np * 64 + g * 16 : WS_OFF; zsel = (np ^ wm) & 1;
          PIN(exw); PIN(exa); PIN(pka); PIN(pza); PIN(cB); PIN(one1); SBAR(); }
        KLOOP();
        float cc[4]; f4 rsk[8], mk[8]; unsigned tf;
        unsigned tC1 = tC ^ 64; PIN(tC1);
#define TB1(n) do { if ((n) < 4) RD32(cc[(n) & 3], ((n) & 1) ? tC1 : tC, ((n) >> 1) * 256); else if ((n) & 1) RD128(rsk[(((n) - 4) >> 1) & 3], tR, 1024 + (((n) - 4) >> 1) * 64); else RD128(mk[(((n) - 4) >> 1) & 3], tR, (((n) - 4) >> 1) * 64); } while (0)
#define TB(n) do { if ((n) == 8) RD32(tf, cA, 20); else if ((n) > 8 && (n) < 21) TB1(((n) - 9) % 12); SBAR(); } while (0)
        KLAST();
#undef TB
        PIN(tf);
        if (__builtin_expect(RFL(tf) == 0u, 0)) {
          lds_wait<0>((LAS unsigned*)(lds + L_CNT) + 5, 1);
          TB1(0); TB1(1); TB1(2); TB1(3); TB1(4); TB1(5); TB1(6); TB1(7); TB1(8); TB1(9); TB1(10); TB1(11); WAITL(0);
        }
#pragma unroll
        for (int i = 0; i < 4; ++i) { PIN(mk[i]); PIN(rsk[i]); PIN(cc[i]); }
#pragma unroll
        for (int i = 4; i < 8; ++i) { RD128(mk[i], tR, i * 64); RD128(rsk[i], tR, 1024 + i * 64); }
        SBAR();
        STMP(2); STMC(41);
        f4 kv[2][2], zz[2];
        kv[0][0] = kv[0][1] = kv[1][0] = kv[1][1] = zz[0] = zz[1] = (f4)(0.f);
        const u4 ones = (u4){0x3F803F80u, 0x3F803F80u, 0x3F803F80u, 0x3F803F80u};
        u4 kf[2], vf[2];
#define LN2(i, jj, e0) (__builtin_elementwise_fma(H2(mk[i], e0), (f2)(cc[jj]), H2(c[i][jj], e0)) * H2(rsk[i], e0))
#define KVM(n) do { if ((n) < 4) kv[(n) >> 1][(n) & 1] = MFMA16(BC8(kf[(n) >> 1]), BC8(vf[(n) & 1]), kv[(n) >> 1][(n) & 1]); else zz[(n) & 1] = MFMA16(BC8(kf[(n) & 1]), BC8(ones), zz[(n) & 1]); } while (0)
#pragma unroll
        for (int ip = 0; ip < 4; ++ip) {
          if (ip == 2) { WAITL(0);
#pragma unroll
            for (int i = 4; i < 8; ++i) { PIN(mk[i]); PIN(rsk[i]); } }
          f2 ak[8], av[8], ek[8];
#pragma unroll
          for (int n = 0; n < 8; ++n) { const int i = 2 * ip + ((n >> 1) & 1), e0 = (n & 1) * 2, t = n >> 2; ak[n] = LN2(i, t, e0); av[n] = LN2(i, 2 + t, e0); }
          SBAR();
#pragma unroll
          for (int n = 0; n < 8; ++n) {
            if (ip > 0 && n < 6) KVM(n);
            ek[n] = (f2){__builtin_amdgcn_fmed3f(__builtin_amdgcn_exp2f(ak[n][0]), 0.f, 1.f), __builtin_amdgcn_fmed3f(__builtin_amdgcn_exp2f(ak[n][1]), 0.f, 1.f)};
            ak[n] = (f2){fmaxf(ak[n][0], 1.44269504e-6f), fmaxf(ak[n][1], 1.44269504e-6f)};
            SBAR();
          }
#pragma unroll
          for (int n = 0; n < 8; ++n) { const f2 r = __builtin_elementwise_fma(ak[n], (f2)(0.693147181f), ek[n]); kf[n >> 2][n & 3] = pk2(r[0], r[1]); vf[n >> 2][n & 3] = pk2(av[n][0], av[n][1]); }
          SBAR();
        }
        KVM(1); KVM(3); KVM(4); KVM(5); KVM(0); KVM(2);
        STMP(19);
        const f4 zs = zsel ? zz[1] : zz[0];
        *(LAS f4*)(size_t)(exw + 1024) = kv[0][1]; *(LAS f4*)(size_t)exw = kv[1][1]; *(LAS f4*)(size_t)(exw + 2048) = zs;
        asm volatile("s_mov_b64 exec, 1\n ds_add_u32 %0, %1 offset:24\n s_mov_b64 exec, -1" ::"v"(cB), "v"(one1) : "memory");
        f4 e0, e1, e2;
#pragma nounroll
        do {
          RD32(tf, cB, 24); RD128(e0, exa, 0); RD128(e1, exa, 1024); RD128(e2, exa, 2048);
          WAITL(0); PIN(tf); PIN(e0); PIN(e1); PIN(e2);
        } while (RFL(tf) < 2u);
        e0 += kv[0][0]; e1 += kv[1][0];
        ST128(san4((u4){pk2(e0[0], e0[1]), pk2(e0[2], e0[3]), pk2(e1[0], e1[1]), pk2(e1[2], e1[3])}), pka);
        ST128(san4(__builtin_bit_cast(u4, (zs + e2) * 1.44269504f)), pza);
        SBAR();
        STMP(3);
        LANE();
        TPOLL(PDK);
        if (j == 1 && w == 0) { const u2 t_ = *(const u2*)(lds + L_TAG); ST128(((u4){t_[0], t_[1], tpoll, 0u}), l < 8 ? WS_HU + xcd * 128 + l * 16 : WS_OFF); }
        lds_arrive((LAS unsigned*)(lds + L_CNT) + 8);
        lds_wait<1>((LAS unsigned*)(lds + L_CNT) + 8, 4);
        XOWN(); SBAR();
        { WO_SETUP(); (void)vX; (void)rsX; if (WE) { WOL(w, 36, 4); } else { WOR(w, 128, 4); } }
        WAITV(WE ? 9 : 32); OINIT();
        WAITV(0);
        lds_arrive((LAS unsigned*)(lds + L_CNT) + 2);
        lds_wait<1>((LAS unsigned*)(lds + L_CNT) + 3, 1);
        CPL(1);
        STMP(20);
      } else {
        constexpr bool TRN = true;
        KLOOP();
        f4 cc[4]; float rs[8], mn[8];
#define TB(n) do { if ((n) < 4) RD128(cc[(n) & 3], tC, ((n) >> 1) * 256 + ((n) & 1) * 64); else if ((n) < 20) { if ((n) & 1) RD32(rs[(((n) - 4) >> 1) & 7], tR, 1024 + (((n) - 4) >> 1) * 64); else RD32(mn[(((n) - 4) >> 1) & 7], tR, (((n) - 4) >> 1) * 64); } SBAR(); } while (0)
        KLAST();
#undef TB
#pragma unroll
        for (int i = 0; i < 8; ++i) { PIN(mn[i]); PIN(rs[i]); PIN(cc[i & 3]); }
        STMP(2); STMC(41);
        LANE();
        u4 pf[8][2];
#pragma unroll
        for (int i = 0; i < 8; ++i)
#pragma unroll
          for (int hh = 0; hh < 2; ++hh) {
#pragma unroll
            for (int e = 0; e < 4; ++e) { const int jj = 2 * hh + (e >> 1), e0 = (e & 1) * 2;
              pf[i][hh][e] = phi2(__builtin_elementwise_fma(H2(cc[jj], e0), (f2)(mn[i]), H2(c[i][jj], e0)) * rs[i]); }
            PIN(pf[i][hh]);
          }
        const u2 tg = *(const u2*)(lds + L_TAG);
        const unsigned ofl = l < 8 ? WS_OFL + ((rg * 2 + wm) * 32 + (qg * 2 + wn) * 2) * 128 + sl * 16 : WS_OFF;
        unsigned oa[8];
#pragma unroll
        for (int i = 0; i < 8; ++i) { oa[i] = WS_OF + ((rg * 16 + wm * 8 + i) * 32 + qg * 4 + wn) * 1024 + l * 16; PIN(oa[i]); }
        STMP(19);
        BAR();
        STMP(22);
        u4 kA[2][2], zA[2];
#define KRD(hh) do { zA[hh] = *(const u4*)(lds + L_ZX + (2 * (hh) + wn) * 64 + g * 16); \
          kA[hh][0] = *(const u4*)(lds + L_KX + (2 * (hh) + wn) * 2048 + l * 16); kA[hh][1] = *(const u4*)(lds + L_KX + (2 * (hh) + wn) * 2048 + 1024 + l * 16); SBAR(); } while (0)
        KRD(0);
        if (FI < 0) { ST128(((u4){tg[0], tg[1], tg[0], tg[1]}), ofl); SBAR(); STMP(24); }
        f4 dn[16], x0[16], x1[16];
#define OM(n) do { dn[n] = MFMA16(BC8(zA[(n) >> 3]), BC8(pf[(n) & 7][(n) >> 3]), (f4)(1.44269504e-6f)); x0[n] = MFMA16(BC8(kA[(n) >> 3][0]), BC8(pf[(n) & 7][(n) >> 3]), (f4)(0.f)); \
          x1[n] = MFMA16(BC8(kA[(n) >> 3][1]), BC8(pf[(n) & 7][(n) >> 3]), (f4)(0.f)); } while (0)
        OM(0); OM(1);
#pragma unroll
        for (int n = 0; n < 16; ++n) {
          SBAR();
          const float inv = __builtin_amdgcn_rcpf(dn[n][0]);
          asm volatile("" :: "v"(dn[n]));
          if (n + 2 < 16 && n != 6 && n != 7) OM(n + 2);
          float y[8];
#pragma unroll
          for (int e = 0; e < 4; ++e) { y[e] = x0[n][e] * inv; y[4 + e] = x1[n][e] * inv; PIN(y[e]); PIN(y[4 + e]); }
          const u4 ov = (u4){pk2(y[0], y[1]), pk2(y[2], y[3]), pk2(y[4], y[5]), pk2(y[6], y[7])};
          ST128(PO && n < 8 ? san4(ov) : ov, oa[n & 7] + (n >> 3) * 2048);
          if (n == FI) { SBAR(); if (!PO) WAITV(FI > 7 ? FI - 7 : 0); ST128(((u4){tg[0], tg[1], tg[0], tg[1]}), ofl); SBAR(); STMP(24); }
          if (n == 7) { SBAR(); BAR(); KRD(1); OM(8); OM(9); }
        }
        SBAR();
        STMP(23);
        WAITV(0);
        ST128(((u4){tg[0], tg[1], tg[0], tg[1]}), ofl + 128);
        SBAR();
        CPL(2);
        STMP(5);
      }
    }
  } else {
    LANE();
    const int jm = j - 24, jsl = 7 - xcd, c0 = 4 - (int)(((size_t)wg >> 7) & 7) + xcd;
    const bool master = jm == 7 && jsl == 7, help = jm == 7 && jsl != 7, half = jm == 7 || jm == jsl;
    const int jc = help ? jsl : jm, hc = jc * 8 + ((c0 + jc) & 7);
    const int row0 = (xcd & 1) * 1024 + (xq * 8 + jm) * 32 + w * 4;
    u4 cw[12], xr[8], wv[2];
    const u4 rsW = mkrs(ws, WS_END), rsR = mkrs((const char*)x, S * D * 2), rsQ = mkrs((const char*)wqkv, D * 6144);
    const unsigned co = ((jm * 32 + w * 4 + g) * 3072 + xcd * 128 + np * 8) * 2;
#define CI(n) (((n) % 3) * 4 + (n) / 3)
#define CL(a, b) do { _Pragma("unroll") for (int n = a; n < b; ++n) \
      asm volatile("buffer_load_dwordx4 %0, %1, %2, %3 offen" : "=v"(cw[CI(n)]) : "v"(co), "s"(rsQ), "s"((unsigned)(((CI(n) & 3) * 768 + ((CI(n) >> 2) + 1) % 3) * 2048)) : "memory"); } while (0)
    if (w == 0 && l < 8) ((unsigned*)(lds + L_CNT))[l] = 0u;
    CL(0, MORD);
    LBAR();
    u2 ep;
    { const unsigned xo = row0 * 2048 + l * 16, l16 = l * 16; unsigned z0 = 0; PIN(z0);
#pragma unroll
      for (int i = 0; i < 8; ++i) { const int n = (i & 3) * 2 + (i >> 2);
        asm volatile("buffer_load_dwordx4 %0, %1, %2, %3 offen offset:%4" : "=v"(xr[n]) : "v"(xo), "s"(rsR), "s"((unsigned)((n >> 2) * 4096)), "i"((n & 3) * 1024) : "memory"); }
      asm volatile("global_load_dwordx4 %0, %2, %3\n global_load_dwordx4 %1, %2, %3 offset:1024" : "=&v"(wv[0]), "=&v"(wv[1]) : "v"(l16), "s"(uptr((const char*)wr)) : "memory");
      ALD64(ep, z0); }
    CL(MORD, CL0);
    WAITV(CL0 - MORD);
#pragma unroll
    for (int i = 0; i < 8; ++i) PIN(xr[i]);
    PIN(wv[0]); PIN(wv[1]); PIN(ep);
    const u64 tag = (((u64)RFL(ep[1]) << 32) | RFL(ep[0])) + TAG_STEP;
    const unsigned tlo = (unsigned)tag, thi = (unsigned)(tag >> 32);
    const u2 tgv = (u2){tlo, thi};
    {
      float a[13];
#pragma unroll
      for (int i = 0; i < 13; ++i) a[i] = 0.f;
      unsigned one2 = 0x3F803F80u; PIN(one2);
#pragma unroll
      for (int jj = 0; jj < 8; ++jj) {
        const unsigned wv_ = wv[jj >> 2][jj & 3];
#pragma unroll
        for (int i = 0; i < 4; ++i) {
          const unsigned xv = xr[2 * i + (jj >> 2)][jj & 3];
          a[3 * i] = DOT2(xv, one2, a[3 * i]); a[3 * i + 1] = DOT2(xv, xv, a[3 * i + 1]); a[3 * i + 2] = DOT2(xv, wv_, a[3 * i + 2]);
        }
        a[12] = DOT2(wv_, one2, a[12]);
      }
#define RSTEP(n, ctrl) _Pragma("unroll") for (int i = 0; i < n; ++i) a[i] += DPPF(a[i], ctrl, 0xf)
      RSTEP(13, 0xB1); RSTEP(13, 0x4E);
#pragma unroll
      for (int i = 0; i < 3; ++i) { const float u0 = (l & 1) ? a[3 + i] : a[i], u1 = (l & 1) ? a[9 + i] : a[6 + i]; a[i] = (l & 2) ? u1 : u0; }
      a[3] = a[12];
      RSTEP(4, 0x124); RSTEP(4, 0x128);
#pragma unroll
      for (int i = 0; i < 4; ++i) {
        auto s16 = __builtin_amdgcn_permlane16_swap(F2U(a[i]), F2U(a[i]), false, false);
        a[i] = U2F(s16[0]) + U2F(s16[1]);
        auto s32 = __builtin_amdgcn_permlane32_swap(F2U(a[i]), F2U(a[i]), false, false);
        a[i] = U2F(s32[0]) + U2F(s32[1]);
      }
      const float mf = a[0] * (1.f / D), m = rbf(mf);
      const float var = rbf(fmaxf(a[1] * (1.f / D) - mf * mf, 0.f));
      const float rs = __builtin_amdgcn_rcpf(rbf(__builtin_amdgcn_sqrtf(rbf(var + 1e-5f))));
      const float dsc = rs * (a[2] - m * a[3]);
      ST128(((u4){pk2(dsc, m), tlo, F2U(rs), thi}), l < 4 ? WS_MS + (row0 + l) * 16 : WS_OFF);
    }
    SBAR();
    STMP(18);
    CL(CL0, 12);
    u4 gw[4][8], gf[4][8], dw[2][8];
    const u16* wp = ((np >> 3) ? wu : wg) + (size_t)(128 * w + (help ? 64 : 0) + 8 * g) * F + 64 * hc + 8 * sl;
#define GL(b, on) GL2(b, on, 0, 8)
#define GL2(b, on, i0, i1) do { const u16* p_ = (on) ? wp + (size_t)(32 * (b)) * F : wr; const unsigned st_ = (on) ? F : 0; \
      _Pragma("unroll") for (int i_ = i0; i_ < i1; ++i_) asm volatile("global_load_dwordx4 %0, %1, off nt" : "=v"(gw[b][i_]) : "v"(p_ + (size_t)(i_ * st_)) : "memory"); } while (0)
#define GT(b, NV) do { WAITV(NV); _Pragma("unroll") for (int c_ = 0; c_ < 8; ++c_) PIN(gw[b][c_]); _Pragma("unroll") for (int c_ = 0; c_ < 8; ++c_) { TRFRAG(gf[b][c_], gw[b], c_); PIN(gf[b][c_]); } SBAR(); } while (0)
    GL2(0, !master, 0, G0A);
    WAITV(G0A);
#pragma unroll
    for (int i = 0; i < 12; ++i) PIN(cw[i]);
    char* scr = lds + L_WO + (((lq ^ (hx >> 3)) & 2) ? 0 : 65536);
    float* red = (float*)scr;
    {
      u4 sel8;
      const unsigned one = (np & 1) ? 0x3F800000u : 0x00003F80u;
#pragma unroll
      for (int e = 0; e < 4; ++e) sel8[e] = ((np >> 1) == e) ? one : 0u;
#pragma unroll
      for (int p = 0; p < 3; ++p) {
        f4 c = (f4)(0.f);
#pragma unroll
        for (int i = 0; i < 4; ++i) c = MFMA16(BC8(sel8), BC8(cw[p * 4 + i]), c);
        if (g < 2) *(f4*)(red + (w * 3 + p) * 128 + 8 * np + 4 * g) = c;
      }
    }
    lds_arrive((LAS unsigned*)(lds + L_CNT) + 2);
    if (w < 3) {
      lds_wait<1>((LAS unsigned*)(lds + L_CNT) + 2, 8);
      if (w == 0) ST64(tgv, l == 0 ? WS_FLAG + gb * 8 : WS_OFF);
      const float* rp = red + w * 128 + g * 32 + np;
      float c0 = rp[0], c1 = rp[16];
#pragma unroll
      for (int i = 1; i < 8; ++i) { c0 += rp[i * 384]; c1 += rp[i * 384 + 16]; }
      ST128(((u4){F2U(c0), tlo, F2U(c1), thi}), WS_CS + ((jm * 96 + (w + 1) % 3 * 32 + xcd * 4) * 16 + l) * 16);
    }
    SBAR();
    STMP(17);
    if (master) {
      u4 rv[4];
      const unsigned ra = WS_MS + (w * 256 + l) * 16;
      unsigned bad = 15;
      if (MD1) PACE(MD1);
#pragma nounroll
      for (unsigned it = 0; it < SPIN_CAP; ++it) {
#pragma unroll
        for (int i = 0; i < 4; ++i) {
          const u4 v = LD128((bad >> i) & 1u ? ra + i * 1024 : WS_OFF);
          rv[i] = (bad >> i) & 1u ? v : rv[i];
        }
        bad = 0;
#pragma unroll
        for (int i = 0; i < 4; ++i) bad |= GBAD(rv[i]) ? 1u << i : 0u;
        if (all64(bad == 0u)) break;
        __builtin_amdgcn_s_sleep(4);
        CLOB();
      }
      STMP(10);
      unsigned key[4];
#pragma unroll
      for (int i = 0; i < 4; ++i) {
        const unsigned b = rv[i][0] << 16;
        key[i] = ((b ^ ((unsigned)((int)b >> 31) | 0x80000000u)) & 0xFFFF0000u) | (unsigned)(0xFFFF - (w * 256 + i * 64 + l));
      }
      unsigned* cand = (unsigned*)(lds + L_ROWM);
      LAS unsigned* cnt = (LAS unsigned*)(lds + L_CNT) + 1;
      { const unsigned m = wave_max(max(mx3(key[0], key[1], key[2]), key[3]));
        cand[l < 8 ? w * 8 + l : 96 + w] = l < 8 ? 0u : m; }
      LBAR();
      { const u4 m0 = *(const u4*)(cand + 96), m1 = *(const u4*)(cand + 100);
        const unsigned T = min(min(min(m0[0], m0[1]), min(m0[2], m0[3])), min(min(m1[0], m1[1]), min(m1[2], m1[3])));
        unsigned pos[4], n = 0, base;
#pragma unroll
        for (int i = 0; i < 4; ++i) { const u64 b = __builtin_amdgcn_ballot_w64(key[i] >= T);
          pos[i] = __builtin_amdgcn_mbcnt_hi((unsigned)(b >> 32), __builtin_amdgcn_mbcnt_lo((unsigned)b, n)); n += __builtin_popcountll(b); }
        asm volatile("s_mov_b64 exec, 1\n ds_add_rtn_u32 %0, %1, %2\n s_mov_b64 exec, -1\n s_waitcnt lgkmcnt(0)" : "=v"(base) : "v"((unsigned)(size_t)cnt), "v"(n) : "memory");
        base = RFL(base);
#pragma unroll
        for (int i = 0; i < 4; ++i) cand[key[i] >= T ? min(base + pos[i], 72u) : 72u + np] = key[i]; }
      LBAR();
      STMP(11);
      if (RFL(*cnt) > 64u || FORCE_SLOW) {
#define CE(i, j) do { const unsigned hi_ = max(key[i], key[j]), lo_ = min(key[i], key[j]); key[i] = hi_; key[j] = lo_; } while (0)
        CE(0, 1); CE(2, 3); CE(0, 2); CE(1, 3); CE(1, 2);
        unsigned cv = 0;
#pragma nounroll
        for (int it = 0; it < 8; ++it) {
          const unsigned m = wave_max(key[0]);
          const bool win = key[0] == m;
          key[0] = win ? key[1] : key[0]; key[1] = win ? key[2] : key[1]; key[2] = win ? key[3] : key[2]; key[3] = win ? 0u : key[3];
          cv = sl == it ? m : cv;
        }
        if (l < 8) cand[w * 8 + l] = cv;
        LBAR();
      }
      {
        const unsigned c = cand[w * 8 + sl];
        const u4 c0 = *(const u4*)(cand + rr * 8), c1 = *(const u4*)(cand + rr * 8 + 4);
        unsigned r = 0;
#pragma unroll
        for (int e = 0; e < 4; ++e) r += (c0[e] > c ? 1u : 0u) + (c1[e] > c ? 1u : 0u);
        r += (unsigned)__builtin_amdgcn_update_dpp(0, (int)r, 0x128, 0xf, 0xf, false);
        auto a16 = __builtin_amdgcn_permlane16_swap(r, r, false, false); r = a16[0] + a16[1];
        auto a32 = __builtin_amdgcn_permlane32_swap(r, r, false, false); r = a32[0] + a32[1];
        ST64(((u2){c, tlo}), r < 8u ? WS_MSG + rr * 256 + 64 + r * 8 : WS_OFF);
        STMP(29); }
    }
    if (master && w != 4) lds_wait<4>((LAS unsigned*)(lds + L_CNT) + 3, 1);
    if (master && w == 4) {
      PACE(MD2);
      bool ok = false;
#pragma nounroll
      for (unsigned it = 0; it < SPIN_CAP; ++it) {
        if (!ok) {
          u4 f0 = LD128(WS_FLAG + l * 32), f1 = LD128(WS_FLAG + l * 32 + 16);
          ok = f0[0] == tlo && f0[1] == thi && f0[2] == tlo && f0[3] == thi && f1[0] == tlo && f1[1] == thi && f1[2] == tlo && f1[3] == thi;
        }
        if (all64(ok)) break;
        __builtin_amdgcn_s_sleep(8);
        CLOB();
      }
      CLOB();
      if (l < 8) ST64(tgv, WS_MSG + l * 256);
      if (l == 0) ST64(tgv, WS_EP);
      STMP(30);
      lds_arrive((LAS unsigned*)(lds + L_CNT) + 3);
    }
    GL2(0, !master, G0A, 8);
    GL(1, !master);
    { WO_SETUP(); (void)vX; WOR(w, 32, 8); }
    GT(0, 12);
    GL(2, !half);
    GT(1, 12);
    u2 f;
    { const unsigned m_ = msg; ALD64(f, m_); }
    GL(3, !half);
    GT(2, 9);
    WAITV(8); PIN(f);
    if (!KEYOK(f)) {
#pragma nounroll
      for (unsigned it = 0; it < SPIN_CAP; ++it) {
        CLOB();
        f = LD64(msg);
        if (KEYOK(f)) break;
        __builtin_amdgcn_s_sleep(16);
      }
    }
    CLOB();
    STMP(9);
    if (w == 0) {
      if (l < 8) ((unsigned*)(lds + L_TAG + 16))[l] = f[0];
      if (l == 8) *(u2*)(lds + L_TAG) = tgv;
    }
    u4 df[2][8];
    u16* hb = (u16*)(lds + L_KX);
#define DT(b) do { _Pragma("unroll") for (int c_ = 0; c_ < 8; ++c_) PIN(dw[b][c_]); _Pragma("unroll") for (int c_ = 0; c_ < 8; ++c_) { TRFRAG(df[b][c_], dw[b], c_); PIN(df[b][c_]); } SBAR(); } while (0)
    {
      const unsigned key_ = (unsigned)__builtin_amdgcn_update_dpp((int)f[0], (int)f[0], 0x128, 0xf, 0xc, false);
      const int idx = (0xFFFF - (int)(key_ & 0xFFFF)) & 2047;
      u4 r0, r1, msx;
      { const u4 rsD = mkrs((const char*)wd + (size_t)hc * 64 * 2048 + w * 256, jm == 7 ? 0 : 63 * 2048 + 256);
        const unsigned xo = (idx * D + 128 * w + (help ? 64 : 0) + 32 * (np >> 3) + 8 * g) * 2, mo = WS_MS + idx * 16, o_ = g * 8 * 2048 + np * 16;
        asm volatile("buffer_load_dwordx4 %0, %2, %3, 0 offen\n buffer_load_dwordx4 %1, %2, %3, 0 offen offset:128" : "=&v"(r0), "=&v"(r1) : "v"(xo), "s"(rsR) : "memory");
        ALD128(msx, mo);
#pragma unroll
        for (int i = 0; i < 16; ++i)
          asm volatile("buffer_load_dwordx4 %0, %1, %2, %3 offen offset:%4 nt" : "=v"(dw[i >> 3][i & 7]) : "v"(o_), "s"(rsD), "s"((unsigned)((i >> 3) * 65536 + (i & 6) * 2048)), "i"((i & 1) * 2048) : "memory"); }
      WAITV(16); PIN(r0); PIN(r1);
#pragma unroll
      for (int i = 0; i < 8; ++i) PIN(gw[3][i]);
      f4 acc[4];
#pragma unroll
      for (int c2 = 0; c2 < 4; ++c2) acc[c2] = (f4)(0.f);
      PIN(msx);
      const float m = hi(msx[0]), rs = U2F(msx[2]);
#pragma unroll
      for (int b2 = 0; b2 < 2; ++b2) {
        if (b2 && half) break;
        u4 xl[2], xh[2];
#pragma unroll
        for (int e = 0; e < 4; ++e) {
          const unsigned r = b2 ? r1[e] : r0[e];
          const unsigned v = pk2((lo(r) - m) * rs, (hi(r) - m) * rs);
          const unsigned o = (unsigned)__builtin_amdgcn_update_dpp(0, (int)v, 0x128, 0xf, 0xf, false);
          xl[0][e] = np < 8 ? v : 0u; xh[0][e] = np < 8 ? 0u : o; xl[1][e] = np < 8 ? o : 0u; xh[1][e] = np < 8 ? 0u : v;
        }
#pragma unroll
        for (int it = 0; it < 2; ++it)
#pragma unroll
          for (int c = 0; c < 8; ++c) {
            if (2 * b2 + it == 3) { TRFRAG(gf[3][c], gw[3], c); }
            acc[c >> 1] = MFMA16(BC8((c & 1) ? xh[it] : xl[it]), BC8(gf[2 * b2 + it][c]), acc[c >> 1]);
          }
        SBAR();
      }
      STMP(12);
      red += 3072;
#pragma unroll
      for (int c2 = 0; c2 < 4; ++c2) *(f4*)(red + (w * 4 + c2) * 256 + l * 4) = acc[c2];
      SBAR();
      WAITV(8);
      DT(0);
      LBAR();
      f2 sv = *(const f2*)(red + (w >> 1) * 256 + l * 4 + (w & 1) * 2);
#pragma unroll
      for (int i = 1; i < 8; ++i) sv += *(const f2*)(red + (i * 4 + (w >> 1)) * 256 + l * 4 + (w & 1) * 2);
      const unsigned gp = WS_GP + (xcd * 512 + w * 64 + l) * 16;
      if (jm == 7) ST128(((u4){F2U(sv[0]), tlo, F2U(sv[1]), thi}), help ? gp : WS_OFF);
      else {
      if (half) {
        u4 pv;
#pragma nounroll
        for (unsigned it = 0; it < SPIN_CAP; ++it) {
          pv = LD128(gp);
          if (all64(GBAD(pv) == 0u)) break;
          __builtin_amdgcn_s_sleep(8);
          CLOB();
        }
        sv += (f2){U2F(pv[0]), U2F(pv[2])};
      }
#pragma unroll
      for (int e = 0; e < 2; ++e) {
        const float gg = rbf(sv[e]), u1 = DPPF(gg, 0x128, 0xf);
        const float hv = rbf(gg * __builtin_amdgcn_rcpf(1.f + __builtin_amdgcn_exp2f(gg * -1.44269504f))) * u1;
        if (np < 8) hb[(4 * (g & 1) + 2 * (w & 1) + e) * 64 + 8 * np + 2 * (w >> 1) + (g >> 1)] = (u16)(pk2(hv, hv) & 0xFFFFu);
      }
      }
      SBAR();
      WAITV(0);
      DT(1);
    }
    LBAR();
    STMP(13);
    u4 hu;
    XOWN();
    { const unsigned ho = w < 4 ? WS_HU + xcd * 128 : WS_OFF; ALD128(hu, ho); }
    if (w >= 4) { WO_SETUP(); (void)vX; WOR(28 + w, 128, 4); }
    u4 yo[4];
    if (jm < 7) {
      f4 acd[4];
#pragma unroll
      for (int c = 0; c < 4; ++c) acd[c] = (f4)(0.f);
#pragma unroll
      for (int fb = 0; fb < 2; ++fb) {
        const u4 ha = *(const u4*)(hb + sl * 64 + 32 * fb + 8 * g);
        u4 hl, hh;
#pragma unroll
        for (int e = 0; e < 4; ++e) { hl[e] = np < 8 ? ha[e] : 0u; hh[e] = np < 8 ? 0u : ha[e]; }
#pragma unroll
        for (int c = 0; c < 4; ++c) acd[c] = MFMA16(BC8(hh), BC8(df[fb][c + 4]), MFMA16(BC8(hl), BC8(df[fb][c]), acd[c]));
      }
#pragma unroll
      for (int e = 0; e < 4; ++e) { yo[e] = tag4((f4){acd[0][e], acd[1][e], acd[2][e], acd[3][e]}, tlo); PIN(yo[e]); }
    }
#pragma unroll
    for (int e = 0; e < 4; ++e) ST128(yo[e], jm < 7 ? WS_YP + (hc * 8 + 4 * (g & 1) + e) * 4096 + w * 512 + (g >> 1) * 256 + np * 16 : WS_OFF);
    SBAR();
    STMP(14);
    WAITV(4); PIN(hu); PIN(st2[0]); PIN(st2[1]);
    if (w >= 4) lds_arrive((LAS unsigned*)(lds + L_CNT) + 2);
    else {
      OINIT();
      if (!all64(hu[0] == tlo && hu[1] == thi)) {
#pragma nounroll
        for (unsigned it = 0; it < SPIN_CAP; ++it) {
          CLOB();
          hu = LD128(WS_HU + xcd * 128); PIN(hu);
          if (all64(hu[0] == tlo && hu[1] == thi)) break;
          __builtin_amdgcn_s_sleep(16);
        }
      }
      tpoll = RFL(hu[2]);
      const unsigned now = (unsigned)__builtin_amdgcn_s_memrealtime(); if (tpoll - now > PDK) tpoll = now;
    }
    STMP(21);
    CPE(4);
    STMP(20);
  }

  LANE();
  LAS unsigned* cnt = (LAS unsigned*)(lds + L_CNT);
  if (ow >= 4) {
#define YD 16
    lds_wait<4>(cnt + 4, 1);
    PACE(YD);
    const unsigned mykey = ((const unsigned*)(lds + L_TAG + 16))[sl], tlo = RFL(*(const unsigned*)(lds + L_TAG));
#pragma unroll 1
    for (int i = ow - 4; i < 8; i += 4) {
      const unsigned kk = (unsigned)__builtin_amdgcn_readlane((int)mykey, i);
      const int ti = 0xFFFF - (int)(kk & 0xFFFF);
      if ((ti >> 7) != mb2) continue;
      const unsigned yb = WS_YP + (g * 14 * 8 + i) * 4096 + (nb >> 1) * 512 + (np >> 3) * 256 + ((nb & 1) * 8 + sl) * 16;
      f4 y;
#pragma nounroll
      for (unsigned it = 0; it < SPIN_CAP; ++it) {
        u4 yv[14];
#pragma unroll
        for (int k = 0; k < 14; ++k) yv[k] = LD128(yb + k * 32768);
        SBAR();
        unsigned bad = 0;
        y = untag4(yv[0], tlo, bad);
#pragma unroll
        for (int k = 1; k < 14; ++k) { const f4 t = untag4(yv[k], tlo, bad); y = (f4){add1(y[0], t[0]), add1(y[1], t[1]), add1(y[2], t[2]), add1(y[3], t[3])}; }
        if (tagok(bad)) break;
        __builtin_amdgcn_s_sleep(32);
        CLOB();
      }
#pragma unroll
      for (int e = 0; e < 4; ++e) {
        auto a16 = __builtin_amdgcn_permlane16_swap(F2U(y[e]), F2U(y[e]), false, false);
        y[e] = add1(U2F(a16[0]), U2F(a16[1]));
        auto a32 = __builtin_amdgcn_permlane32_swap(F2U(y[e]), F2U(y[e]), false, false);
        y[e] = add1(U2F(a32[0]), U2F(a32[1]));
      }
      const unsigned mono = kk >> 16;
      const float rv = U2F(((mono & 0x8000u) ? (mono & 0x7FFFu) : (~mono & 0xFFFFu)) << 16);
      const float sg = rbf(__builtin_amdgcn_rcpf(1.f + __builtin_amdgcn_exp2f(rv * -1.44269504f)));
#pragma unroll
      for (int e = 0; e < 4; ++e) y[e] = rbf(mul1(rbf(y[e]), sg));
      if (g == 0) *(f4*)(lds + L_YS + i * 256 + sl * 32 + (np >> 3) * 16) = y;
    }
    if (ow == 4) ((unsigned*)(lds + L_YS + 2048))[l] = 0u;
    lds_arrive(cnt);
    CPE(5);
    STMP(32);
    STDUMP();
    return;
  }
  const u2 tg = *(const u2*)(lds + L_TAG);
  const int trow = 0xFFFF - (int)(((const unsigned*)(lds + L_TAG + 16))[sl] & 0xFFFFu);
  const unsigned hit = (unsigned)__builtin_amdgcn_ballot_w64((trow >> 5) == mb2 * 4 + ow) & 0xFFu;
  unsigned ya[2] = {8u, 8u}, xw[4], xr_ = ldsb + L_XT + (ow * 32 + rr) * 128 + ((sl ^ rr) * 16), oo[4], cA = ldsb + L_CNT;
  if (hit) {
#pragma unroll 1
    for (int i = 0; i < 8; ++i) { const int row = __builtin_amdgcn_readlane(trow, i) - mb2 * 128 - ow * 32 - np; ya[0] = row == 0 ? i : ya[0]; ya[1] = row == 16 ? i : ya[1]; }
  }
#pragma unroll
  for (int i = 0; i < 4; ++i) { xw[i] = ldsb + L_XT + (ow * 32 + np) * 128 + (g & 1) * 8 + (((2 * i + (g >> 1)) ^ sl) * 16);
    oo[i] = (mb2 * 128 + ow * 32 + 8 * i + rr) * 2048 + nb * 128 + sl * 16; PIN(xw[i]); PIN(oo[i]); }
  ya[0] = ldsb + L_YS + ya[0] * 256 + g * 16; ya[1] = ldsb + L_YS + ya[1] * 256 + g * 16;
  PIN(ya[0]); PIN(ya[1]); PIN(xr_); PIN(cA);
  const __amdgpu_buffer_rsrc_t ors = __builtin_amdgcn_make_buffer_rsrc(out, 0, S * D * 2, 0x00020000);
  unsigned woad[2][4];
  { const int q = np >> 2, p = l & 3, row = 4 * g + q;
#pragma unroll
    for (int ct = 0; ct < 4; ++ct) { woad[0][ct] = ldsb + L_WO + row * 128 + ((ct ^ ((row >> 1) & 3)) * 32) + p * 8; woad[1][ct] = woad[0][ct] + 65536; } }
  s4 b[2][16];
#define K3RD(hc, i) RDTR(b[(hc) & 1][i], woad[((hc) >> 2) & 1][((i) >> 1) & 3], ((((hc) & 3) * 4 + ((hc) >> 3) * 2 + ((i) >> 3)) * 4096 + ((i) & 1) * 2048))
  const unsigned flg = l < 32 ? WS_OFL + (mb2 * 32 + (np ^ (hx >> 1)) * 2 + g) * 128 : WS_OFF;
#pragma nounroll
  while ((int)((unsigned)__builtin_amdgcn_s_memrealtime() - tpoll) < 0) __builtin_amdgcn_s_sleep(1);
  STMP(26);
  unsigned rdy = 0;
#define OPOLL(MASK, SL) _Pragma("nounroll") for (unsigned it = 0; it < SPIN_CAP && (rdy & (MASK)) != (MASK); ++it) { \
    u2 f_ = LD64((((PM ? (MASK) & ~rdy : ~rdy) >> (l & 31)) & 1u) ? flg : WS_OFF); \
    rdy |= (unsigned)__builtin_amdgcn_ballot_w64(f_[0] == tg[0] && f_[1] == tg[1]); \
    if ((SL) && rdy == 0) __builtin_amdgcn_s_sleep(SL); \
    CLOB(); }
#define OG 16
  OPOLL((1u << (OG / 2)) - 1, OSL);
  CLOB();
  STMP(6);
#define OPF 3
#define OW0 6
#define OW 12
  u4 a[32]; u2 fl;
  const unsigned abase = WS_OF + (mb2 * 8 + ow * 2) * 32768 + l * 16, abase1 = abase + 32768;
#define OL1(f) a[(f) & 31] = __builtin_amdgcn_raw_buffer_load_b128(wrs, (((f) & 1) ? abase1 : abase) + (((f) >> 5) * 2 + (((f) >> 1) & 1)) * 1024, ((((f) >> 2) & 7) * 4096) ^ (hx * 1024), 16)
  unsigned mxo;
#define OMX(hc, k) mxo = mx3((k) ? mxo : 0u, a[(hc) * 4 + ((k) >> 1)][((k) & 1) * 2], a[(hc) * 4 + ((k) >> 1)][((k) & 1) * 2 + 1])
#define OFIX(hc) do { if (__builtin_expect(!clean(mxo), 0)) { NFIX(); _Pragma("nounroll") for (unsigned it = 0; it < SPIN_CAP; ++it) { CLOB(); \
      OL1((hc) * 4); OL1((hc) * 4 + 1); OL1((hc) * 4 + 2); OL1((hc) * 4 + 3); \
      if (clean(mx4(mx4(mx4(mx4(0u, a[(hc) * 4]), a[(hc) * 4 + 1]), a[(hc) * 4 + 2]), a[(hc) * 4 + 3]))) break; } } } while (0)
#define NFIX()
#define OMK(f) ((unsigned)((1ull << (((f) + OG) / 2)) - 1))
#define OLD(f) do { if ((f) < 64) { if ((f) && !((f) % OG) && (rdy & OMK(f)) != OMK(f)) { rdy |= (unsigned)__builtin_amdgcn_ballot_w64(fl[0] == tg[0] && fl[1] == tg[1]); OPOLL(OMK(f), 0); CLOB(); } \
    OL1(f); \
    if (((f) % OG) == OPF && (f) < 64 - OG) fl = LD64((((PM == 1 ? OMK((f) - OPF + OG) & ~rdy : ~rdy) >> (l & 31)) & 1u) ? flg : WS_OFF); } } while (0)
#pragma unroll
  for (int f = 0; f < OW0; ++f) { OLD(f); SBAR(); }
  lds_wait<0>(cnt + 2, j < 16 ? 7 : j < 24 ? 0 : 12);
  if (ow == 0) lds_arrive(cnt + 4);
  K3RD(0, 0); K3RD(0, 1); K3RD(0, 2); K3RD(0, 3); K3RD(0, 4); K3RD(0, 5); K3RD(0, 6); K3RD(0, 7);
  K3RD(0, 8); K3RD(0, 9); K3RD(0, 10); K3RD(0, 11); K3RD(0, 12); K3RD(0, 13); K3RD(0, 14); K3RD(0, 15);
#define OHI(c) ((OW0 + 2 * (c)) < (OW + (c)) ? (OW0 + 2 * (c)) : (OW + (c)))
#define K3R(i) (((i) ^ ((i) >> 1)) & 1)
#define K3C(hc, i) ((hc) * 4 + ((i) >> 2))
#define K3M(hc, i) do { if (!((i) & 1)) WAITL((hc) < 15 ? 14 : 14 - (i)); SBAR(); \
    oc[K3R(i)][((i) >> 1) & 3] = MFMA16(CAT(b[(hc) & 1][(i) & ~1], b[(hc) & 1][(i) | 1]), BC8(a[((hc) * 4 + ((i) >> 3) * 2 + K3R(i)) & 31]), oc[K3R(i)][((i) >> 1) & 3]); SBAR(); \
    if ((hc) < 15) { K3RD((hc) + 1, i); } \
    if (PO && (hc) < 7 && (i) >= 6 && (i) < 14) { OMX(((hc) + 1) & 7, ((i) - 6) & 7); PIN(mxo); } \
    if (PO && (hc) < 7 && (i) == 15) { SBAR(); OFIX(((hc) + 1) & 7); } \
    if (((i) & 3) == 3) { OLD(OHI(K3C(hc, i) + 1) - 1); } else if (((i) & 3) == 1 && OHI(K3C(hc, i) + 1) - OHI(K3C(hc, i)) == 2) { OLD(OHI(K3C(hc, i))); } \
    SBAR(); } while (0)
#define K3H(hc) do { K3M(hc, 0); K3M(hc, 1); K3M(hc, 2); K3M(hc, 3); K3M(hc, 4); K3M(hc, 5); K3M(hc, 6); K3M(hc, 7); \
    K3M(hc, 8); K3M(hc, 9); K3M(hc, 10); K3M(hc, 11); K3M(hc, 12); K3M(hc, 13); K3M(hc, 14); K3M(hc, 15); } while (0)
  if (PO) { OMX(0, 0); OMX(0, 1); OMX(0, 2); OMX(0, 3); OMX(0, 4); OMX(0, 5); OMX(0, 6); OMX(0, 7); OFIX(0); }
  STMP(28);
  K3H(0); STMP(27); K3H(1); K3H(2); K3H(3); K3H(4); K3H(5); K3H(6); K3H(7); STMP(31); STMC(42); K3H(8); K3H(9); K3H(10); K3H(11); K3H(12); K3H(13); K3H(14);
  f4 yv[2][4]; unsigned yf = 4u;
#define K3L(r, n) do { oc[r][(n) & 3] = MFMA16(CAT(b[1][2 * (n)], b[1][2 * (n) + 1]), BC8(a[28 + ((n) >> 2) * 2 + (r)]), oc[r][(n) & 3]); SBAR(); } while (0)
#define YRD() do { _Pragma("unroll") for (int i = 0; i < 8; ++i) RD128(yv[i >> 2][i & 3], ya[i >> 2], (i & 3) * 64); } while (0)
#define OWR(r, ct) do { const u2 v_ = (u2){pk2(oc[r][ct][0], oc[r][ct][1]), pk2(oc[r][ct][2], oc[r][ct][3])}; \
    asm volatile("ds_write_b64 %0, %1 offset:%2" :: "v"(xw[ct]), "v"(v_), "i"((r) * 2048) : "memory"); SBAR(); } while (0)
  u4 ov[4];
  WAITL(8); SBAR(); K3L(0, 0); K3L(0, 1); K3L(0, 2); K3L(0, 3); WAITL(0);
  if (hit) { RD32(yf, cA, 0); YRD(); }
  SBAR();
  K3L(0, 4); K3L(0, 5); K3L(0, 6); K3L(0, 7); K3L(1, 0); K3L(1, 1); K3L(1, 2); K3L(1, 3);
  if (hit) {
    K3L(1, 4); K3L(1, 5); K3L(1, 6); K3L(1, 7);
    WAITL(0); PIN(yf);
    if (RFL(yf) < 4u) { lds_wait<0>(cnt, 4); YRD(); WAITL(0); }
#pragma unroll
    for (int i = 0; i < 8; ++i) { PIN(yv[i >> 2][i & 3]); oc[i >> 2][i & 3] += yv[i >> 2][i & 3]; }
    OWR(0, 0); OWR(0, 1); OWR(0, 2); OWR(0, 3);
  } else {
    K3L(1, 4); OWR(0, 0); K3L(1, 5); OWR(0, 1); K3L(1, 6); OWR(0, 2); K3L(1, 7); OWR(0, 3);
  }
  STMP(7); STMC(43);
  RD128(ov[0], xr_, 0); RD128(ov[1], xr_, 1024);
  OWR(1, 0); OWR(1, 1); OWR(1, 2); OWR(1, 3);
  RD128(ov[2], xr_, 2048); RD128(ov[3], xr_, 3072);
  WAITL(6); PIN(ov[0]); PIN(ov[1]);
  __builtin_amdgcn_raw_buffer_store_b128(ov[0], ors, oo[0], 0, 16); __builtin_amdgcn_raw_buffer_store_b128(ov[1], ors, oo[1], 0, 16);
  SBAR();
  WAITL(0); PIN(ov[2]); PIN(ov[3]);
  __builtin_amdgcn_raw_buffer_store_b128(ov[2], ors, oo[2], 0, 16); __builtin_amdgcn_raw_buffer_store_b128(ov[3], ors, oo[3], 0, 16);
  CPE(6);
  STMP(16);
  STDUMP();
}

extern "C" void kernel_launch(void* const* d_in, const int* in_sizes, int n_in, void* d_out, int out_size,
                              void* d_ws, size_t ws_size, hipStream_t stream) {
  const u16* x = (const u16*)d_in[0];
  const u16* wqkv = (const u16*)d_in[1];
  const u16* wo = (const u16*)d_in[2];
  const u16* wr = (const u16*)d_in[4];
  const u16* wg = (const u16*)d_in[5];
  const u16* wu = (const u16*)d_in[6];
  const u16* wd = (const u16*)d_in[7];
  (void)hipFuncSetAttribute((const void*)fused, hipFuncAttributeMaxDynamicSharedMemorySize, K_LDS);
  fused<<<256, 512, K_LDS, stream>>>(x, wqkv, wo, wr, wg, wu, wd, (char*)d_ws, (u16*)d_out);
}
```

```cpp
#include <hip/hip_runtime.h>
#include <hip/hip_bf16.h>
#include <stdint.h>

typedef unsigned short u16;
typedef unsigned long long u64;
typedef short s4 __attribute__((ext_vector_type(4)));
typedef short s8 __attribute__((ext_vector_type(8)));
typedef float f4 __attribute__((ext_vector_type(4)));
typedef float f2 __attribute__((ext_vector_type(2)));
typedef unsigned u4 __attribute__((ext_vector_type(4)));
typedef unsigned u2 __attribute__((ext_vector_type(2)));
typedef __bf16 bf2 __attribute__((ext_vector_type(2)));
#define LAS __attribute__((address_space(3)))
#define DEV __device__ __forceinline__

DEV float U2F(unsigned u) { return __builtin_bit_cast(float, u); }
DEV unsigned F2U(float f) { return __builtin_bit_cast(unsigned, f); }
DEV float I2F(int u) { return __builtin_bit_cast(float, u); }
DEV int F2I(float f) { return __builtin_bit_cast(int, f); }

constexpr int S = 2048, D = 1024, F = 3584;

constexpr unsigned WS_EP = 0;
constexpr unsigned WS_FLAG = 4096;
constexpr unsigned WS_MSG = 6144;
constexpr unsigned WS_HU = 8192;
constexpr unsigned WS_MS = 32768;
constexpr unsigned WS_CS = 256u << 10;
constexpr unsigned WS_GP = 512u << 10;
constexpr unsigned WS_YP = 1280u << 10;
constexpr unsigned WS_KVP = 3328u << 10;
constexpr unsigned WS_OF = 6u << 20;
constexpr unsigned WS_OFL = 10u << 20;
constexpr unsigned WS_END = (10u << 20) + (128u << 10);
constexpr unsigned WS_OFF = 0x7FFF0000u;
constexpr unsigned KVS = 2304;
constexpr unsigned POISON = 0xFFFFFFFFu;

DEV unsigned pk2(float a, float b) { bf2 v; v[0] = (__bf16)a; v[1] = (__bf16)b; return __builtin_bit_cast(unsigned, v); }
DEV float lo(unsigned u) { return U2F(u << 16); }
DEV float hi(unsigned u) { return U2F(u & 0xFFFF0000u); }
DEV float rbf(float a) { return lo(pk2(a, a)); }
DEV float add1(float a, float b) { float r; asm("v_add_f32 %0, %1, %2" : "=v"(r) : "v"(a), "v"(b)); return r; }
DEV float mul1(float a, float b) { float r; asm("v_mul_f32 %0, %1, %2" : "=v"(r) : "v"(a), "v"(b)); return r; }
DEV unsigned san(unsigned u) { return min(u, 0xFFFFFFFEu); }
DEV u4 san4(u4 v) { v[0] = san(v[0]); v[1] = san(v[1]); v[2] = san(v[2]); v[3] = san(v[3]); return v; }
DEV unsigned mx3(unsigned a, unsigned b, unsigned c) { return max(max(a, b), c); }
DEV unsigned mx4(unsigned m, u4 v) { return mx3(mx3(m, v[0], v[1]), v[2], v[3]); }
DEV u4 tag4(f4 v, unsigned t) { u4 r; _Pragma("unroll") for (int e = 0; e < 4; ++e) r[e] = (F2U(v[e]) & 0xFFFFFF00u) | ((t >> (8 * e)) & 0xFFu); return r; }
DEV f4 untag4(u4 v, unsigned t, unsigned& bad) {
  f4 r; _Pragma("unroll") for (int e = 0; e < 4; ++e) { const unsigned u = v[e] ^ ((t >> (8 * e)) & 0xFFu); bad |= u; r[e] = U2F(u); } return r; }
DEV bool tagok(unsigned bad) { return __builtin_amdgcn_ballot_w64((bad & 0xFFu) != 0u) == 0ull; }
DEV bool clean(unsigned m) { return __builtin_amdgcn_ballot_w64(m == POISON) == 0ull; }
DEV bool all64(bool c) { return __builtin_amdgcn_ballot_w64(!c) == 0ull; }
DEV unsigned phi2(f2 a) {
  const f2 e = (f2){__builtin_amdgcn_fmed3f(__builtin_amdgcn_exp2f(a[0]), 0.f, 1.f), __builtin_amdgcn_fmed3f(__builtin_amdgcn_exp2f(a[1]), 0.f, 1.f)};
  const f2 r = __builtin_elementwise_fma((f2){fmaxf(a[0], 1.44269504e-6f), fmaxf(a[1], 1.44269504e-6f)}, (f2)(0.693147181f), e);
  return pk2(r[0], r[1]);
}
#define RD128(dst, addr, OFF) asm volatile("ds_read_b128 %0, %1 offset:%2" : "=v"(dst) : "v"(addr), "i"(OFF) : "memory")
#define RD32(dst, addr, OFF) asm volatile("ds_read_b32 %0, %1 offset:%2" : "=v"(dst) : "v"(addr), "i"(OFF) : "memory")
#define RDTR(dst, addr, OFF) asm volatile("ds_read_b64_tr_b16 %0, %1 offset:%2" : "=v"(dst) : "v"(addr), "i"(OFF) : "memory")
#define SBAR() __builtin_amdgcn_sched_barrier(0)
#define CAT(x, y) __builtin_shufflevector(x, y, 0, 1, 2, 3, 4, 5, 6, 7)
#define WAITV(n) asm volatile("s_waitcnt vmcnt(%0)" ::"i"(n) : "memory")
#define WAITL(n) asm volatile("s_waitcnt lgkmcnt(%0)" ::"i"(n) : "memory")
#define CLOB() asm volatile("" ::: "memory")
#define BAR() asm volatile("s_barrier" ::: "memory")
#define LBAR() asm volatile("s_waitcnt lgkmcnt(0)\n s_barrier" ::: "memory")
#define PIN(v) asm volatile("" : "+v"(v))
#define PACE(n) do { SBAR(); __builtin_amdgcn_s_sleep(n); SBAR(); } while (0)
#define RFL(v) ((unsigned)__builtin_amdgcn_readfirstlane(v))
DEV const char* uptr(const char* p) {
  u64 v = (u64)p;
  return (const char*)(((u64)RFL((unsigned)(v >> 32)) << 32) | RFL((unsigned)v));
}
DEV u4 mkrs(const char* p, unsigned bytes) {
  const u64 v = (u64)uptr(p);
  u4 r; r[0] = (unsigned)v; r[1] = (unsigned)(v >> 32) & 0xFFFFu; r[2] = RFL(bytes); r[3] = 0x00020000u;
  return r;
}
#define BLDS(voff, rs, so, OFF) asm volatile("buffer_load_dwordx4 %0, %1, %2 offen offset:%3 lds" ::"v"(voff), "s"(rs), "s"(so), "i"(OFF) : "memory")
#define MFMA16(a, b, c) __builtin_amdgcn_mfma_f32_16x16x32_bf16(a, b, c, 0, 0, 0)
#define DOT2(a, b, c) __builtin_amdgcn_fdot2_f32_bf16(__builtin_bit_cast(bf2, (unsigned)(a)), __builtin_bit_cast(bf2, (unsigned)(b)), c, false)
#define BC8(v) __builtin_bit_cast(s8, v)
#define LD128(off) __builtin_amdgcn_raw_buffer_load_b128(wrs, (off), 0, 16)
#define LD64(off) __builtin_amdgcn_raw_buffer_load_b64(wrs, (off), 0, 16)
#define ST128(v, off) __builtin_amdgcn_raw_buffer_store_b128((v), wrs, (off), 0, 16)
#define ST64(v, off) __builtin_amdgcn_raw_buffer_store_b64((v), wrs, (off), 0, 16)
#define ALD128(dst, off) asm volatile("buffer_load_dwordx4 %0, %1, %2, 0 offen sc1" : "=v"(dst) : "v"(off), "s"(rsW) : "memory")
#define ALD64(dst, off) asm volatile("buffer_load_dwordx2 %0, %1, %2, 0 offen sc1" : "=v"(dst) : "v"(off), "s"(rsW) : "memory")
constexpr unsigned SPIN_CAP = 1u << 20;

DEV void lds_arrive(LAS unsigned* c) {
  asm volatile("s_mov_b64 exec, 1\n ds_add_u32 %0, %1\n s_mov_b64 exec, -1" ::"v"((unsigned)(size_t)c), "v"(1u) : "memory");
}
template <int SL> DEV void lds_wait(LAS unsigned* c, unsigned target) {
#pragma nounroll
  while (RFL(__hip_atomic_load(c, __ATOMIC_RELAXED, __HIP_MEMORY_SCOPE_WORKGROUP)) < target) { if (SL) __builtin_amdgcn_s_sleep(SL); }
  CLOB();
}

#define STMP(idx)
#define STMC(idx)
#define STDUMP()
#define DPPF(v, ctrl, rm) I2F(__builtin_amdgcn_update_dpp(0, F2I(v), ctrl, rm, 0xf, false))
DEV unsigned wave_max(unsigned v) {
  v = max(v, (unsigned)__builtin_amdgcn_update_dpp(0, (int)v, 0x111, 0xf, 0xf, false));
  v = max(v, (unsigned)__builtin_amdgcn_update_dpp(0, (int)v, 0x112, 0xf, 0xf, false));
  v = max(v, (unsigned)__builtin_amdgcn_update_dpp(0, (int)v, 0x114, 0xf, 0xf, false));
  v = max(v, (unsigned)__builtin_amdgcn_update_dpp(0, (int)v, 0x118, 0xf, 0xf, false));
  v = max(v, (unsigned)__builtin_amdgcn_update_dpp(0, (int)v, 0x142, 0xa, 0xf, false));
  v = max(v, (unsigned)__builtin_amdgcn_update_dpp(0, (int)v, 0x143, 0xc, 0xf, false));
  return (unsigned)__builtin_amdgcn_readlane((int)v, 63);
}

constexpr int SLOT = 49152, L_B = 32768;
constexpr int L_EX = SLOT;
constexpr int L_ROWM = 159744;
constexpr int L_ROWS = L_ROWM + 1024;
constexpr int L_COL = L_ROWS + 1024;
constexpr int L_TAG = L_COL + 512;
constexpr int L_CNT = L_TAG + 64;
constexpr int L_XT = 0;
constexpr int L_KX = 16384;
constexpr int L_ZX = L_KX + 8192;
constexpr int L_YS = L_ZX + 256;
constexpr int L_WO = 28672;
constexpr int K_LDS = 163840;

#define TRFRAG(f, W, c) do { _Pragma("unroll") for (int q_ = 0; q_ < 4; ++q_) { unsigned e_ = W[2 * q_][(c) >> 1], o_ = W[2 * q_ + 1][(c) >> 1]; \
    f[q_] = ((c) & 1) ? __builtin_amdgcn_perm(o_, e_, 0x07060302u) : __builtin_amdgcn_perm(o_, e_, 0x05040100u); } } while (0)
constexpr u64 TAG_STEP = 0x9E3779B97F4A7C15ull;

struct FW { s8 a[8]; s4 b[8]; };
#define RA(G, s, u, i) RD128(G.a[i], aad[((s) % 3) == 2][u], (((s) % 3) & 1) * SLOT + (i) * 2048)
#define RB(G, s, u, n) RDTR(G.b[n], bad[((s) % 3) == 2][((n) >> 1) & 1], (((s) % 3) & 1) * SLOT + ((n) >> 2) * 8192 + (u) * 4096 + ((n) & 1) * 512)
#define FB(G, j) CAT(G.b[2 * (j)], G.b[2 * (j) + 1])
#define RW(G, s, u, n) do { if ((n) < 8) RB(G, s, u, (n) & 7); else RA(G, s, u, ((n) - 8) & 7); SBAR(); } while (0)
#define SNK(n) ((((n) >> 2) & 1) ? 3 - ((n) & 3) : (n) & 3)
#define MW(G, n) do { if (TRN) c[(n) >> 2][SNK(n)] = MFMA16(FB(G, SNK(n)), G.a[(n) >> 2], c[(n) >> 2][SNK(n)]); \
    else c[(n) >> 2][SNK(n)] = MFMA16(G.a[(n) >> 2], FB(G, SNK(n)), c[(n) >> 2][SNK(n)]); SBAR(); } while (0)
#define MFA(c_, a_, b_) asm volatile("v_mfma_f32_16x16x32_bf16 %0, %1, %2, %0" : "+v"(c_) : "v"(a_), "v"(b_))
#define MA(G, n) do { if (TRN) MFA(c[(n) >> 2][SNK(n)], FB(G, SNK(n)), G.a[(n) >> 2]); else MFA(c[(n) >> 2][SNK(n)], G.a[(n) >> 2], FB(G, SNK(n))); SBAR(); } while (0)
#define MW2(Fq, G, sn, un, n) do { MA(Fq, 2 * (n)); if ((n) < 12) RW(G, sn, un, (n) + ((n) + 2) / 3); MA(Fq, 2 * (n) + 1); if ((n) < 12 && (n) % 3 == 0) RW(G, sn, un, (n) + (n) / 3 + 1); } while (0)
#define HALFW(Fq, G, sn, un) do { MW2(Fq, G, sn, un, 0); MW2(Fq, G, sn, un, 1); MW2(Fq, G, sn, un, 2); MW2(Fq, G, sn, un, 3); MW2(Fq, G, sn, un, 4); MW2(Fq, G, sn, un, 5); MW2(Fq, G, sn, un, 6); MW2(Fq, G, sn, un, 7); \
    MW2(Fq, G, sn, un, 8); MW2(Fq, G, sn, un, 9); MW2(Fq, G, sn, un, 10); MW2(Fq, G, sn, un, 11); MW2(Fq, G, sn, un, 12); MW2(Fq, G, sn, un, 13); MW2(Fq, G, sn, un, 14); MW2(Fq, G, sn, un, 15); WAITL(0); SBAR(); } while (0)
#define STAGEW(s) do { HALFW(f0, f1, s, 1); BAR(); SBAR(); HALFW(f1, f0, (s) + 1, 0); } while (0)
#define KLOOP() \
      unsigned aad[2][2], bad[2][2]; \
      { LANE(); const int q = np >> 2, p = l & 3, f = ((q >> 1) & 1) | ((g & 1) << 1); \
        _Pragma("unroll") for (int u = 0; u < 2; ++u) { aad[0][u] = ldsb + (wm * 128 + np) * 128 + (((4 * u + g) ^ (np >> 1)) * 16); aad[1][u] = aad[0][u] + 2 * SLOT; \
          bad[0][u] = ldsb + L_B + (8 * g + q) * 128 + (((2 * wn + (TRN ? u : u ^ wm)) ^ f) * 32) + p * 8; bad[1][u] = bad[0][u] + 2 * SLOT; \
          PIN(aad[0][u]); PIN(aad[1][u]); PIN(bad[0][u]); PIN(bad[1][u]); } } \
      unsigned tR, tC; \
      { LANE(); tR = ldsb + L_ROWM + (wm * 128 + (TRN ? np : 4 * g)) * 4; tC = ldsb + L_COL + (wn * 32 + (TRN ? 4 * g : wm * 16 + np)) * 4; PIN(tR); PIN(tC); SBAR(); } \
      f4 c[8][4]; \
      _Pragma("unroll") for (int i = 0; i < 8; ++i) _Pragma("unroll") for (int jj = 0; jj < 4; ++jj) { c[i][jj] = (f4)(0.f); PIN(c[i][jj]); } \
      FW f0, f1; \
      BAR(); \
      STMP(1); STMC(40); \
      RW(f0, 0, 0, 0); RW(f0, 0, 0, 1); RW(f0, 0, 0, 8); RW(f0, 0, 0, 2); RW(f0, 0, 0, 3); RW(f0, 0, 0, 4); RW(f0, 0, 0, 5); RW(f0, 0, 0, 6); RW(f0, 0, 0, 7); \
      RW(f0, 0, 0, 9); RW(f0, 0, 0, 10); RW(f0, 0, 0, 11); RW(f0, 0, 0, 12); RW(f0, 0, 0, 13); RW(f0, 0, 0, 14); RW(f0, 0, 0, 15); \
      WAITL(0); SBAR(); \
      STAGEW(0); STAGEW(1); STAGEW(2); STAGEW(3); STAGEW(4); STAGEW(5); STAGEW(6); STAGEW(7); \
      STAGEW(8); STAGEW(9); STAGEW(10); STAGEW(11); STAGEW(12); STAGEW(13); STAGEW(14); \
      HALFW(f0, f1, 15, 1); if (TRN) BAR(); SBAR()
#define MT(n) do { MW(f1, n); TB(n); } while (0)
#define KLAST() do { MT(0); MT(1); MT(2); MT(3); MT(4); MT(5); MT(6); MT(7); MT(8); MT(9); MT(10); MT(11); MT(12); MT(13); MT(14); MT(15); \
      MT(16); MT(17); MT(18); MT(19); MT(20); MT(21); MT(22); MT(23); MT(24); MT(25); MT(26); MT(27); MT(28); MT(29); MT(30); MT(31); WAITL(0); } while (0)
#define H2(v, e0) ((f2){(v)[e0], (v)[(e0) + 1]})

#define WQ0 56
#define QROT2 1
#define MORD 3
#define FI 11
#define FORCE_SLOW 0
#define GP1 3
#define G0A 4
#define KD 90
#define KSL 2
#define OSL 1
#define TKP 13
#define CL0 6

__global__ __launch_bounds__(512, 2) void fused(const u16* __restrict__ x, const u16* __restrict__ wqkv, const u16* __restrict__ wo, const u16* __restrict__ wr,
                                                const u16* __restrict__ wg, const u16* __restrict__ wu, const u16* __restrict__ wd, char* ws,
                                                u16* __restrict__ out) {
  extern __shared__ __attribute__((aligned(16))) char lds[];
  const int w = __builtin_amdgcn_readfirstlane(threadIdx.x >> 6);
#define LANE() int l; asm volatile("v_mbcnt_lo_u32_b32 %0, -1, 0\n v_mbcnt_hi_u32_b32 %0, -1, %0" : "=v"(l)); \
  const int np = l & 15, g = l >> 4, rr = l >> 3, sl = l & 7; (void)np; (void)g; (void)rr; (void)sl
  STMP(0);
  const int gb = blockIdx.x, xcd = gb & 7, j = gb >> 3, xq = xcd >> 1;
  const unsigned ldsb = (unsigned)(size_t)(LAS char*)lds;
  const bool isQ = j >= 16;
  const int rg = (xcd & 1) * 4 + (j & 3), hp = xq * 4 + (j >> 2), qg = xq * 2 + ((j >> 2) & 1);
  const int hd0 = isQ ? qg * 4 : 32 + hp * 2, hd1 = isQ ? 2 : 32;
  const bool isB = w == 6;
  unsigned mb_, step, d1, d2, d3, so, lim; u4 rs;
#define VSET() unsigned v[4]; { const unsigned pitch = isB ? 6144 : 2048, sm = isB ? 2 : 7; \
    _Pragma("unroll") for (int i = 0; i < 4; ++i) v[i] = (8 * i + rr) * pitch + ((sl ^ ((isB ? rr : rr >> 1) & sm) ^ ((i & 1) << 2)) * 16) - i * 1024; }
#define QL 4
#define QROT 1
#define G4(s, q, dq) do { unsigned t_; asm volatile("s_add_u32 m0, %1, %2\n s_add_u32 %0, %3, %4" : "=s"(t_) : "s"(mb_), "i"(((s) % 3) * SLOT + (q) * 4096), "s"(so), "s"(dq) : "scc", "memory"); \
        BLDS(v[0], rs, t_, 0); BLDS(v[1], rs, t_, 1024); BLDS(v[2], rs, t_, 2048); BLDS(v[3], rs, t_, 3072); } while (0)
#define PA(s) do { G4(s, 0, 0u); G4(s, 1, d1); } while (0)
#define PB(s) do { G4(s, 2, d2); G4(s, 3, d3); asm volatile("s_add_u32 %0, %0, %1\n s_cmp_eq_u32 %0, %2\n s_cselect_b32 %0, 0, %0" : "+s"(so) : "s"(step), "s"(lim) : "scc"); } while (0)
  if (__builtin_expect(j < 24 && w >= 4 && w < 7, 1)) {
    LANE();
    rs = isB ? mkrs((const char*)wqkv + hd0 * 64, 1024 * 6144 - hd0 * 64) : mkrs((const char*)x + (size_t)(rg * 256 + (w - 4) * 128) * 2048, 128 * 2048);
    mb_ = RFL(ldsb + (w - 4) * 16384); step = isB ? 64 * 6144 : 128;
    d1 = isB ? 32 * 6144 : 65536; d2 = isB ? hd1 * 64 : 131072; d3 = d1 + d2;
    so = isQ ? (QROT + QROT2 * (qg & 1)) * step : 0; lim = QL * step;
    VSET();
    STMP(37);
    PA(0); PB(0); PA(1);
    STMP(38);
  }
  u4 rsC;
  { u64 pc; unsigned len;
    asm volatile("s_getpc_b64 %0\n.Lcp0:\n s_mov_b32 %1, .Lcp1-.Lcp0\n s_max_i32 %1, %1, .Lcp2-.Lcp0\n s_max_i32 %1, %1, .Lcp3-.Lcp0\n s_max_i32 %1, %1, .Lcp4-.Lcp0\n s_max_i32 %1, %1, .Lcp5-.Lcp0\n s_max_i32 %1, %1, .Lcp6-.Lcp0\n s_max_i32 %1, %1, 0" : "=&s"(pc), "=&s"(len) :: "scc");
    rsC = (u4){(unsigned)pc & ~127u, (unsigned)(pc >> 32) & 0xFFFFu, len, 0x00020000u}; }
#define CODE(n) asm volatile("s_mov_b32 m0, %2\n s_nop 0\n buffer_load_dwordx4 %0, %1, 0 offen lds" :: "v"((n) * 1024 + l * 16), "s"(rsC), "s"(RFL(ldsb + L_ROWM + 3072)) : "memory")
  if (j < 24 && w < 4) { LANE(); CODE(j * 4 + w); }
#define CPL(n) do { __builtin_amdgcn_s_waitcnt(0x0F70); asm volatile(".Lcp" #n ":"); } while (0)
#define CPE(n) asm volatile(".Lcp" #n ":")
  const __amdgpu_buffer_rsrc_t wrs = __builtin_amdgcn_make_buffer_rsrc(ws, 0, WS_END, 0x00020000);
  const int mb2 = (xcd & 1) * 8 + (xcd >> 2) * 4 + (j >> 3), nb = ((xcd >> 1) & 1) * 8 + (j & 7), lq = (mb2 + (mb2 >> 2)) & 3;
#define msg (WS_MSG + xcd * 256 + ((l & 8) ? 0 : 64 + sl * 8))
#define MSGOK(f) all64((((l & 8) ? ((f)[0] ^ tlo) | ((f)[1] ^ thi) : (f)[1] ^ tlo)) == 0u)
#define KEYOK(f) all64((l & 8) || (f)[1] == tlo)
#define GBAD(v) (((v)[1] ^ tlo) | ((v)[3] ^ thi))
#define WO_SETUP() const u4 rsO = mkrs((const char*)wo + nb * 128, 1024 * 2048 - nb * 128); const unsigned vW = rr * 2048 + ((sl ^ (((rr >> 1) & 3) << 1)) * 16)
#define DMA1(v, rs, m, so) asm volatile("s_mov_b32 m0, %2\n s_nop 0\n buffer_load_dwordx4 %0, %1, %3 offen lds" ::"v"(v), "s"(rs), "s"(RFL(m)), "s"(RFL(so)) : "memory")
#define WOL(n0, n1, st) _Pragma("nounroll") for (int n_ = (n0); n_ < (n1); n_ += (st)) DMA1(vW, rsO, ldsb + L_WO + n_ * 1024, n_ * 16384)
#define WOR(n0, n1, st) _Pragma("nounroll") for (int n_ = (n0); n_ < (n1); n_ += (st)) DMA1(vW, rsO, ldsb + L_WO + (n_ ^ (lq * 32)) * 1024, (n_ ^ (lq * 32)) * 16384)
  const int ow = isQ && j < 24 ? w ^ 4 : w;
  f4 oc[2][4];
  u4 st2[2];
  unsigned tpoll = 0;
#define TPOLL(d) tpoll = (unsigned)__builtin_amdgcn_s_memrealtime() + (d)
#define PDK 280
#define PDQ 80
#define XT(r, ct) (xt + (r) * 2048 + (((2 * (ct) + (g >> 1)) ^ sl) * 16))
#define S2OK() all64(((st2[0][1] ^ t_[0]) | (st2[0][3] ^ t_[1]) | (st2[1][1] ^ t_[0]) | (st2[1][3] ^ t_[1])) == 0u)
#define OINIT() do { PIN(st2[0]); PIN(st2[1]); const char* xt = lds + L_XT + (ow * 32 + np) * 128 + (g & 1) * 8; \
    { const u2 t_ = *(const u2*)(lds + L_TAG); if (__builtin_expect(!S2OK(), 0)) { _Pragma("nounroll") for (unsigned it = 0; it < SPIN_CAP; ++it) { CLOB(); ST2(); if (S2OK()) break; } } } \
    _Pragma("unroll") for (int r = 0; r < 2; ++r) { \
      const float c1 = 1.f + U2F(st2[r][2]), c0 = -hi(st2[r][0]) * U2F(st2[r][2]); \
      _Pragma("unroll") for (int ct = 0; ct < 4; ++ct) { const u2 v = *(const u2*)XT(r, ct); \
        oc[r][ct] = (f4){__builtin_fmaf(lo(v[0]), c1, c0), __builtin_fmaf(hi(v[0]), c1, c0), __builtin_fmaf(lo(v[1]), c1, c0), __builtin_fmaf(hi(v[1]), c1, c0)}; PIN(oc[r][ct]); } \
      } } while (0)
#define XOWN() do { const u4 rsX = mkrs((const char*)x + (size_t)mb2 * 128 * 2048 + nb * 128, 128 * 2048 - nb * 128); const unsigned vX = rr * 2048 + ((sl ^ rr) * 16); \
    if (ow < 4) _Pragma("unroll") for (int n_ = 0; n_ < 4; ++n_) DMA1(vX, rsX, ldsb + L_XT + (ow * 4 + n_) * 1024, (ow * 4 + n_) * 16384); \
    const u4 rsW = mkrs(ws, WS_END); const unsigned sto = WS_MS + (mb2 * 128 + (w & 3) * 32 + np) * 16, sto1 = sto + 256; ALD128(st2[0], sto); ALD128(st2[1], sto1); } while (0)
#define ST2() do { const unsigned sto = WS_MS + (mb2 * 128 + (w & 3) * 32 + np) * 16; st2[0] = LD128(sto); st2[1] = LD128(sto + 256); } while (0)

  if (j < 24) {
    if (w >= 4) {
      LANE();
      unsigned tlo = 0, thi = 0;
      WO_SETUP();
      u2 f = (u2){0u, 0u};
      if (w == 7) {
        const u4 rsW = mkrs(ws, WS_END);
        int nbar = 16;
#define TICK() do { if (nbar) { BAR(); --nbar; } } while (0)
        if (l < 16) ((unsigned*)(lds + L_CNT))[l] = 0u;
        WAITL(0);
        TICK();
        u2 ep; unsigned z0 = 0; PIN(z0);
        ALD64(ep, z0);
        { const u4 pz = (u4){POISON, POISON, POISON, POISON};
          if (isQ) {
            ST128(pz, WS_OFL + ((rg * 2 + (l >> 5)) * 32 + qg * 4) * 128 + (l & 31) * 16);
          } else {
            const unsigned kb = WS_KVP + (hp * 16 + rg) * KVS + l * 16;
            ST128(pz, kb); ST128(pz, kb + 1024); ST128(pz, kb + 8 * KVS); ST128(pz, kb + 8 * KVS + 1024);
            ST128(pz, l < 16 ? WS_KVP + (hp * 16 + rg + (l & 8)) * KVS + 2048 + sl * 16 : WS_OFF);
          } }
        SBAR();
        TICK(); TICK(); TICK();
        WAITV(0); PIN(ep);
        const u64 tag = (((u64)RFL(ep[1]) << 32) | RFL(ep[0])) + TAG_STEP;
        tlo = (unsigned)tag; thi = (unsigned)(tag >> 32);
        ST64(((u2){tlo, thi}), l == 0 ? WS_FLAG + gb * 8 : WS_OFF);
        STMP(8);
#pragma nounroll
        while (nbar > 16 - TKP) { TICK();
#define CPF2 10
          if (CPF2 && nbar == 16 - CPF2) { CODE(j * 4); CODE(j * 4 + 1); CODE(j * 4 + 2); CODE(j * 4 + 3); }
        }
        u4 ms[4], cs[8];
        const unsigned a0 = WS_MS + (rg * 256 + l) * 16, a2 = WS_CS + ((g * 96 + hd0) * 16 + np) * 16, a3 = a2 + hd1 * 256, a4 = a2 + 4 * 96 * 256, a5 = a3 + 4 * 96 * 256, a6 = msg;
#pragma nounroll
        for (unsigned it = 0; it < SPIN_CAP; ++it) {
#pragma unroll
          for (int i = 0; i < 4; ++i) asm volatile("buffer_load_dwordx4 %0, %1, %2, 0 offen offset:%3 sc1" : "=v"(ms[i]) : "v"(a0), "s"(rsW), "i"(i * 1024) : "memory");
#pragma unroll
          for (int i = 0; i < 8; ++i) asm volatile("buffer_load_dwordx4 %0, %1, %2, 0 offen offset:%3 sc1" : "=v"(cs[i]) : "v"(i < 2 ? a2 : i < 4 ? a3 : i < 6 ? a4 : a5), "s"(rsW), "i"((i & 1) * 256) : "memory");
          ALD64(f, a6);
          if (nbar > 1) TICK();
          if (nbar > 1 && it == 0) TICK();
          WAITV(0);
          unsigned bad = 0;
#pragma unroll
          for (int i = 0; i < 4; ++i) { PIN(ms[i]); PIN(cs[i]); PIN(cs[4 + i]); bad |= GBAD(ms[i]) | GBAD(cs[i]) | GBAD(cs[4 + i]); }
          PIN(f);
          if (all64(bad == 0u)) break;
        }
        STMP(9);
        float ct[2];
#pragma unroll
        for (int e = 0; e < 2; ++e) {
          float t[4];
#pragma unroll
          for (int i = 0; i < 4; ++i) t[i] = add1(U2F(cs[i][2 * e]), U2F(cs[4 + i][2 * e]));
#pragma unroll
          for (int i = 0; i < 2; ++i) { auto r = __builtin_amdgcn_permlane32_swap(F2U(t[i]), F2U(t[i + 2]), false, false); t[i] = add1(U2F(r[0]), U2F(r[1])); }
          auto r = __builtin_amdgcn_permlane16_swap(F2U(t[0]), F2U(t[1]), false, false);
          ct[e] = add1(U2F(r[0]), U2F(r[1])); PIN(ct[e]);
        }
#pragma unroll
        for (int i = 0; i < 4; ++i) { ms[i][0] = (ms[i][0] & 0xFFFF0000u) ^ 0x80000000u; ms[i][1] = F2U(mul1(U2F(ms[i][2]), 1.44269504f)); PIN(ms[i]); }
#pragma nounroll
        while (nbar) TICK();
#pragma unroll
        for (int i = 0; i < 4; ++i) { ((unsigned*)(lds + L_ROWM))[i * 64 + l] = ms[i][0]; ((unsigned*)(lds + L_ROWS))[i * 64 + l] = ms[i][1]; }
        ((float*)(lds + L_COL))[g * 32 + np] = ct[0]; ((float*)(lds + L_COL))[g * 32 + 16 + np] = ct[1];
        if (l == 8) *(u2*)(lds + L_TAG) = (u2){tlo, thi};
        if (!isQ) lds_arrive((LAS unsigned*)(lds + L_CNT) + 5);
      } else {
        VSET();
        WAITV(8); BAR(); PB(1); PA(2); WAITV(8); BAR(); STMP(44); PB(2); if (QL == 3) so = lim; PA(0); PB(0);
#define LS(s) do { if ((s) < 14) WAITV(16); else WAITV(0); BAR(); STMP(44 + (s)); if ((s) + 3 < 16) { if ((s) + 3 == QL) so = lim; PA(s); PB(s); } } while (0)
        LS(1); LS(2); LS(3); LS(4); LS(5); LS(6); LS(7); LS(8); LS(9); LS(10); LS(11); LS(12); LS(13); LS(14);
      }
#define KEYS() do { if (w == 7) { _Pragma("nounroll") for (unsigned it = 0; it < SPIN_CAP && !MSGOK(f); ++it) { f = LD64(msg); CLOB(); } \
        if (l < 8) ((unsigned*)(lds + L_TAG + 16))[l] = f[0]; \
        lds_arrive((LAS unsigned*)(lds + L_CNT) + 3); } } while (0)
      if (isQ) {
        if (w < 7) { WOL(20 + w - 4, WQ0, 3); }
        LBAR(); STMP(59);
        XOWN(); SBAR(); WOL(WQ0 + w - 4, 128, 4); WOL(w - 4, 20, 4);
        const unsigned tq = (unsigned)__builtin_amdgcn_s_memrealtime();
        WAITV(5); OINIT();
        KEYS();
        STMP(21);
        const unsigned sb = WS_KVP + (qg * 4 + (w & 1)) * 8 * KVS + ((w >> 1) & 1) * 1024 + l * 16;
        const unsigned sz = (w & 2) ? WS_OFF : WS_KVP + ((qg * 4 + (w & 1)) * 8 + (np >> 1)) * KVS + 2048 + (np & 1) * 64 + g * 16;
        u4 prm[2];
#pragma unroll
        for (int e = 0; e < 4; ++e) {
          const unsigned o = (np >> 2) == g ? ((np & 1) ? 0x3F800000u : 0x00003F80u) : 0u;
          prm[0][e] = e == (w & 2) + ((np >> 1) & 1) ? o : 0u; prm[1][e] = e == 2 - (w & 2) + ((np >> 1) & 1) ? o : 0u;
        }
        PIN(prm[0]); PIN(prm[1]);
        __builtin_amdgcn_s_setprio(2);
#pragma nounroll
        while ((unsigned)__builtin_amdgcn_s_memrealtime() - tq < KD) __builtin_amdgcn_s_sleep(1);
        lds_wait<1>((LAS unsigned*)(lds + L_CNT) + 3, 1);
        u4 v[16], zv[2]; f4 kg[4][2];
#define KVLD(gq) do { _Pragma("unroll") for (int p = 4 * (gq); p < 4 * (gq) + 4; ++p) v[p] = __builtin_amdgcn_raw_buffer_load_b128(wrs, sb, ((p & 7) + (p >> 3) * 16) * KVS, 16); } while (0)
#define ZLD(hh) zv[hh] = __builtin_amdgcn_raw_buffer_load_b128(wrs, sz, (hh) * 16 * KVS, 16)
        ZLD(0); KVLD(0); KVLD(1); SBAR(); if (GP1) PACE(GP1); ZLD(1); KVLD(2); KVLD(3); SBAR();
#pragma unroll
        for (int hh = 0; hh < 2; ++hh) {
          unsigned pend = 7;
#pragma nounroll
          for (unsigned it = 0; it < SPIN_CAP; ++it) {
#pragma unroll
            for (int gq = 2 * hh; gq < 2 * hh + 2; ++gq)
              if (pend & (1u << (gq & 1))) {
                f4 t0_ = (f4)(0.f), t1_ = (f4)(0.f); unsigned mx = 0;
#pragma unroll
                for (int p = 4 * gq; p < 4 * gq + 4; ++p) { mx = mx4(mx, v[p]); t0_ = MFMA16(BC8(prm[0]), BC8(v[p]), t0_); t1_ = MFMA16(BC8(prm[1]), BC8(v[p]), t1_); }
                kg[gq][0] = t0_; kg[gq][1] = t1_;
                if (clean(mx)) pend &= ~(1u << (gq & 1));
              }
            if ((pend & 4u) && clean(mx4(0u, zv[hh]))) pend &= ~4u;
            if (!pend) break;
            if (KSL) __builtin_amdgcn_s_sleep(KSL);
            CLOB();
            if (pend & 4u) ZLD(hh);
            if (pend & 1u) KVLD(2 * hh);
            if (pend & 2u) KVLD(2 * hh + 1);
            if (hh == 0) { ZLD(1); KVLD(2); KVLD(3); }
            SBAR();
          }
          if (hh) STMP(25); else STMP(4);
          const f4 k0 = kg[2 * hh][0] + kg[2 * hh + 1][0], k1 = kg[2 * hh][1] + kg[2 * hh + 1][1];
          *(u4*)(lds + L_KX + ((2 * hh + (w & 1)) * 2 + ((w >> 1) & 1)) * 1024 + l * 16) = (u4){pk2(k0[0], k0[1]), pk2(k0[2], k0[3]), pk2(k1[0], k1[1]), pk2(k1[2], k1[3])};
          if (!(w & 2)) {
            f4 z = __builtin_bit_cast(f4, zv[hh]);
#pragma unroll
            for (int e = 0; e < 4; ++e) { z[e] += DPPF(z[e], 0x122, 0xf); z[e] += DPPF(z[e], 0x124, 0xf); z[e] += DPPF(z[e], 0x128, 0xf); }
            if (np < 2) *(u2*)(lds + L_ZX + (2 * hh + (w & 1)) * 64 + g * 16 + np * 8) = (u2){pk2(z[0], z[1]), pk2(z[2], z[3])};
          }
          LBAR();
        }
        __builtin_amdgcn_s_setprio(0);
        CPL(3);
        TPOLL(PDQ);
      } else {
        if (w < 7) { WOL(32 + w, 128, 3); WAITV(0); STMP(35); lds_arrive((LAS unsigned*)(lds + L_CNT) + 2); }
        KEYS();
        __builtin_amdgcn_s_waitcnt(0x0F70);
      }
    } else {
      const int wm = w >> 1, wn = w & 1;
      if (!isQ) {
        constexpr bool TRN = false;
        unsigned cA = ldsb + L_CNT; PIN(cA);
        unsigned exw, exa, pka, pza, cB = cA + wn * 4, one1 = 1u; bool zsel;
        { LANE(); const unsigned kb = WS_KVP + ((hp * 2 + wn) * 8 + rg) * KVS;
          exw = ldsb + L_EX + l * 16 + w * 4096; exa = exw ^ 8192; pka = kb + wm * 1024 + l * 16; pza = wm == 0 && np < 2 ? kb + 2048 + np * 64 + g * 16 : WS_OFF; zsel = (np ^ wm) & 1;
          PIN(exw); PIN(exa); PIN(pka); PIN(pza); PIN(cB); PIN(one1); SBAR(); }
        KLOOP();
        float cc[4]; f4 rsk[8], mk[8]; unsigned tf;
        unsigned tC1 = tC ^ 64; PIN(tC1);
#define TB1(n) do { if ((n) < 4) RD32(cc[(n) & 3], ((n) & 1) ? tC1 : tC, ((n) >> 1) * 256); else if ((n) & 1) RD128(rsk[(((n) - 4) >> 1) & 3], tR, 1024 + (((n) - 4) >> 1) * 64); else RD128(mk[(((n) - 4) >> 1) & 3], tR, (((n) - 4) >> 1) * 64); } while (0)
#define TB(n) do { if ((n) == 8) RD32(tf, cA, 20); else if ((n) > 8 && (n) < 21) TB1(((n) - 9) % 12); SBAR(); } while (0)
        KLAST();
#undef TB
        PIN(tf);
        if (__builtin_expect(RFL(tf) == 0u, 0)) {
          lds_wait<0>((LAS unsigned*)(lds + L_CNT) + 5, 1);
          TB1(0); TB1(1); TB1(2); TB1(3); TB1(4); TB1(5); TB1(6); TB1(7); TB1(8); TB1(9); TB1(10); TB1(11); WAITL(0);
        }
#pragma unroll
        for (int i = 0; i < 4; ++i) { PIN(mk[i]); PIN(rsk[i]); PIN(cc[i]); }
#pragma unroll
        for (int i = 4; i < 8; ++i) { RD128(mk[i], tR, i * 64); RD128(rsk[i], tR, 1024 + i * 64); }
        SBAR();
        STMP(2); STMC(41);
        f4 kv[2][2], zz[2];
        kv[0][0] = kv[0][1] = kv[1][0] = kv[1][1] = zz[0] = zz[1] = (f4)(0.f);
        const u4 ones = (u4){0x3F803F80u, 0x3F803F80u, 0x3F803F80u, 0x3F803F80u};
        u4 kf[2], vf[2];
#define LN2(i, jj, e0) (__builtin_elementwise_fma(H2(mk[i], e0), (f2)(cc[jj]), H2(c[i][jj], e0)) * H2(rsk[i], e0))
#define KVM(n) do { if ((n) < 4) kv[(n) >> 1][(n) & 1] = MFMA16(BC8(kf[(n) >> 1]), BC8(vf[(n) & 1]), kv[(n) >> 1][(n) & 1]); else zz[(n) & 1] = MFMA16(BC8(kf[(n) & 1]), BC8(ones), zz[(n) & 1]); } while (0)
#pragma unroll
        for (int ip = 0; ip < 4; ++ip) {
          if (ip == 2) { WAITL(0);
#pragma unroll
            for (int i = 4; i < 8; ++i) { PIN(mk[i]); PIN(rsk[i]); } }
          f2 ak[8], av[8], ek[8];
#pragma unroll
          for (int n = 0; n < 8; ++n) { const int i = 2 * ip + ((n >> 1) & 1), e0 = (n & 1) * 2, t = n >> 2; ak[n] = LN2(i, t, e0); av[n] = LN2(i, 2 + t, e0); }
          SBAR();
#pragma unroll
          for (int n = 0; n < 8; ++n) {
            if (ip > 0 && n < 6) KVM(n);
            ek[n] = (f2){__builtin_amdgcn_fmed3f(__builtin_amdgcn_exp2f(ak[n][0]), 0.f, 1.f), __builtin_amdgcn_fmed3f(__builtin_amdgcn_exp2f(ak[n][1]), 0.f, 1.f)};
            ak[n] = (f2){fmaxf(ak[n][0], 1.44269504e-6f), fmaxf(ak[n][1], 1.44269504e-6f)};
            SBAR();
          }
#pragma unroll
          for (int n = 0; n < 8; ++n) { const f2 r = __builtin_elementwise_fma(ak[n], (f2)(0.693147181f), ek[n]); kf[n >> 2][n & 3] = pk2(r[0], r[1]); vf[n >> 2][n & 3] = pk2(av[n][0], av[n][1]); }
          SBAR();
        }
        KVM(1); KVM(3); KVM(4); KVM(5); KVM(0); KVM(2);
        STMP(19);
        const f4 zs = zsel ? zz[1] : zz[0];
        *(LAS f4*)(size_t)(exw + 1024) = kv[0][1]; *(LAS f4*)(size_t)exw = kv[1][1]; *(LAS f4*)(size_t)(exw + 2048) = zs;
        asm volatile("s_mov_b64 exec, 1\n ds_add_u32 %0, %1 offset:24\n s_mov_b64 exec, -1" ::"v"(cB), "v"(one1) : "memory");
        f4 e0, e1, e2;
#pragma nounroll
        do {
          RD32(tf, cB, 24); RD128(e0, exa, 0); RD128(e1, exa, 1024); RD128(e2, exa, 2048);
          WAITL(0); PIN(tf); PIN(e0); PIN(e1); PIN(e2);
        } while (RFL(tf) < 2u);
        e0 += kv[0][0]; e1 += kv[1][0];
        ST128(san4((u4){pk2(e0[0], e0[1]), pk2(e0[2], e0[3]), pk2(e1[0], e1[1]), pk2(e1[2], e1[3])}), pka);
        ST128(san4(__builtin_bit_cast(u4, (zs + e2) * 1.44269504f)), pza);
        SBAR();
        STMP(3);
        LANE();
        TPOLL(PDK);
        if (j == 1 && w == 0) { const u2 t_ = *(const u2*)(lds + L_TAG); ST128(((u4){t_[0], t_[1], tpoll, 0u}), l < 8 ? WS_HU + xcd * 128 + l * 16 : WS_OFF); }
        lds_arrive((LAS unsigned*)(lds + L_CNT) + 8);
        lds_wait<1>((LAS unsigned*)(lds + L_CNT) + 8, 4);
        XOWN(); SBAR();
        { WO_SETUP(); WOL(w, 36, 4); }
        WAITV(9); OINIT();
        WAITV(0);
        lds_arrive((LAS unsigned*)(lds + L_CNT) + 2);
        lds_wait<1>((LAS unsigned*)(lds + L_CNT) + 3, 1);
        CPL(1);
        STMP(20);
      } else {
        constexpr bool TRN = true;
        KLOOP();
        f4 cc[4]; float rs[8], mn[8];
#define TB(n) do { if ((n) < 4) RD128(cc[(n) & 3], tC, ((n) >> 1) * 256 + ((n) & 1) * 64); else if ((n) < 20) { if ((n) & 1) RD32(rs[(((n) - 4) >> 1) & 7], tR, 1024 + (((n) - 4) >> 1) * 64); else RD32(mn[(((n) - 4) >> 1) & 7], tR, (((n) - 4) >> 1) * 64); } SBAR(); } while (0)
        KLAST();
#undef TB
#pragma unroll
        for (int i = 0; i < 8; ++i) { PIN(mn[i]); PIN(rs[i]); PIN(cc[i & 3]); }
        STMP(2); STMC(41);
        LANE();
        u4 pf[8][2];
#pragma unroll
        for (int i = 0; i < 8; ++i)
#pragma unroll
          for (int hh = 0; hh < 2; ++hh) {
#pragma unroll
            for (int e = 0; e < 4; ++e) { const int jj = 2 * hh + (e >> 1), e0 = (e & 1) * 2;
              pf[i][hh][e] = phi2(__builtin_elementwise_fma(H2(cc[jj], e0), (f2)(mn[i]), H2(c[i][jj], e0)) * rs[i]); }
            PIN(pf[i][hh]);
          }
        const u2 tg = *(const u2*)(lds + L_TAG);
        const unsigned ofl = l < 8 ? WS_OFL + ((rg * 2 + wm) * 32 + (qg * 2 + wn) * 2) * 128 + sl * 16 : WS_OFF;
        unsigned oa[8];
#pragma unroll
        for (int i = 0; i < 8; ++i) { oa[i] = WS_OF + ((rg * 16 + wm * 8 + i) * 32 + qg * 4 + wn) * 1024 + l * 16; PIN(oa[i]); }
        STMP(19);
        BAR();
        STMP(22);
        u4 kA[2][2], zA[2];
#define KRD(hh) do { zA[hh] = *(const u4*)(lds + L_ZX + (2 * (hh) + wn) * 64 + g * 16); \
          kA[hh][0] = *(const u4*)(lds + L_KX + (2 * (hh) + wn) * 2048 + l * 16); kA[hh][1] = *(const u4*)(lds + L_KX + (2 * (hh) + wn) * 2048 + 1024 + l * 16); SBAR(); } while (0)
        KRD(0);
        f4 dn[16], x0[16], x1[16];
#define OM(n) do { dn[n] = MFMA16(BC8(zA[(n) >> 3]), BC8(pf[(n) & 7][(n) >> 3]), (f4)(1.44269504e-6f)); x0[n] = MFMA16(BC8(kA[(n) >> 3][0]), BC8(pf[(n) & 7][(n) >> 3]), (f4)(0.f)); \
          x1[n] = MFMA16(BC8(kA[(n) >> 3][1]), BC8(pf[(n) & 7][(n) >> 3]), (f4)(0.f)); } while (0)
        OM(0); OM(1);
#pragma unroll
        for (int n = 0; n < 16; ++n) {
          SBAR();
          const float inv = __builtin_amdgcn_rcpf(dn[n][0]);
          asm volatile("" :: "v"(dn[n]));
          if (n + 2 < 16 && n != 6 && n != 7) OM(n + 2);
          float y[8];
#pragma unroll
          for (int e = 0; e < 4; ++e) { y[e] = x0[n][e] * inv; y[4 + e] = x1[n][e] * inv; PIN(y[e]); PIN(y[4 + e]); }
          ST128(((u4){pk2(y[0], y[1]), pk2(y[2], y[3]), pk2(y[4], y[5]), pk2(y[6], y[7])}), oa[n & 7] + (n >> 3) * 2048);
          if (n == FI) { SBAR(); WAITV(FI - 7); ST128(((u4){tg[0], tg[1], tg[0], tg[1]}), ofl); SBAR(); STMP(24); }
          if (n == 7) { SBAR(); BAR(); KRD(1); OM(8); OM(9); }
        }
        SBAR();
        STMP(23);
        WAITV(0);
        ST128(((u4){tg[0], tg[1], tg[0], tg[1]}), ofl + 128);
        SBAR();
        CPL(2);
        STMP(5);
      }
    }
  } else {
    LANE();
    const int jm = j - 24, jsl = 7 - xcd, c0 = 4 - (int)(((size_t)wg >> 7) & 7) + xcd;
    const bool master = jm == 7 && jsl == 7, help = jm == 7 && jsl != 7, half = jm == 7 || jm == jsl;
    const int jc = help ? jsl : jm, hc = jc * 8 + ((c0 + jc) & 7);
    const int row0 = (xcd & 1) * 1024 + (xq * 8 + jm) * 32 + w * 4;
    u4 cw[12], xr[8], wv[2];
    const u4 rsW = mkrs(ws, WS_END), rsR = mkrs((const char*)x, S * D * 2), rsQ = mkrs((const char*)wqkv, D * 6144);
    const unsigned co = ((jm * 32 + w * 4 + g) * 3072 + xcd * 128 + np * 8) * 2;
#define CI(n) (((n) % 3) * 4 + (n) / 3)
#define CL(a, b) do { _Pragma("unroll") for (int n = a; n < b; ++n) \
      asm volatile("buffer_load_dwordx4 %0, %1, %2, %3 offen" : "=v"(cw[CI(n)]) : "v"(co), "s"(rsQ), "s"((unsigned)(((CI(n) & 3) * 768 + ((CI(n) >> 2) + 1) % 3) * 2048)) : "memory"); } while (0)
    if (w == 0 && l < 8) ((unsigned*)(lds + L_CNT))[l] = 0u;
    CL(0, MORD);
    LBAR();
    u2 ep;
    { const unsigned xo = row0 * 2048 + l * 16, l16 = l * 16; unsigned z0 = 0; PIN(z0);
#pragma unroll
      for (int i = 0; i < 8; ++i) { const int n = (i & 3) * 2 + (i >> 2);
        asm volatile("buffer_load_dwordx4 %0, %1, %2, %3 offen offset:%4" : "=v"(xr[n]) : "v"(xo), "s"(rsR), "s"((unsigned)((n >> 2) * 4096)), "i"((n & 3) * 1024) : "memory"); }
      asm volatile("global_load_dwordx4 %0, %2, %3\n global_load_dwordx4 %1, %2, %3 offset:1024" : "=&v"(wv[0]), "=&v"(wv[1]) : "v"(l16), "s"(uptr((const char*)wr)) : "memory");
      ALD64(ep, z0); }
    CL(MORD, CL0);
    WAITV(CL0 - MORD);
#pragma unroll
    for (int i = 0; i < 8; ++i) PIN(xr[i]);
    PIN(wv[0]); PIN(wv[1]); PIN(ep);
    const u64 tag = (((u64)RFL(ep[1]) << 32) | RFL(ep[0])) + TAG_STEP;
    const unsigned tlo = (unsigned)tag, thi = (unsigned)(tag >> 32);
    const u2 tgv = (u2){tlo, thi};
    {
      float a[13];
#pragma unroll
      for (int i = 0; i < 13; ++i) a[i] = 0.f;
      unsigned one2 = 0x3F803F80u; PIN(one2);
#pragma unroll
      for (int jj = 0; jj < 8; ++jj) {
        const unsigned wv_ = wv[jj >> 2][jj & 3];
#pragma unroll
        for (int i = 0; i < 4; ++i) {
          const unsigned xv = xr[2 * i + (jj >> 2)][jj & 3];
          a[3 * i] = DOT2(xv, one2, a[3 * i]); a[3 * i + 1] = DOT2(xv, xv, a[3 * i + 1]); a[3 * i + 2] = DOT2(xv, wv_, a[3 * i + 2]);
        }
        a[12] = DOT2(wv_, one2, a[12]);
      }
#define RSTEP(n, ctrl) _Pragma("unroll") for (int i = 0; i < n; ++i) a[i] += DPPF(a[i], ctrl, 0xf)
      RSTEP(13, 0xB1); RSTEP(13, 0x4E);
#pragma unroll
      for (int i = 0; i < 3; ++i) { const float u0 = (l & 1) ? a[3 + i] : a[i], u1 = (l & 1) ? a[9 + i] : a[6 + i]; a[i] = (l & 2) ? u1 : u0; }
      a[3] = a[12];
      RSTEP(4, 0x124); RSTEP(4, 0x128);
#pragma unroll
      for (int i = 0; i < 4; ++i) {
        auto s16 = __builtin_amdgcn_permlane16_swap(F2U(a[i]), F2U(a[i]), false, false);
        a[i] = U2F(s16[0]) + U2F(s16[1]);
        auto s32 = __builtin_amdgcn_permlane32_swap(F2U(a[i]), F2U(a[i]), false, false);
        a[i] = U2F(s32[0]) + U2F(s32[1]);
      }
      const float mf = a[0] * (1.f / D), m = rbf(mf);
      const float var = rbf(fmaxf(a[1] * (1.f / D) - mf * mf, 0.f));
      const float rs = __builtin_amdgcn_rcpf(rbf(__builtin_amdgcn_sqrtf(rbf(var + 1e-5f))));
      const float dsc = rs * (a[2] - m * a[3]);
      ST128(((u4){pk2(dsc, m), tlo, F2U(rs), thi}), l < 4 ? WS_MS + (row0 + l) * 16 : WS_OFF);
    }
    SBAR();
    STMP(18);
    CL(CL0, 12);
    u4 gw[4][8], gf[4][8], dw[2][8];
    const u16* wp = ((np >> 3) ? wu : wg) + (size_t)(128 * w + (help ? 64 : 0) + 8 * g) * F + 64 * hc + 8 * sl;
#define GL(b, on) GL2(b, on, 0, 8)
#define GL2(b, on, i0, i1) do { const u16* p_ = (on) ? wp + (size_t)(32 * (b)) * F : wr; const unsigned st_ = (on) ? F : 0; \
      _Pragma("unroll") for (int i_ = i0; i_ < i1; ++i_) asm volatile("global_load_dwordx4 %0, %1, off nt" : "=v"(gw[b][i_]) : "v"(p_ + (size_t)(i_ * st_)) : "memory"); } while (0)
#define GT(b, NV) do { WAITV(NV); _Pragma("unroll") for (int c_ = 0; c_ < 8; ++c_) PIN(gw[b][c_]); _Pragma("unroll") for (int c_ = 0; c_ < 8; ++c_) { TRFRAG(gf[b][c_], gw[b], c_); PIN(gf[b][c_]); } SBAR(); } while (0)
    GL2(0, !master, 0, G0A);
    WAITV(G0A);
#pragma unroll
    for (int i = 0; i < 12; ++i) PIN(cw[i]);
    char* scr = lds + L_WO + ((lq & 2) ? 0 : 65536);
    float* red = (float*)scr;
    {
      u4 sel8;
      const unsigned one = (np & 1) ? 0x3F800000u : 0x00003F80u;
#pragma unroll
      for (int e = 0; e < 4; ++e) sel8[e] = ((np >> 1) == e) ? one : 0u;
#pragma unroll
      for (int p = 0; p < 3; ++p) {
        f4 c = (f4)(0.f);
#pragma unroll
        for (int i = 0; i < 4; ++i) c = MFMA16(BC8(sel8), BC8(cw[p * 4 + i]), c);
        if (g < 2) *(f4*)(red + (w * 3 + p) * 128 + 8 * np + 4 * g) = c;
      }
    }
    lds_arrive((LAS unsigned*)(lds + L_CNT) + 2);
    if (w < 3) {
      lds_wait<1>((LAS unsigned*)(lds + L_CNT) + 2, 8);
      if (w == 0) ST64(tgv, l == 0 ? WS_FLAG + gb * 8 : WS_OFF);
      const float* rp = red + w * 128 + g * 32 + np;
      float c0 = rp[0], c1 = rp[16];
#pragma unroll
      for (int i = 1; i < 8; ++i) { c0 += rp[i * 384]; c1 += rp[i * 384 + 16]; }
      ST128(((u4){F2U(c0), tlo, F2U(c1), thi}), WS_CS + ((jm * 96 + (w + 1) % 3 * 32 + xcd * 4) * 16 + l) * 16);
    }
    SBAR();
    STMP(17);
    if (master) {
      u4 rv[4];
      const unsigned ra = WS_MS + (w * 256 + l) * 16;
      unsigned bad = 15;
#pragma nounroll
      for (unsigned it = 0; it < SPIN_CAP; ++it) {
#pragma unroll
        for (int i = 0; i < 4; ++i) {
          const u4 v = LD128((bad >> i) & 1u ? ra + i * 1024 : WS_OFF);
          rv[i] = (bad >> i) & 1u ? v : rv[i];
        }
        bad = 0;
#pragma unroll
        for (int i = 0; i < 4; ++i) bad |= GBAD(rv[i]) ? 1u << i : 0u;
        if (all64(bad == 0u)) break;
        __builtin_amdgcn_s_sleep(4);
        CLOB();
      }
      STMP(10);
      unsigned key[4];
#pragma unroll
      for (int i = 0; i < 4; ++i) {
        const unsigned b = rv[i][0] << 16;
        key[i] = ((b ^ ((unsigned)((int)b >> 31) | 0x80000000u)) & 0xFFFF0000u) | (unsigned)(0xFFFF - (w * 256 + i * 64 + l));
      }
      unsigned* cand = (unsigned*)(lds + L_ROWM);
      LAS unsigned* cnt = (LAS unsigned*)(lds + L_CNT) + 1;
      { const unsigned m = wave_max(max(mx3(key[0], key[1], key[2]), key[3]));
        cand[l < 8 ? w * 8 + l : 96 + w] = l < 8 ? 0u : m; }
      LBAR();
      { const u4 m0 = *(const u4*)(cand + 96), m1 = *(const u4*)(cand + 100);
        const unsigned T = min(min(min(m0[0], m0[1]), min(m0[2], m0[3])), min(min(m1[0], m1[1]), min(m1[2], m1[3])));
        unsigned pos[4], n = 0, base;
#pragma unroll
        for (int i = 0; i < 4; ++i) { const u64 b = __builtin_amdgcn_ballot_w64(key[i] >= T);
          pos[i] = __builtin_amdgcn_mbcnt_hi((unsigned)(b >> 32), __builtin_amdgcn_mbcnt_lo((unsigned)b, n)); n += __builtin_popcountll(b); }
        asm volatile("s_mov_b64 exec, 1\n ds_add_rtn_u32 %0, %1, %2\n s_mov_b64 exec, -1\n s_waitcnt lgkmcnt(0)" : "=v"(base) : "v"((unsigned)(size_t)cnt), "v"(n) : "memory");
        base = RFL(base);
#pragma unroll
        for (int i = 0; i < 4; ++i) cand[key[i] >= T ? min(base + pos[i], 72u) : 72u + np] = key[i]; }
      LBAR();
      STMP(11);
      if (RFL(*cnt) > 64u || FORCE_SLOW) {
#define CE(i, j) do { const unsigned hi_ = max(key[i], key[j]), lo_ = min(key[i], key[j]); key[i] = hi_; key[j] = lo_; } while (0)
        CE(0, 1); CE(2, 3); CE(0, 2); CE(1, 3); CE(1, 2);
        unsigned cv = 0;
#pragma nounroll
        for (int it = 0; it < 8; ++it) {
          const unsigned m = wave_max(key[0]);
          const bool win = key[0] == m;
          key[0] = win ? key[1] : key[0]; key[1] = win ? key[2] : key[1]; key[2] = win ? key[3] : key[2]; key[3] = win ? 0u : key[3];
          cv = sl == it ? m : cv;
        }
        if (l < 8) cand[w * 8 + l] = cv;
        LBAR();
      }
      {
        const unsigned c = cand[w * 8 + sl];
        const u4 c0 = *(const u4*)(cand + rr * 8), c1 = *(const u4*)(cand + rr * 8 + 4);
        unsigned r = 0;
#pragma unroll
        for (int e = 0; e < 4; ++e) r += (c0[e] > c ? 1u : 0u) + (c1[e] > c ? 1u : 0u);
        r += (unsigned)__builtin_amdgcn_update_dpp(0, (int)r, 0x128, 0xf, 0xf, false);
        auto a16 = __builtin_amdgcn_permlane16_swap(r, r, false, false); r = a16[0] + a16[1];
        auto a32 = __builtin_amdgcn_permlane32_swap(r, r, false, false); r = a32[0] + a32[1];
        ST64(((u2){c, tlo}), r < 8u ? WS_MSG + rr * 256 + 64 + r * 8 : WS_OFF);
        STMP(29); }
    }
    if (master && w != 4) lds_wait<4>((LAS unsigned*)(lds + L_CNT) + 3, 1);
    if (master && w == 4) {
      bool ok = false;
#pragma nounroll
      for (unsigned it = 0; it < SPIN_CAP; ++it) {
        if (!ok) {
          u4 f0 = LD128(WS_FLAG + l * 32), f1 = LD128(WS_FLAG + l * 32 + 16);
          ok = f0[0] == tlo && f0[1] == thi && f0[2] == tlo && f0[3] == thi && f1[0] == tlo && f1[1] == thi && f1[2] == tlo && f1[3] == thi;
        }
        if (all64(ok)) break;
        __builtin_amdgcn_s_sleep(8);
        CLOB();
      }
      CLOB();
      if (l < 8) ST64(tgv, WS_MSG + l * 256);
      if (l == 0) ST64(tgv, WS_EP);
      STMP(30);
      lds_arrive((LAS unsigned*)(lds + L_CNT) + 3);
    }
    GL2(0, !master, G0A, 8);
    GL(1, !master);
    { WO_SETUP(); WOR(w, 32, 8); }
    GT(0, 12);
    GL(2, !half);
    GT(1, 12);
    u2 f;
    { const unsigned m_ = msg; ALD64(f, m_); }
    GL(3, !half);
    GT(2, 9);
    WAITV(8); PIN(f);
    if (!KEYOK(f)) {
#pragma nounroll
      for (unsigned it = 0; it < SPIN_CAP; ++it) {
        CLOB();
        f = LD64(msg);
        if (KEYOK(f)) break;
        __builtin_amdgcn_s_sleep(16);
      }
    }
    CLOB();
    STMP(9);
    if (w == 0) {
      if (l < 8) ((unsigned*)(lds + L_TAG + 16))[l] = f[0];
      if (l == 8) *(u2*)(lds + L_TAG) = tgv;
    }
    u4 df[2][8];
    u16* hb = (u16*)(lds + L_KX);
#define DT(b) do { _Pragma("unroll") for (int c_ = 0; c_ < 8; ++c_) PIN(dw[b][c_]); _Pragma("unroll") for (int c_ = 0; c_ < 8; ++c_) { TRFRAG(df[b][c_], dw[b], c_); PIN(df[b][c_]); } SBAR(); } while (0)
    {
      const unsigned key_ = (unsigned)__builtin_amdgcn_update_dpp((int)f[0], (int)f[0], 0x128, 0xf, 0xc, false);
      const int idx = (0xFFFF - (int)(key_ & 0xFFFF)) & 2047;
      u4 r0, r1, msx;
      { const u4 rsD = mkrs((const char*)wd + (size_t)hc * 64 * 2048 + w * 256, jm == 7 ? 0 : 63 * 2048 + 256);
        const unsigned xo = (idx * D + 128 * w + (help ? 64 : 0) + 32 * (np >> 3) + 8 * g) * 2, mo = WS_MS + idx * 16, o_ = g * 8 * 2048 + np * 16;
        asm volatile("buffer_load_dwordx4 %0, %2, %3, 0 offen\n buffer_load_dwordx4 %1, %2, %3, 0 offen offset:128" : "=&v"(r0), "=&v"(r1) : "v"(xo), "s"(rsR) : "memory");
        ALD128(msx, mo);
#pragma unroll
        for (int i = 0; i < 16; ++i)
          asm volatile("buffer_load_dwordx4 %0, %1, %2, %3 offen offset:%4 nt" : "=v"(dw[i >> 3][i & 7]) : "v"(o_), "s"(rsD), "s"((unsigned)((i >> 3) * 65536 + (i & 6) * 2048)), "i"((i & 1) * 2048) : "memory"); }
      WAITV(16); PIN(r0); PIN(r1);
#pragma unroll
      for (int i = 0; i < 8; ++i) PIN(gw[3][i]);
      f4 acc[4];
#pragma unroll
      for (int c2 = 0; c2 < 4; ++c2) acc[c2] = (f4)(0.f);
      PIN(msx);
      const float m = hi(msx[0]), rs = U2F(msx[2]);
#pragma unroll
      for (int b2 = 0; b2 < 2; ++b2) {
        if (b2 && half) break;
        u4 xl[2], xh[2];
#pragma unroll
        for (int e = 0; e < 4; ++e) {
          const unsigned r = b2 ? r1[e] : r0[e];
          const unsigned v = pk2((lo(r) - m) * rs, (hi(r) - m) * rs);
          const unsigned o = (unsigned)__builtin_amdgcn_update_dpp(0, (int)v, 0x128, 0xf, 0xf, false);
          xl[0][e] = np < 8 ? v : 0u; xh[0][e] = np < 8 ? 0u : o; xl[1][e] = np < 8 ? o : 0u; xh[1][e] = np < 8 ? 0u : v;
        }
#pragma unroll
        for (int it = 0; it < 2; ++it)
#pragma unroll
          for (int c = 0; c < 8; ++c) {
            if (2 * b2 + it == 3) { TRFRAG(gf[3][c], gw[3], c); }
            acc[c >> 1] = MFMA16(BC8((c & 1) ? xh[it] : xl[it]), BC8(gf[2 * b2 + it][c]), acc[c >> 1]);
          }
        SBAR();
      }
      STMP(12);
      red += 3072;
#pragma unroll
      for (int c2 = 0; c2 < 4; ++c2) *(f4*)(red + (w * 4 + c2) * 256 + l * 4) = acc[c2];
      SBAR();
      WAITV(8);
      DT(0);
      LBAR();
      f2 sv = *(const f2*)(red + (w >> 1) * 256 + l * 4 + (w & 1) * 2);
#pragma unroll
      for (int i = 1; i < 8; ++i) sv += *(const f2*)(red + (i * 4 + (w >> 1)) * 256 + l * 4 + (w & 1) * 2);
      const unsigned gp = WS_GP + (xcd * 512 + w * 64 + l) * 16;
      if (jm == 7) ST128(((u4){F2U(sv[0]), tlo, F2U(sv[1]), thi}), help ? gp : WS_OFF);
      else {
      if (half) {
        u4 pv;
#pragma nounroll
        for (unsigned it = 0; it < SPIN_CAP; ++it) {
          pv = LD128(gp);
          if (all64(GBAD(pv) == 0u)) break;
          __builtin_amdgcn_s_sleep(8);
          CLOB();
        }
        sv += (f2){U2F(pv[0]), U2F(pv[2])};
      }
#pragma unroll
      for (int e = 0; e < 2; ++e) {
        const float gg = rbf(sv[e]), u1 = DPPF(gg, 0x128, 0xf);
        const float hv = rbf(gg * __builtin_amdgcn_rcpf(1.f + __builtin_amdgcn_exp2f(gg * -1.44269504f))) * u1;
        if (np < 8) hb[(4 * (g & 1) + 2 * (w & 1) + e) * 64 + 8 * np + 2 * (w >> 1) + (g >> 1)] = (u16)(pk2(hv, hv) & 0xFFFFu);
      }
      }
      SBAR();
      WAITV(0);
      DT(1);
    }
    LBAR();
    STMP(13);
    u4 hu;
    XOWN();
    { const unsigned ho = w < 4 ? WS_HU + xcd * 128 : WS_OFF; ALD128(hu, ho); }
    if (w >= 4) { WO_SETUP(); WOR(28 + w, 128, 4); }
    u4 yo[4];
    if (jm < 7) {
      f4 acd[4];
#pragma unroll
      for (int c = 0; c < 4; ++c) acd[c] = (f4)(0.f);
#pragma unroll
      for (int fb = 0; fb < 2; ++fb) {
        const u4 ha = *(const u4*)(hb + sl * 64 + 32 * fb + 8 * g);
        u4 hl, hh;
#pragma unroll
        for (int e = 0; e < 4; ++e) { hl[e] = np < 8 ? ha[e] : 0u; hh[e] = np < 8 ? 0u : ha[e]; }
#pragma unroll
        for (int c = 0; c < 4; ++c) acd[c] = MFMA16(BC8(hh), BC8(df[fb][c + 4]), MFMA16(BC8(hl), BC8(df[fb][c]), acd[c]));
      }
#pragma unroll
      for (int e = 0; e < 4; ++e) { yo[e] = tag4((f4){acd[0][e], acd[1][e], acd[2][e], acd[3][e]}, tlo); PIN(yo[e]); }
    }
#pragma unroll
    for (int e = 0; e < 4; ++e) ST128(yo[e], jm < 7 ? WS_YP + (hc * 8 + 4 * (g & 1) + e) * 4096 + w * 512 + (g >> 1) * 256 + np * 16 : WS_OFF);
    SBAR();
    STMP(14);
    WAITV(4); PIN(hu); PIN(st2[0]); PIN(st2[1]);
    if (w >= 4) lds_arrive((LAS unsigned*)(lds + L_CNT) + 2);
    else {
      OINIT();
      if (!all64(hu[0] == tlo && hu[1] == thi)) {
#pragma nounroll
        for (unsigned it = 0; it < SPIN_CAP; ++it) {
          CLOB();
          hu = LD128(WS_HU + xcd * 128); PIN(hu);
          if (all64(hu[0] == tlo && hu[1] == thi)) break;
          __builtin_amdgcn_s_sleep(16);
        }
      }
      tpoll = RFL(hu[2]);
      const unsigned now = (unsigned)__builtin_amdgcn_s_memrealtime(); if (tpoll - now > PDK) tpoll = now;
    }
    STMP(21);
    CPE(4);
    STMP(20);
  }

  LANE();
  LAS unsigned* cnt = (LAS unsigned*)(lds + L_CNT);
  if (ow >= 4) {
#define YD 16
    lds_wait<4>(cnt + 4, 1);
    PACE(YD);
    const unsigned mykey = ((const unsigned*)(lds + L_TAG + 16))[sl], tlo = RFL(*(const unsigned*)(lds + L_TAG));
#pragma unroll 1
    for (int i = ow - 4; i < 8; i += 4) {
      const unsigned kk = (unsigned)__builtin_amdgcn_readlane((int)mykey, i);
      const int ti = 0xFFFF - (int)(kk & 0xFFFF);
      if ((ti >> 7) != mb2) continue;
      const unsigned yb = WS_YP + (g * 14 * 8 + i) * 4096 + (nb >> 1) * 512 + (np >> 3) * 256 + ((nb & 1) * 8 + sl) * 16;
      f4 y;
#pragma nounroll
      for (unsigned it = 0; it < SPIN_CAP; ++it) {
        u4 yv[14];
#pragma unroll
        for (int k = 0; k < 14; ++k) yv[k] = LD128(yb + k * 32768);
        SBAR();
        unsigned bad = 0;
        y = untag4(yv[0], tlo, bad);
#pragma unroll
        for (int k = 1; k < 14; ++k) { const f4 t = untag4(yv[k], tlo, bad); y = (f4){add1(y[0], t[0]), add1(y[1], t[1]), add1(y[2], t[2]), add1(y[3], t[3])}; }
        if (tagok(bad)) break;
        __builtin_amdgcn_s_sleep(32);
        CLOB();
      }
#pragma unroll
      for (int e = 0; e < 4; ++e) {
        auto a16 = __builtin_amdgcn_permlane16_swap(F2U(y[e]), F2U(y[e]), false, false);
        y[e] = add1(U2F(a16[0]), U2F(a16[1]));
        auto a32 = __builtin_amdgcn_permlane32_swap(F2U(y[e]), F2U(y[e]), false, false);
        y[e] = add1(U2F(a32[0]), U2F(a32[1]));
      }
      const unsigned mono = kk >> 16;
      const float rv = U2F(((mono & 0x8000u) ? (mono & 0x7FFFu) : (~mono & 0xFFFFu)) << 16);
      const float sg = rbf(__builtin_amdgcn_rcpf(1.f + __builtin_amdgcn_exp2f(rv * -1.44269504f)));
#pragma unroll
      for (int e = 0; e < 4; ++e) y[e] = rbf(mul1(rbf(y[e]), sg));
      if (g == 0) *(f4*)(lds + L_YS + i * 256 + sl * 32 + (np >> 3) * 16) = y;
    }
    if (ow == 4) ((unsigned*)(lds + L_YS + 2048))[l] = 0u;
    lds_arrive(cnt);
    CPE(5);
    STMP(32);
    STDUMP();
    return;
  }
  const u2 tg = *(const u2*)(lds + L_TAG);
  const int trow = 0xFFFF - (int)(((const unsigned*)(lds + L_TAG + 16))[sl] & 0xFFFFu);
  const unsigned hit = (unsigned)__builtin_amdgcn_ballot_w64((trow >> 5) == mb2 * 4 + ow) & 0xFFu;
  unsigned ya[2] = {8u, 8u}, xw[4], xr_ = ldsb + L_XT + (ow * 32 + rr) * 128 + ((sl ^ rr) * 16), oo[4], cA = ldsb + L_CNT;
  if (hit) {
#pragma unroll 1
    for (int i = 0; i < 8; ++i) { const int row = __builtin_amdgcn_readlane(trow, i) - mb2 * 128 - ow * 32 - np; ya[0] = row == 0 ? i : ya[0]; ya[1] = row == 16 ? i : ya[1]; }
  }
#pragma unroll
  for (int i = 0; i < 4; ++i) { xw[i] = ldsb + L_XT + (ow * 32 + np) * 128 + (g & 1) * 8 + (((2 * i + (g >> 1)) ^ sl) * 16);
    oo[i] = (mb2 * 128 + ow * 32 + 8 * i + rr) * 2048 + nb * 128 + sl * 16; PIN(xw[i]); PIN(oo[i]); }
  ya[0] = ldsb + L_YS + ya[0] * 256 + g * 16; ya[1] = ldsb + L_YS + ya[1] * 256 + g * 16;
  PIN(ya[0]); PIN(ya[1]); PIN(xr_); PIN(cA);
  const __amdgpu_buffer_rsrc_t ors = __builtin_amdgcn_make_buffer_rsrc(out, 0, S * D * 2, 0x00020000);
  unsigned woad[2][4];
  { const int q = np >> 2, p = l & 3, row = 4 * g + q;
#pragma unroll
    for (int ct = 0; ct < 4; ++ct) { woad[0][ct] = ldsb + L_WO + row * 128 + ((ct ^ ((row >> 1) & 3)) * 32) + p * 8; woad[1][ct] = woad[0][ct] + 65536; } }
  s4 b[2][16];
#define K3RD(hc, i) RDTR(b[(hc) & 1][i], woad[((hc) >> 2) & 1][((i) >> 1) & 3], ((((hc) & 3) * 4 + ((hc) >> 3) * 2 + ((i) >> 3)) * 4096 + ((i) & 1) * 2048))
#define K3FIRST() do { lds_wait<0>(cnt + 2, wneed); K3RD(0, 0); K3RD(0, 1); K3RD(0, 2); K3RD(0, 3); K3RD(0, 4); K3RD(0, 5); K3RD(0, 6); K3RD(0, 7); \
    K3RD(0, 8); K3RD(0, 9); K3RD(0, 10); K3RD(0, 11); K3RD(0, 12); K3RD(0, 13); K3RD(0, 14); K3RD(0, 15); WAITL(0); \
    _Pragma("unroll") for (int i = 0; i < 16; ++i) PIN(b[0][i]); } while (0)
  const unsigned wneed = j < 16 ? 7 : j < 24 ? 0 : 12;
  const bool wlate = j >= 24 && (int)((unsigned)__builtin_amdgcn_s_memrealtime() + 60u - tpoll) > 0;
  if (!wlate) K3FIRST();
  const unsigned flg = l < 32 ? WS_OFL + (mb2 * 32 + np * 2 + g) * 128 : WS_OFF;
#pragma nounroll
  while ((int)((unsigned)__builtin_amdgcn_s_memrealtime() - tpoll) < 0) __builtin_amdgcn_s_sleep(1);
  STMP(26);
  unsigned rdy = 0;
#define OPOLL(MASK, SL) _Pragma("nounroll") for (unsigned it = 0; it < SPIN_CAP && (rdy & (MASK)) != (MASK); ++it) { \
    u2 f_ = LD64(((((MASK) & ~rdy) >> (l & 31)) & 1u) ? flg : WS_OFF); \
    rdy |= (unsigned)__builtin_amdgcn_ballot_w64(f_[0] == tg[0] && f_[1] == tg[1]); \
    if ((SL) && rdy == 0) __builtin_amdgcn_s_sleep(SL); \
    CLOB(); }
#define OG 16
  OPOLL((1u << (OG / 2)) - 1, OSL);
  CLOB();
  STMP(6);
#define OPF 3
#define OW0 6
#define OW 12
  u4 a[32]; u2 fl;
  const unsigned abase = WS_OF + (mb2 * 8 + ow * 2) * 32768 + l * 16, abase1 = abase + 32768;
#define OL1(f) a[(f) & 31] = __builtin_amdgcn_raw_buffer_load_b128(wrs, (((f) & 1) ? abase1 : abase) + (((f) >> 5) * 2 + (((f) >> 1) & 1)) * 1024, (((f) >> 2) & 7) * 4096, 16)
#define OMK(f) ((unsigned)((1ull << (((f) + OG) / 2)) - 1))
#define OLD(f) do { if ((f) < 64) { if ((f) && !((f) % OG) && (rdy & OMK(f)) != OMK(f)) { rdy |= (unsigned)__builtin_amdgcn_ballot_w64(fl[0] == tg[0] && fl[1] == tg[1]); OPOLL(OMK(f), 0); CLOB(); } \
    OL1(f); \
    if (((f) % OG) == OPF && (f) < 64 - OG) fl = LD64((((~rdy) >> (l & 31)) & 1u) ? flg : WS_OFF); } } while (0)
#pragma unroll
  for (int f = 0; f < OW0; ++f) { OLD(f); SBAR(); }
  if (ow == 0) lds_arrive(cnt + 4);
  if (wlate) K3FIRST();
#define OHI(c) ((OW0 + 2 * (c)) < (OW + (c)) ? (OW0 + 2 * (c)) : (OW + (c)))
#define K3R(i) (((i) ^ ((i) >> 1)) & 1)
#define K3C(hc, i) ((hc) * 4 + ((i) >> 2))
#define K3M(hc, i) do { if (!((i) & 1)) WAITL((hc) < 15 ? 14 : 14 - (i)); SBAR(); \
    oc[K3R(i)][((i) >> 1) & 3] = MFMA16(CAT(b[(hc) & 1][(i) & ~1], b[(hc) & 1][(i) | 1]), BC8(a[((hc) * 4 + ((i) >> 3) * 2 + K3R(i)) & 31]), oc[K3R(i)][((i) >> 1) & 3]); SBAR(); \
    if ((hc) < 15) { K3RD((hc) + 1, i); } \
    if (((i) & 3) == 3) { OLD(OHI(K3C(hc, i) + 1) - 1); } else if (((i) & 3) == 1 && OHI(K3C(hc, i) + 1) - OHI(K3C(hc, i)) == 2) { OLD(OHI(K3C(hc, i))); } \
    SBAR(); } while (0)
#define K3H(hc) do { K3M(hc, 0); K3M(hc, 1); K3M(hc, 2); K3M(hc, 3); K3M(hc, 4); K3M(hc, 5); K3M(hc, 6); K3M(hc, 7); \
    K3M(hc, 8); K3M(hc, 9); K3M(hc, 10); K3M(hc, 11); K3M(hc, 12); K3M(hc, 13); K3M(hc, 14); K3M(hc, 15); } while (0)
  STMP(28);
  K3H(0); STMP(27); K3H(1); K3H(2); K3H(3); K3H(4); K3H(5); K3H(6); K3H(7); STMP(31); STMC(42); K3H(8); K3H(9); K3H(10); K3H(11); K3H(12); K3H(13); K3H(14);
  f4 yv[2][4]; unsigned yf = 4u;
#define K3L(r, n) do { oc[r][(n) & 3] = MFMA16(CAT(b[1][2 * (n)], b[1][2 * (n) + 1]), BC8(a[28 + ((n) >> 2) * 2 + (r)]), oc[r][(n) & 3]); SBAR(); } while (0)
#define YRD() do { _Pragma("unroll") for (int i = 0; i < 8; ++i) RD128(yv[i >> 2][i & 3], ya[i >> 2], (i & 3) * 64); } while (0)
#define OWR(r, ct) do { const u2 v_ = (u2){pk2(oc[r][ct][0], oc[r][ct][1]), pk2(oc[r][ct][2], oc[r][ct][3])}; \
    asm volatile("ds_write_b64 %0, %1 offset:%2" :: "v"(xw[ct]), "v"(v_), "i"((r) * 2048) : "memory"); SBAR(); } while (0)
  u4 ov[4];
  WAITL(8); SBAR(); K3L(0, 0); K3L(0, 1); K3L(0, 2); K3L(0, 3); WAITL(0);
  if (hit) { RD32(yf, cA, 0); YRD(); }
  SBAR();
  K3L(0, 4); K3L(0, 5); K3L(0, 6); K3L(0, 7); K3L(1, 0); K3L(1, 1); K3L(1, 2); K3L(1, 3);
  if (hit) {
    K3L(1, 4); K3L(1, 5); K3L(1, 6); K3L(1, 7);
    WAITL(0); PIN(yf);
    if (RFL(yf) < 4u) { lds_wait<0>(cnt, 4); YRD(); WAITL(0); }
#pragma unroll
    for (int i = 0; i < 8; ++i) { PIN(yv[i >> 2][i & 3]); oc[i >> 2][i & 3] += yv[i >> 2][i & 3]; }
    OWR(0, 0); OWR(0, 1); OWR(0, 2); OWR(0, 3);
  } else {
    K3L(1, 4); OWR(0, 0); K3L(1, 5); OWR(0, 1); K3L(1, 6); OWR(0, 2); K3L(1, 7); OWR(0, 3);
  }
  STMP(7); STMC(43);
  RD128(ov[0], xr_, 0); RD128(ov[1], xr_, 1024);
  OWR(1, 0); OWR(1, 1); OWR(1, 2); OWR(1, 3);
  RD128(ov[2], xr_, 2048); RD128(ov[3], xr_, 3072);
  WAITL(6); PIN(ov[0]); PIN(ov[1]);
  __builtin_amdgcn_raw_buffer_store_b128(ov[0], ors, oo[0], 0, 16); __builtin_amdgcn_raw_buffer_store_b128(ov[1], ors, oo[1], 0, 16);
  SBAR();
  WAITL(0); PIN(ov[2]); PIN(ov[3]);
  __builtin_amdgcn_raw_buffer_store_b128(ov[2], ors, oo[2], 0, 16); __builtin_amdgcn_raw_buffer_store_b128(ov[3], ors, oo[3], 0, 16);
  CPE(6);
  STMP(16);
  STDUMP();
}

extern "C" void kernel_launch(void* const* d_in, const int* in_sizes, int n_in, void* d_out, int out_size,
                              void* d_ws, size_t ws_size, hipStream_t stream) {
  const u16* x = (const u16*)d_in[0];
  const u16* wqkv = (const u16*)d_in[1];
  const u16* wo = (const u16*)d_in[2];
  const u16* wr = (const u16*)d_in[4];
  const u16* wg = (const u16*)d_in[5];
  const u16* wu = (const u16*)d_in[6];
  const u16* wd = (const u16*)d_in[7];
  (void)hipFuncSetAttribute((const void*)fused, hipFuncAttributeMaxDynamicSharedMemorySize, K_LDS);
  fused<<<256, 512, K_LDS, stream>>>(x, wqkv, wo, wr, wg, wu, wd, (char*)d_ws, (u16*)d_out);
}
```

```cpp
#include <hip/hip_runtime.h>
#include <hip/hip_bf16.h>
#include <stdint.h>

typedef unsigned short u16;
typedef unsigned long long u64;
typedef short s4 __attribute__((ext_vector_type(4)));
typedef short s8 __attribute__((ext_vector_type(8)));
typedef float f4 __attribute__((ext_vector_type(4)));
typedef float f2 __attribute__((ext_vector_type(2)));
typedef unsigned u4 __attribute__((ext_vector_type(4)));
typedef unsigned u2 __attribute__((ext_vector_type(2)));
typedef __bf16 bf2 __attribute__((ext_vector_type(2)));
#define LAS __attribute__((address_space(3)))
#define DEV __device__ __forceinline__

DEV float U2F(unsigned u) { return __builtin_bit_cast(float, u); }
DEV unsigned F2U(float f) { return __builtin_bit_cast(unsigned, f); }
DEV float I2F(int u) { return __builtin_bit_cast(float, u); }
DEV int F2I(float f) { return __builtin_bit_cast(int, f); }

constexpr int S = 2048, D = 1024, F = 3584;

constexpr unsigned WS_EP = 0;
constexpr unsigned WS_FLAG = 4096;
constexpr unsigned WS_MSG = 6144;
constexpr unsigned WS_HU = 8192;
constexpr unsigned WS_MS = 32768;
constexpr unsigned WS_CS = 256u << 10;
constexpr unsigned WS_GP = 512u << 10;
constexpr unsigned WS_YP = 1280u << 10;
constexpr unsigned WS_KVP = 3328u << 10;
constexpr unsigned WS_OF = 6u << 20;
constexpr unsigned WS_OFL = 10u << 20;
constexpr unsigned WS_END = (10u << 20) + (128u << 10);
constexpr unsigned WS_OFF = 0x7FFF0000u;
constexpr unsigned KVS = 2304;
constexpr unsigned POISON = 0xFFFFFFFFu;

DEV unsigned pk2(float a, float b) { bf2 v; v[0] = (__bf16)a; v[1] = (__bf16)b; return __builtin_bit_cast(unsigned, v); }
DEV float lo(unsigned u) { return U2F(u << 16); }
DEV float hi(unsigned u) { return U2F(u & 0xFFFF0000u); }
DEV float rbf(float a) { return lo(pk2(a, a)); }
DEV float add1(float a, float b) { float r; asm("v_add_f32 %0, %1, %2" : "=v"(r) : "v"(a), "v"(b)); return r; }
DEV float mul1(float a, float b) { float r; asm("v_mul_f32 %0, %1, %2" : "=v"(r) : "v"(a), "v"(b)); return r; }
DEV unsigned san(unsigned u) { return min(u, 0xFFFFFFFEu); }
DEV u4 san4(u4 v) { v[0] = san(v[0]); v[1] = san(v[1]); v[2] = san(v[2]); v[3] = san(v[3]); return v; }
DEV unsigned mx3(unsigned a, unsigned b, unsigned c) { return max(max(a, b), c); }
DEV unsigned mx4(unsigned m, u4 v) { return mx3(mx3(m, v[0], v[1]), v[2], v[3]); }
DEV u4 tag4(f4 v, unsigned t) { u4 r; _Pragma("unroll") for (int e = 0; e < 4; ++e) r[e] = (F2U(v[e]) & 0xFFFFFF00u) | ((t >> (8 * e)) & 0xFFu); return r; }
DEV f4 untag4(u4 v, unsigned t, unsigned& bad) {
  f4 r; _Pragma("unroll") for (int e = 0; e < 4; ++e) { const unsigned u = v[e] ^ ((t >> (8 * e)) & 0xFFu); bad |= u; r[e] = U2F(u); } return r; }
DEV bool tagok(unsigned bad) { return __builtin_amdgcn_ballot_w64((bad & 0xFFu) != 0u) == 0ull; }
DEV bool clean(unsigned m) { return __builtin_amdgcn_ballot_w64(m == POISON) == 0ull; }
DEV bool all64(bool c) { return __builtin_amdgcn_ballot_w64(!c) == 0ull; }
DEV unsigned phi2(f2 a) {
  const f2 e = (f2){__builtin_amdgcn_fmed3f(__builtin_amdgcn_exp2f(a[0]), 0.f, 1.f), __builtin_amdgcn_fmed3f(__builtin_amdgcn_exp2f(a[1]), 0.f, 1.f)};
  const f2 r = __builtin_elementwise_fma((f2){fmaxf(a[0], 1.44269504e-6f), fmaxf(a[1], 1.44269504e-6f)}, (f2)(0.693147181f), e);
  return pk2(r[0], r[1]);
}
#define RD128(dst, addr, OFF) asm volatile("ds_read_b128 %0, %1 offset:%2" : "=v"(dst) : "v"(addr), "i"(OFF) : "memory")
#define RD32(dst, addr, OFF) asm volatile("ds_read_b32 %0, %1 offset:%2" : "=v"(dst) : "v"(addr), "i"(OFF) : "memory")
#define RDTR(dst, addr, OFF) asm volatile("ds_read_b64_tr_b16 %0, %1 offset:%2" : "=v"(dst) : "v"(addr), "i"(OFF) : "memory")
#define SBAR() __builtin_amdgcn_sched_barrier(0)
#define CAT(x, y) __builtin_shufflevector(x, y, 0, 1, 2, 3, 4, 5, 6, 7)
#define WAITV(n) asm volatile("s_waitcnt vmcnt(%0)" ::"i"(n) : "memory")
#define WAITL(n) asm volatile("s_waitcnt lgkmcnt(%0)" ::"i"(n) : "memory")
#define CLOB() asm volatile("" ::: "memory")
#define BAR() asm volatile("s_barrier" ::: "memory")
#define LBAR() asm volatile("s_waitcnt lgkmcnt(0)\n s_barrier" ::: "memory")
#define PIN(v) asm volatile("" : "+v"(v))
#define PACE(n) do { SBAR(); __builtin_amdgcn_s_sleep(n); SBAR(); } while (0)
#define RFL(v) ((unsigned)__builtin_amdgcn_readfirstlane(v))
DEV const char* uptr(const char* p) {
  u64 v = (u64)p;
  return (const char*)(((u64)RFL((unsigned)(v >> 32)) << 32) | RFL((unsigned)v));
}
DEV u4 mkrs(const char* p, unsigned bytes) {
  const u64 v = (u64)uptr(p);
  u4 r; r[0] = (unsigned)v; r[1] = (unsigned)(v >> 32) & 0xFFFFu; r[2] = RFL(bytes); r[3] = 0x00020000u;
  return r;
}
#define BLDS(voff, rs, so, OFF) asm volatile("buffer_load_dwordx4 %0, %1, %2 offen offset:%3 lds" ::"v"(voff), "s"(rs), "s"(so), "i"(OFF) : "memory")
#define MFMA16(a, b, c) __builtin_amdgcn_mfma_f32_16x16x32_bf16(a, b, c, 0, 0, 0)
#define DOT2(a, b, c) __builtin_amdgcn_fdot2_f32_bf16(__builtin_bit_cast(bf2, (unsigned)(a)), __builtin_bit_cast(bf2, (unsigned)(b)), c, false)
#define BC8(v) __builtin_bit_cast(s8, v)
#define LD128(off) __builtin_amdgcn_raw_buffer_load_b128(wrs, (off), 0, 16)
#define LD64(off) __builtin_amdgcn_raw_buffer_load_b64(wrs, (off), 0, 16)
#define ST128(v, off) __builtin_amdgcn_raw_buffer_store_b128((v), wrs, (off), 0, 16)
#define ST64(v, off) __builtin_amdgcn_raw_buffer_store_b64((v), wrs, (off), 0, 16)
#define ALD128(dst, off) asm volatile("buffer_load_dwordx4 %0, %1, %2, 0 offen sc1" : "=v"(dst) : "v"(off), "s"(rsW) : "memory")
#define ALD64(dst, off) asm volatile("buffer_load_dwordx2 %0, %1, %2, 0 offen sc1" : "=v"(dst) : "v"(off), "s"(rsW) : "memory")
constexpr unsigned SPIN_CAP = 1u << 20;

DEV void lds_arrive(LAS unsigned* c) {
  asm volatile("s_mov_b64 exec, 1\n ds_add_u32 %0, %1\n s_mov_b64 exec, -1" ::"v"((unsigned)(size_t)c), "v"(1u) : "memory");
}
template <int SL> DEV void lds_wait(LAS unsigned* c, unsigned target) {
#pragma nounroll
  while (RFL(__hip_atomic_load(c, __ATOMIC_RELAXED, __HIP_MEMORY_SCOPE_WORKGROUP)) < target) { if (SL) __builtin_amdgcn_s_sleep(SL); }
  CLOB();
}

#define STMP(idx)
#define STMC(idx)
#define STDUMP()
#define DPPF(v, ctrl, rm) I2F(__builtin_amdgcn_update_dpp(0, F2I(v), ctrl, rm, 0xf, false))
DEV unsigned wave_max(unsigned v) {
  v = max(v, (unsigned)__builtin_amdgcn_update_dpp(0, (int)v, 0x111, 0xf, 0xf, false));
  v = max(v, (unsigned)__builtin_amdgcn_update_dpp(0, (int)v, 0x112, 0xf, 0xf, false));
  v = max(v, (unsigned)__builtin_amdgcn_update_dpp(0, (int)v, 0x114, 0xf, 0xf, false));
  v = max(v, (unsigned)__builtin_amdgcn_update_dpp(0, (int)v, 0x118, 0xf, 0xf, false));
  v = max(v, (unsigned)__builtin_amdgcn_update_dpp(0, (int)v, 0x142, 0xa, 0xf, false));
  v = max(v, (unsigned)__builtin_amdgcn_update_dpp(0, (int)v, 0x143, 0xc, 0xf, false));
  return (unsigned)__builtin_amdgcn_readlane((int)v, 63);
}

constexpr int SLOT = 49152, L_B = 32768;
constexpr int L_EX = SLOT;
constexpr int L_ROWM = 159744;
constexpr int L_ROWS = L_ROWM + 1024;
constexpr int L_COL = L_ROWS + 1024;
constexpr int L_TAG = L_COL + 512;
constexpr int L_CNT = L_TAG + 64;
constexpr int L_XT = 0;
constexpr int L_KX = 16384;
constexpr int L_ZX = L_KX + 8192;
constexpr int L_YS = L_ZX + 256;
constexpr int L_WO = 28672;
constexpr int K_LDS = 163840;

#define TRFRAG(f, W, c) do { _Pragma("unroll") for (int q_ = 0; q_ < 4; ++q_) { unsigned e_ = W[2 * q_][(c) >> 1], o_ = W[2 * q_ + 1][(c) >> 1]; \
    f[q_] = ((c) & 1) ? __builtin_amdgcn_perm(o_, e_, 0x07060302u) : __builtin_amdgcn_perm(o_, e_, 0x05040100u); } } while (0)
constexpr u64 TAG_STEP = 0x9E3779B97F4A7C15ull;

struct FW { s8 a[8]; s4 b[8]; };
#define RA(G, s, u, i) RD128(G.a[i], aad[((s) % 3) == 2][u], (((s) % 3) & 1) * SLOT + (i) * 2048)
#define RB(G, s, u, n) RDTR(G.b[n], bad[((s) % 3) == 2][((n) >> 1) & 1], (((s) % 3) & 1) * SLOT + ((n) >> 2) * 8192 + (u) * 4096 + ((n) & 1) * 512)
#define FB(G, j) CAT(G.b[2 * (j)], G.b[2 * (j) + 1])
#define RW(G, s, u, n) do { if ((n) < 8) RB(G, s, u, (n) & 7); else RA(G, s, u, ((n) - 8) & 7); SBAR(); } while (0)
#define SNK(n) ((((n) >> 2) & 1) ? 3 - ((n) & 3) : (n) & 3)
#define MW(G, n) do { if (TRN) c[(n) >> 2][SNK(n)] = MFMA16(FB(G, SNK(n)), G.a[(n) >> 2], c[(n) >> 2][SNK(n)]); \
    else c[(n) >> 2][SNK(n)] = MFMA16(G.a[(n) >> 2], FB(G, SNK(n)), c[(n) >> 2][SNK(n)]); SBAR(); } while (0)
#define MFA(c_, a_, b_) asm volatile("v_mfma_f32_16x16x32_bf16 %0, %1, %2, %0" : "+v"(c_) : "v"(a_), "v"(b_))
#define MA(G, n) do { if (TRN) MFA(c[(n) >> 2][SNK(n)], FB(G, SNK(n)), G.a[(n) >> 2]); else MFA(c[(n) >> 2][SNK(n)], G.a[(n) >> 2], FB(G, SNK(n))); SBAR(); } while (0)
#define MW2(Fq, G, sn, un, n) do { MA(Fq, 2 * (n)); if ((n) < 12) RW(G, sn, un, (n) + ((n) + 2) / 3); MA(Fq, 2 * (n) + 1); if ((n) < 12 && (n) % 3 == 0) RW(G, sn, un, (n) + (n) / 3 + 1); } while (0)
#define HALFW(Fq, G, sn, un) do { MW2(Fq, G, sn, un, 0); MW2(Fq, G, sn, un, 1); MW2(Fq, G, sn, un, 2); MW2(Fq, G, sn, un, 3); MW2(Fq, G, sn, un, 4); MW2(Fq, G, sn, un, 5); MW2(Fq, G, sn, un, 6); MW2(Fq, G, sn, un, 7); \
    MW2(Fq, G, sn, un, 8); MW2(Fq, G, sn, un, 9); MW2(Fq, G, sn, un, 10); MW2(Fq, G, sn, un, 11); MW2(Fq, G, sn, un, 12); MW2(Fq, G, sn, un, 13); MW2(Fq, G, sn, un, 14); MW2(Fq, G, sn, un, 15); WAITL(0); SBAR(); } while (0)
#define STAGEW(s) do { HALFW(f0, f1, s, 1); BAR(); SBAR(); HALFW(f1, f0, (s) + 1, 0); } while (0)
#define KLOOP() \
      unsigned aad[2][2], bad[2][2]; \
      { LANE(); const int q = np >> 2, p = l & 3, f = ((q >> 1) & 1) | ((g & 1) << 1); \
        _Pragma("unroll") for (int u = 0; u < 2; ++u) { aad[0][u] = ldsb + (wm * 128 + np) * 128 + (((4 * u + g) ^ (np >> 1)) * 16); aad[1][u] = aad[0][u] + 2 * SLOT; \
          bad[0][u] = ldsb + L_B + (8 * g + q) * 128 + (((2 * wn + (TRN ? u : u ^ wm)) ^ f) * 32) + p * 8; bad[1][u] = bad[0][u] + 2 * SLOT; \
          PIN(aad[0][u]); PIN(aad[1][u]); PIN(bad[0][u]); PIN(bad[1][u]); } } \
      unsigned tR, tC; \
      { LANE(); tR = ldsb + L_ROWM + (wm * 128 + (TRN ? np : 4 * g)) * 4; tC = ldsb + L_COL + (wn * 32 + (TRN ? 4 * g : wm * 16 + np)) * 4; PIN(tR); PIN(tC); SBAR(); } \
      f4 c[8][4]; \
      _Pragma("unroll") for (int i = 0; i < 8; ++i) _Pragma("unroll") for (int jj = 0; jj < 4; ++jj) { c[i][jj] = (f4)(0.f); PIN(c[i][jj]); } \
      FW f0, f1; \
      BAR(); \
      STMP(1); STMC(40); \
      RW(f0, 0, 0, 0); RW(f0, 0, 0, 1); RW(f0, 0, 0, 8); RW(f0, 0, 0, 2); RW(f0, 0, 0, 3); RW(f0, 0, 0, 4); RW(f0, 0, 0, 5); RW(f0, 0, 0, 6); RW(f0, 0, 0, 7); \
      RW(f0, 0, 0, 9); RW(f0, 0, 0, 10); RW(f0, 0, 0, 11); RW(f0, 0, 0, 12); RW(f0, 0, 0, 13); RW(f0, 0, 0, 14); RW(f0, 0, 0, 15); \
      WAITL(0); SBAR(); \
      STAGEW(0); STAGEW(1); STAGEW(2); STAGEW(3); STAGEW(4); STAGEW(5); STAGEW(6); STAGEW(7); \
      STAGEW(8); STAGEW(9); STAGEW(10); STAGEW(11); STAGEW(12); STAGEW(13); STAGEW(14); \
      HALFW(f0, f1, 15, 1); if (TRN) BAR(); SBAR()
#define MT(n) do { MW(f1, n); TB(n); } while (0)
#define KLAST() do { MT(0); MT(1); MT(2); MT(3); MT(4); MT(5); MT(6); MT(7); MT(8); MT(9); MT(10); MT(11); MT(12); MT(13); MT(14); MT(15); \
      MT(16); MT(17); MT(18); MT(19); MT(20); MT(21); MT(22); MT(23); MT(24); MT(25); MT(26); MT(27); MT(28); MT(29); MT(30); MT(31); WAITL(0); } while (0)
#define H2(v, e0) ((f2){(v)[e0], (v)[(e0) + 1]})

#define WQ0 56
#define QROT2 1
#define MORD 3
#define FI 11
#define FORCE_SLOW 0
#define GP1 3
#define G0A 4
#define ES 2
#define GT_ 1
#define NBR 4
#define CBX 1
#define YO 64
#define YSL 16
#define KD 90
#define KSL 2
#define OSL 1
#define TKP 13
#define CL0 6

__global__ __launch_bounds__(512, 2) void fused(const u16*, const u16*, const u16*, const u16*, const u16*, const u16*, const u16*, char*, u16*) {
  const __attribute__((address_space(4))) u64* ka = (const __attribute__((address_space(4))) u64*)__builtin_amdgcn_kernarg_segment_ptr();
  u4 a01;
  int gb = blockIdx.x, w0 = __builtin_amdgcn_readfirstlane(threadIdx.x);
  asm volatile("s_load_dwordx4 %0, %3, 0" : "=&s"(a01), "+s"(gb), "+s"(w0) : "s"(ka));
  extern __shared__ __attribute__((aligned(16))) char lds[];
  const int w = (w0 >> 6) & 7;
#define LANE() int l; asm volatile("v_mbcnt_lo_u32_b32 %0, -1, 0\n v_mbcnt_hi_u32_b32 %0, -1, %0" : "=v"(l)); \
  const int np = l & 15, g = l >> 4, rr = l >> 3, sl = l & 7; (void)np; (void)g; (void)rr; (void)sl
  STMP(0);
  const int xcd = gb & 7, j = gb >> 3, xq = xcd >> 1;
  const unsigned ldsb = (unsigned)(size_t)(LAS char*)lds;
  const bool isQ = j >= 16;
  const int rg = (xcd & 1) * 4 + (j & 3), hp = xq * 4 + (j >> 2), qg = xq * 2 + ((j >> 2) & 1);
  const int hd0 = isQ ? qg * 4 : 32 + hp * 2, hd1 = isQ ? 2 : 32;
  const bool isB = w == 6;
  unsigned mb_, step, d1, d2, d3, so, lim; u4 rs;
#define VSET() unsigned v[4]; { const unsigned pitch = isB ? 6144 : 2048, sm = isB ? 2 : 7; \
    _Pragma("unroll") for (int i = 0; i < 4; ++i) v[i] = (8 * i + rr) * pitch + ((sl ^ ((isB ? rr : rr >> 1) & sm) ^ ((i & 1) << 2)) * 16) - i * 1024; }
#define QL 4
#define QROT 1
#define G4(s, q, dq) do { unsigned t_; asm volatile("s_add_u32 m0, %1, %2\n s_add_u32 %0, %3, %4" : "=s"(t_) : "s"(mb_), "i"(((s) % 3) * SLOT + (q) * 4096), "s"(so), "s"(dq) : "scc", "memory"); \
        BLDS(v[0], rs, t_, 0); BLDS(v[1], rs, t_, 1024); BLDS(v[2], rs, t_, 2048); BLDS(v[3], rs, t_, 3072); } while (0)
#define PA(s) do { G4(s, 0, 0u); G4(s, 1, d1); } while (0)
#define PB(s) do { G4(s, 2, d2); G4(s, 3, d3); asm volatile("s_add_u32 %0, %0, %1\n s_cmp_eq_u32 %0, %2\n s_cselect_b32 %0, 0, %0" : "+s"(so) : "s"(step), "s"(lim) : "scc"); } while (0)
  {
    LANE();
    mb_ = RFL(ldsb + (w - 4) * 16384); step = isB ? 64 * 6144 : 128;
    d1 = isB ? 32 * 6144 : 65536; d2 = isB ? hd1 * 64 : 131072; d3 = d1 + d2;
    so = isQ ? QROT * step + ((qg & 1) ? QROT2 * step : 0) : 0; lim = QL * step;
    unsigned off = isB ? hd0 * 64 : (rg * 256 + (w - 4) * 128) * 2048;
    unsigned r2 = isB ? 1024 * 6144 - off : 128 * 2048, ldr = (j < 24 ? w : 0) - 4, b0, b1;
    VSET();
    PIN(v[0]); PIN(v[1]); PIN(v[2]); PIN(v[3]);
    asm volatile("" : "+s"(off), "+s"(mb_), "+s"(so), "+s"(d1), "+s"(d2), "+s"(d3), "+s"(step), "+s"(lim), "+s"(r2), "+s"(ldr));
    SBAR();
    asm volatile("s_waitcnt lgkmcnt(0)" : "+s"(a01));
    asm volatile("s_cmp_eq_u32 %6, 2\n s_cselect_b32 %0, %4, %2\n s_cselect_b32 %1, %5, %3\n s_add_u32 %0, %0, %7\n s_addc_u32 %1, %1, 0\n s_and_b32 %1, %1, 0xffff"
                 : "=&s"(b0), "=&s"(b1) : "s"(a01[0]), "s"(a01[1]), "s"(a01[2]), "s"(a01[3]), "s"(ldr), "s"(off) : "scc");
    rs = (u4){b0, b1, r2, 0x00020000u};
    STMP(37);
    if (__builtin_expect(ldr < 3u, 1)) { PA(0); PB(0); PA(1); if (ES) PB(1); }
    STMP(38);
  }
  asm volatile("" : "+s"(ka));
  const u16* x = (const u16*)(((u64)a01[1] << 32) | a01[0]); const u16* wqkv = (const u16*)(((u64)a01[3] << 32) | a01[2]);
  const u16* wo = (const u16*)ka[2]; char* ws = (char*)ka[7];
  u4 rsC;
  { u64 pc; unsigned len;
    asm volatile("s_getpc_b64 %0\n.Lcp0:\n s_mov_b32 %1, .Lcp1-.Lcp0\n s_max_i32 %1, %1, .Lcp2-.Lcp0\n s_max_i32 %1, %1, .Lcp3-.Lcp0\n s_max_i32 %1, %1, .Lcp4-.Lcp0\n s_max_i32 %1, %1, .Lcp5-.Lcp0\n s_max_i32 %1, %1, .Lcp6-.Lcp0\n s_max_i32 %1, %1, 0" : "=&s"(pc), "=&s"(len) :: "scc");
    rsC = (u4){(unsigned)pc & ~127u, (unsigned)(pc >> 32) & 0xFFFFu, len, 0x00020000u}; }
#define CODE(n) asm volatile("s_mov_b32 m0, %2\n s_nop 0\n buffer_load_dwordx4 %0, %1, 0 offen lds" :: "v"((n) * 1024 + l * 16), "s"(rsC), "s"(RFL(ldsb + L_ROWM + 3072)) : "memory")
  if (j < 24 && w < 4) { LANE(); CODE(j * 4 + w); }
#define CPL(n) do { __builtin_amdgcn_s_waitcnt(0x0F70); asm volatile(".Lcp" #n ":"); } while (0)
#define CPE(n) asm volatile(".Lcp" #n ":")
  const __amdgpu_buffer_rsrc_t wrs = __builtin_amdgcn_make_buffer_rsrc(ws, 0, WS_END, 0x00020000);
  const int mb2 = (xcd & 1) * 8 + (xcd >> 2) * 4 + (j >> 3), nb = ((xcd >> 1) & 1) * 8 + ((j + NBR) & 7), lq = (mb2 + (mb2 >> 2)) & 3;
#define msg (WS_MSG + xcd * 256 + ((l & 8) ? 0 : 64 + sl * 8))
#define MSGOK(f) all64((((l & 8) ? ((f)[0] ^ tlo) | ((f)[1] ^ thi) : (f)[1] ^ tlo)) == 0u)
#define KEYOK(f) all64((l & 8) || (f)[1] == tlo)
#define GBAD(v) (((v)[1] ^ tlo) | ((v)[3] ^ thi))
#define WO_SETUP() const u4 rsO = mkrs((const char*)wo + nb * 128, 1024 * 2048 - nb * 128); const unsigned vW = rr * 2048 + ((sl ^ (((rr >> 1) & 3) << 1)) * 16)
#define DMA1(v, rs, m, so) asm volatile("s_mov_b32 m0, %2\n s_nop 0\n buffer_load_dwordx4 %0, %1, %3 offen lds" ::"v"(v), "s"(rs), "s"(RFL(m)), "s"(RFL(so)) : "memory")
#define WOL(n0, n1, st) _Pragma("nounroll") for (int n_ = (n0); n_ < (n1); n_ += (st)) DMA1(vW, rsO, ldsb + L_WO + n_ * 1024, n_ * 16384)
#define WOR(n0, n1, st) _Pragma("nounroll") for (int n_ = (n0); n_ < (n1); n_ += (st)) DMA1(vW, rsO, ldsb + L_WO + (n_ ^ (lq * 32)) * 1024, (n_ ^ (lq * 32)) * 16384)
  const int ow = isQ && j < 24 ? w ^ 4 : w;
  f4 oc[2][4];
  u4 st2[2];
  unsigned tpoll = 0;
#define TPOLL(d) tpoll = (unsigned)__builtin_amdgcn_s_memrealtime() + (d)
#define PDK 280
#define PDQ 80
#define XT(r, ct) (xt + (r) * 2048 + (((2 * (ct) + (g >> 1)) ^ sl) * 16))
#define S2OK() all64(((st2[0][1] ^ t_[0]) | (st2[0][3] ^ t_[1]) | (st2[1][1] ^ t_[0]) | (st2[1][3] ^ t_[1])) == 0u)
#define OINIT() do { PIN(st2[0]); PIN(st2[1]); const char* xt = lds + L_XT + (ow * 32 + np) * 128 + (g & 1) * 8; \
    { const u2 t_ = *(const u2*)(lds + L_TAG); if (__builtin_expect(!S2OK(), 0)) { _Pragma("nounroll") for (unsigned it = 0; it < SPIN_CAP; ++it) { CLOB(); ST2(); if (S2OK()) break; } } } \
    _Pragma("unroll") for (int r = 0; r < 2; ++r) { \
      const float c1 = 1.f + U2F(st2[r][2]), c0 = -hi(st2[r][0]) * U2F(st2[r][2]); \
      _Pragma("unroll") for (int ct = 0; ct < 4; ++ct) { const u2 v = *(const u2*)XT(r, ct); \
        oc[r][ct] = (f4){__builtin_fmaf(lo(v[0]), c1, c0), __builtin_fmaf(hi(v[0]), c1, c0), __builtin_fmaf(lo(v[1]), c1, c0), __builtin_fmaf(hi(v[1]), c1, c0)}; PIN(oc[r][ct]); } \
      } } while (0)
#define XOWN() do { const u4 rsX = mkrs((const char*)x + (size_t)mb2 * 128 * 2048 + nb * 128, 128 * 2048 - nb * 128); const unsigned vX = rr * 2048 + ((sl ^ rr) * 16); \
    if (ow < 4) _Pragma("unroll") for (int n_ = 0; n_ < 4; ++n_) DMA1(vX, rsX, ldsb + L_XT + (ow * 4 + n_) * 1024, (ow * 4 + n_) * 16384); \
    const u4 rsW = mkrs(ws, WS_END); const unsigned sto = WS_MS + (mb2 * 128 + (w & 3) * 32 + np) * 16, sto1 = sto + 256; ALD128(st2[0], sto); ALD128(st2[1], sto1); } while (0)
#define ST2() do { const unsigned sto = WS_MS + (mb2 * 128 + (w & 3) * 32 + np) * 16; st2[0] = LD128(sto); st2[1] = LD128(sto + 256); } while (0)

  if (j < 24) {
    if (w >= 4) {
      LANE();
      unsigned tlo = 0, thi = 0;
      WO_SETUP();
      u2 f = (u2){0u, 0u};
      if (w == 7) {
        const u4 rsW = mkrs(ws, WS_END);
        int nbar = 16;
#define TICK() do { if (nbar) { BAR(); --nbar; } } while (0)
        if (l < 16) ((unsigned*)(lds + L_CNT))[l] = 0u;
        WAITL(0);
        TICK();
        u2 ep; unsigned z0 = 0; PIN(z0);
        ALD64(ep, z0);
        { const u4 pz = (u4){POISON, POISON, POISON, POISON};
          if (isQ) {
            ST128(pz, WS_OFL + ((rg * 2 + (l >> 5)) * 32 + qg * 4) * 128 + (l & 31) * 16);
          } else {
            const unsigned kb = WS_KVP + (hp * 16 + rg) * KVS + l * 16;
            ST128(pz, kb); ST128(pz, kb + 1024); ST128(pz, kb + 8 * KVS); ST128(pz, kb + 8 * KVS + 1024);
            ST128(pz, l < 16 ? WS_KVP + (hp * 16 + rg + (l & 8)) * KVS + 2048 + sl * 16 : WS_OFF);
          } }
        SBAR();
        TICK(); TICK(); TICK();
        WAITV(0); PIN(ep);
        const u64 tag = (((u64)RFL(ep[1]) << 32) | RFL(ep[0])) + TAG_STEP;
        tlo = (unsigned)tag; thi = (unsigned)(tag >> 32);
        ST64(((u2){tlo, thi}), l == 0 ? WS_FLAG + gb * 8 : WS_OFF);
        STMP(8);
#pragma nounroll
        while (nbar > 16 - TKP) { TICK();
#define CPF2 10
          if (CPF2 && nbar == 16 - CPF2) { CODE(j * 4); CODE(j * 4 + 1); CODE(j * 4 + 2); CODE(j * 4 + 3); }
        }
        u4 ms[4], cs[8];
        const unsigned a0 = WS_MS + (rg * 256 + l) * 16, a2 = WS_CS + ((g * 96 + hd0) * 16 + np) * 16, a3 = a2 + hd1 * 256, a4 = a2 + 4 * 96 * 256, a5 = a3 + 4 * 96 * 256, a6 = msg;
#pragma nounroll
        for (unsigned it = 0; it < SPIN_CAP; ++it) {
#pragma unroll
          for (int i = 0; i < 4; ++i) asm volatile("buffer_load_dwordx4 %0, %1, %2, 0 offen offset:%3 sc1" : "=v"(ms[i]) : "v"(a0), "s"(rsW), "i"(i * 1024) : "memory");
#pragma unroll
          for (int i = 0; i < 8; ++i) asm volatile("buffer_load_dwordx4 %0, %1, %2, 0 offen offset:%3 sc1" : "=v"(cs[i]) : "v"(i < 2 ? a2 : i < 4 ? a3 : i < 6 ? a4 : a5), "s"(rsW), "i"((i & 1) * 256) : "memory");
          ALD64(f, a6);
          if (nbar > 1) TICK();
          if (nbar > 1 && it == 0) TICK();
          WAITV(0);
          unsigned bad = 0;
#pragma unroll
          for (int i = 0; i < 4; ++i) { PIN(ms[i]); PIN(cs[i]); PIN(cs[4 + i]); bad |= GBAD(ms[i]) | GBAD(cs[i]) | GBAD(cs[4 + i]); }
          PIN(f);
          if (all64(bad == 0u)) break;
        }
        STMP(9);
        float ct[2];
#pragma unroll
        for (int e = 0; e < 2; ++e) {
          float t[4];
#pragma unroll
          for (int i = 0; i < 4; ++i) t[i] = add1(U2F(cs[i][2 * e]), U2F(cs[4 + i][2 * e]));
#pragma unroll
          for (int i = 0; i < 2; ++i) { auto r = __builtin_amdgcn_permlane32_swap(F2U(t[i]), F2U(t[i + 2]), false, false); t[i] = add1(U2F(r[0]), U2F(r[1])); }
          auto r = __builtin_amdgcn_permlane16_swap(F2U(t[0]), F2U(t[1]), false, false);
          ct[e] = add1(U2F(r[0]), U2F(r[1])); PIN(ct[e]);
        }
#pragma unroll
        for (int i = 0; i < 4; ++i) { ms[i][0] = (ms[i][0] & 0xFFFF0000u) ^ 0x80000000u; ms[i][1] = F2U(mul1(U2F(ms[i][2]), 1.44269504f)); PIN(ms[i]); }
#pragma nounroll
        while (nbar) TICK();
#pragma unroll
        for (int i = 0; i < 4; ++i) { ((unsigned*)(lds + L_ROWM))[i * 64 + l] = ms[i][0]; ((unsigned*)(lds + L_ROWS))[i * 64 + l] = ms[i][1]; }
        ((float*)(lds + L_COL))[g * 32 + np] = ct[0]; ((float*)(lds + L_COL))[g * 32 + 16 + np] = ct[1];
        if (l == 8) *(u2*)(lds + L_TAG) = (u2){tlo, thi};
        if (!isQ) lds_arrive((LAS unsigned*)(lds + L_CNT) + 5);
      } else {
        VSET();
        if (ES == 0) { WAITV(8); BAR(); PB(1); PA(2); WAITV(8); BAR(); STMP(44); PB(2); }
        else if (ES == 1) { WAITV(16); BAR(); PA(2); PB(2); WAITV(16); BAR(); STMP(44); }
        else { WAITV(16); BAR(); PA(2); WAITV(8); BAR(); STMP(44); PB(2); }
        PA(0); PB(0);
#define LS(s) do { if ((s) < 14) WAITV(16); else WAITV(0); BAR(); STMP(44 + (s)); if ((s) + 3 < 16) { if ((s) + 3 == QL) so = lim; PA(s); PB(s); } } while (0)
        LS(1); LS(2); LS(3); LS(4); LS(5); LS(6); LS(7); LS(8); LS(9); LS(10); LS(11); LS(12); LS(13); LS(14);
      }
#define KEYS() do { if (w == 7) { _Pragma("nounroll") for (unsigned it = 0; it < SPIN_CAP && !MSGOK(f); ++it) { f = LD64(msg); CLOB(); } \
        if (l < 8) ((unsigned*)(lds + L_TAG + 16))[l] = f[0]; \
        lds_arrive((LAS unsigned*)(lds + L_CNT) + 3); } } while (0)
      if (isQ) {
        if (w < 7) { WOL(20 + w - 4, WQ0, 3); }
        LBAR(); STMP(59);
        XOWN(); SBAR(); WOL(WQ0 + w - 4, 128, 4); WOL(w - 4, 20, 4);
        const unsigned tq = (unsigned)__builtin_amdgcn_s_memrealtime();
        WAITV(5); OINIT();
        KEYS();
        STMP(21);
        const unsigned sb = WS_KVP + (qg * 4 + (w & 1)) * 8 * KVS + ((w >> 1) & 1) * 1024 + l * 16;
        const unsigned sz = (w & 2) ? WS_OFF : WS_KVP + ((qg * 4 + (w & 1)) * 8 + (np >> 1)) * KVS + 2048 + (np & 1) * 64 + g * 16;
        u4 prm[2];
#pragma unroll
        for (int e = 0; e < 4; ++e) {
          const unsigned o = (np >> 2) == g ? ((np & 1) ? 0x3F800000u : 0x00003F80u) : 0u;
          prm[0][e] = e == (w & 2) + ((np >> 1) & 1) ? o : 0u; prm[1][e] = e == 2 - (w & 2) + ((np >> 1) & 1) ? o : 0u;
        }
        PIN(prm[0]); PIN(prm[1]);
        __builtin_amdgcn_s_setprio(2);
#pragma nounroll
        while ((unsigned)__builtin_amdgcn_s_memrealtime() - tq < KD) __builtin_amdgcn_s_sleep(1);
        lds_wait<1>((LAS unsigned*)(lds + L_CNT) + 3, 1);
        u4 v[16], zv[2]; f4 kg[4][2];
#define KVLD(gq) do { _Pragma("unroll") for (int p = 4 * (gq); p < 4 * (gq) + 4; ++p) v[p] = __builtin_amdgcn_raw_buffer_load_b128(wrs, sb, ((p & 7) + (p >> 3) * 16) * KVS, 16); } while (0)
#define ZLD(hh) zv[hh] = __builtin_amdgcn_raw_buffer_load_b128(wrs, sz, (hh) * 16 * KVS, 16)
        ZLD(0); KVLD(0); KVLD(1); SBAR(); if (GP1) PACE(GP1); ZLD(1); KVLD(2); KVLD(3); SBAR();
#pragma unroll
        for (int hh = 0; hh < 2; ++hh) {
          unsigned pend = 7;
#pragma nounroll
          for (unsigned it = 0; it < SPIN_CAP; ++it) {
#pragma unroll
            for (int gq = 2 * hh; gq < 2 * hh + 2; ++gq)
              if (pend & (1u << (gq & 1))) {
                f4 t0_ = (f4)(0.f), t1_ = (f4)(0.f); unsigned mx = 0;
#pragma unroll
                for (int p = 4 * gq; p < 4 * gq + 4; ++p) { mx = mx4(mx, v[p]); t0_ = MFMA16(BC8(prm[0]), BC8(v[p]), t0_); t1_ = MFMA16(BC8(prm[1]), BC8(v[p]), t1_); }
                kg[gq][0] = t0_; kg[gq][1] = t1_;
                if (clean(mx)) pend &= ~(1u << (gq & 1));
              }
            if ((pend & 4u) && clean(mx4(0u, zv[hh]))) pend &= ~4u;
            if (!pend) break;
            if (KSL) __builtin_amdgcn_s_sleep(KSL);
            CLOB();
            if (pend & 4u) ZLD(hh);
            if (pend & 1u) KVLD(2 * hh);
            if (pend & 2u) KVLD(2 * hh + 1);
            if (hh == 0) { ZLD(1); KVLD(2); KVLD(3); }
            SBAR();
          }
          if (hh) STMP(25); else STMP(4);
          const f4 k0 = kg[2 * hh][0] + kg[2 * hh + 1][0], k1 = kg[2 * hh][1] + kg[2 * hh + 1][1];
          *(u4*)(lds + L_KX + ((2 * hh + (w & 1)) * 2 + ((w >> 1) & 1)) * 1024 + l * 16) = (u4){pk2(k0[0], k0[1]), pk2(k0[2], k0[3]), pk2(k1[0], k1[1]), pk2(k1[2], k1[3])};
          if (!(w & 2)) {
            f4 z = __builtin_bit_cast(f4, zv[hh]);
#pragma unroll
            for (int e = 0; e < 4; ++e) { z[e] += DPPF(z[e], 0x122, 0xf); z[e] += DPPF(z[e], 0x124, 0xf); z[e] += DPPF(z[e], 0x128, 0xf); }
            if (np < 2) *(u2*)(lds + L_ZX + (2 * hh + (w & 1)) * 64 + g * 16 + np * 8) = (u2){pk2(z[0], z[1]), pk2(z[2], z[3])};
          }
          LBAR();
        }
        __builtin_amdgcn_s_setprio(0);
        CPL(3);
        TPOLL(PDQ);
      } else {
        if (w < 7) { WOL(32 + w, 128, 3); WAITV(0); STMP(35); lds_arrive((LAS unsigned*)(lds + L_CNT) + 2); }
        KEYS();
        __builtin_amdgcn_s_waitcnt(0x0F70);
      }
    } else {
      const int wm = w >> 1, wn = w & 1;
      if (!isQ) {
        constexpr bool TRN = false;
        unsigned cA = ldsb + L_CNT; PIN(cA);
        unsigned exw, exa, pka, pza, cB = cA + wn * 4, one1 = 1u; bool zsel;
        { LANE(); const unsigned kb = WS_KVP + ((hp * 2 + wn) * 8 + rg) * KVS;
          exw = ldsb + L_EX + l * 16 + w * 4096; exa = exw ^ 8192; pka = kb + wm * 1024 + l * 16; pza = wm == 0 && np < 2 ? kb + 2048 + np * 64 + g * 16 : WS_OFF; zsel = (np ^ wm) & 1;
          PIN(exw); PIN(exa); PIN(pka); PIN(pza); PIN(cB); PIN(one1); SBAR(); }
        KLOOP();
        float cc[4]; f4 rsk[8], mk[8]; unsigned tf;
        unsigned tC1 = tC ^ 64; PIN(tC1);
#define TB1(n) do { if ((n) < 4) RD32(cc[(n) & 3], ((n) & 1) ? tC1 : tC, ((n) >> 1) * 256); else if ((n) & 1) RD128(rsk[(((n) - 4) >> 1) & 3], tR, 1024 + (((n) - 4) >> 1) * 64); else RD128(mk[(((n) - 4) >> 1) & 3], tR, (((n) - 4) >> 1) * 64); } while (0)
#define TB(n) do { if ((n) == 8) RD32(tf, cA, 20); else if ((n) > 8 && (n) < 21) TB1(((n) - 9) % 12); SBAR(); } while (0)
        KLAST();
#undef TB
        PIN(tf);
        if (__builtin_expect(RFL(tf) == 0u, 0)) {
          lds_wait<0>((LAS unsigned*)(lds + L_CNT) + 5, 1);
          TB1(0); TB1(1); TB1(2); TB1(3); TB1(4); TB1(5); TB1(6); TB1(7); TB1(8); TB1(9); TB1(10); TB1(11); WAITL(0);
        }
#pragma unroll
        for (int i = 0; i < 4; ++i) { PIN(mk[i]); PIN(rsk[i]); PIN(cc[i]); }
#pragma unroll
        for (int i = 4; i < 8; ++i) { RD128(mk[i], tR, i * 64); RD128(rsk[i], tR, 1024 + i * 64); }
        SBAR();
        STMP(2); STMC(41);
        f4 kv[2][2], zz[2];
        kv[0][0] = kv[0][1] = kv[1][0] = kv[1][1] = zz[0] = zz[1] = (f4)(0.f);
        const u4 ones = (u4){0x3F803F80u, 0x3F803F80u, 0x3F803F80u, 0x3F803F80u};
        u4 kf[2], vf[2];
#define LN2(i, jj, e0) (__builtin_elementwise_fma(H2(mk[i], e0), (f2)(cc[jj]), H2(c[i][jj], e0)) * H2(rsk[i], e0))
#define KVM(n) do { if ((n) < 4) kv[(n) >> 1][(n) & 1] = MFMA16(BC8(kf[(n) >> 1]), BC8(vf[(n) & 1]), kv[(n) >> 1][(n) & 1]); else zz[(n) & 1] = MFMA16(BC8(kf[(n) & 1]), BC8(ones), zz[(n) & 1]); } while (0)
#pragma unroll
        for (int ip = 0; ip < 4; ++ip) {
          if (ip == 2) { WAITL(0);
#pragma unroll
            for (int i = 4; i < 8; ++i) { PIN(mk[i]); PIN(rsk[i]); } }
          f2 ak[8], av[8], ek[8];
#pragma unroll
          for (int n = 0; n < 8; ++n) { const int i = 2 * ip + ((n >> 1) & 1), e0 = (n & 1) * 2, t = n >> 2; ak[n] = LN2(i, t, e0); av[n] = LN2(i, 2 + t, e0); }
          SBAR();
#pragma unroll
          for (int n = 0; n < 8; ++n) {
            if (ip > 0 && n < 6) KVM(n);
            ek[n] = (f2){__builtin_amdgcn_fmed3f(__builtin_amdgcn_exp2f(ak[n][0]), 0.f, 1.f), __builtin_amdgcn_fmed3f(__builtin_amdgcn_exp2f(ak[n][1]), 0.f, 1.f)};
            ak[n] = (f2){fmaxf(ak[n][0], 1.44269504e-6f), fmaxf(ak[n][1], 1.44269504e-6f)};
            SBAR();
          }
#pragma unroll
          for (int n = 0; n < 8; ++n) { const f2 r = __builtin_elementwise_fma(ak[n], (f2)(0.693147181f), ek[n]); kf[n >> 2][n & 3] = pk2(r[0], r[1]); vf[n >> 2][n & 3] = pk2(av[n][0], av[n][1]); }
          SBAR();
        }
        KVM(1); KVM(3); KVM(4); KVM(5); KVM(0); KVM(2);
        STMP(19);
        const f4 zs = zsel ? zz[1] : zz[0];
        *(LAS f4*)(size_t)(exw + 1024) = kv[0][1]; *(LAS f4*)(size_t)exw = kv[1][1]; *(LAS f4*)(size_t)(exw + 2048) = zs;
        asm volatile("s_mov_b64 exec, 1\n ds_add_u32 %0, %1 offset:24\n s_mov_b64 exec, -1" ::"v"(cB), "v"(one1) : "memory");
        f4 e0, e1, e2;
#pragma nounroll
        do {
          RD32(tf, cB, 24); RD128(e0, exa, 0); RD128(e1, exa, 1024); RD128(e2, exa, 2048);
          WAITL(0); PIN(tf); PIN(e0); PIN(e1); PIN(e2);
        } while (RFL(tf) < 2u);
        e0 += kv[0][0]; e1 += kv[1][0];
        ST128(san4((u4){pk2(e0[0], e0[1]), pk2(e0[2], e0[3]), pk2(e1[0], e1[1]), pk2(e1[2], e1[3])}), pka);
        ST128(san4(__builtin_bit_cast(u4, (zs + e2) * 1.44269504f)), pza);
        SBAR();
        STMP(3);
        LANE();
        TPOLL(PDK);
        if (j == 1 && w == 0 && (xcd < 4 || !GT_)) { const u2 t_ = *(const u2*)(lds + L_TAG); ST128(((u4){t_[0], t_[1], tpoll, 0u}), l < (GT_ ? 16 : 8) ? WS_HU + (xcd + rr * 4) * 128 + sl * 16 : WS_OFF); }
        lds_arrive((LAS unsigned*)(lds + L_CNT) + 8);
        lds_wait<1>((LAS unsigned*)(lds + L_CNT) + 8, 4);
        XOWN(); SBAR();
        { WO_SETUP(); WOL(w, 36, 4); }
        WAITV(9); OINIT();
        if (GT_ && xcd >= 4) {
          u4 hu = LD128(WS_HU + xcd * 128); const u2 t_ = *(const u2*)(lds + L_TAG); PIN(hu);
          if (all64(hu[0] == t_[0] && hu[1] == t_[1]) && (int)(RFL(hu[2]) - tpoll) < 0) tpoll = RFL(hu[2]);
        }
        WAITV(0);
        lds_arrive((LAS unsigned*)(lds + L_CNT) + 2);
        lds_wait<1>((LAS unsigned*)(lds + L_CNT) + 3, 1);
        CPL(1);
        STMP(20);
      } else {
        constexpr bool TRN = true;
        KLOOP();
        f4 cc[4]; float rs[8], mn[8];
#define TB(n) do { if ((n) < 4) RD128(cc[(n) & 3], tC, ((n) >> 1) * 256 + ((n) & 1) * 64); else if ((n) < 20) { if ((n) & 1) RD32(rs[(((n) - 4) >> 1) & 7], tR, 1024 + (((n) - 4) >> 1) * 64); else RD32(mn[(((n) - 4) >> 1) & 7], tR, (((n) - 4) >> 1) * 64); } SBAR(); } while (0)
        KLAST();
#undef TB
#pragma unroll
        for (int i = 0; i < 8; ++i) { PIN(mn[i]); PIN(rs[i]); PIN(cc[i & 3]); }
        STMP(2); STMC(41);
        LANE();
        u4 pf[8][2];
#pragma unroll
        for (int i = 0; i < 8; ++i)
#pragma unroll
          for (int hh = 0; hh < 2; ++hh) {
#pragma unroll
            for (int e = 0; e < 4; ++e) { const int jj = 2 * hh + (e >> 1), e0 = (e & 1) * 2;
              pf[i][hh][e] = phi2(__builtin_elementwise_fma(H2(cc[jj], e0), (f2)(mn[i]), H2(c[i][jj], e0)) * rs[i]); }
            PIN(pf[i][hh]);
          }
        const u2 tg = *(const u2*)(lds + L_TAG);
        const unsigned ofl = l < 8 ? WS_OFL + ((rg * 2 + wm) * 32 + (qg * 2 + wn) * 2) * 128 + sl * 16 : WS_OFF;
        unsigned oa[8];
#pragma unroll
        for (int i = 0; i < 8; ++i) { oa[i] = WS_OF + ((rg * 16 + wm * 8 + i) * 32 + qg * 4 + wn) * 1024 + l * 16; PIN(oa[i]); }
        STMP(19);
        BAR();
        STMP(22);
        u4 kA[2][2], zA[2];
#define KRD(hh) do { zA[hh] = *(const u4*)(lds + L_ZX + (2 * (hh) + wn) * 64 + g * 16); \
          kA[hh][0] = *(const u4*)(lds + L_KX + (2 * (hh) + wn) * 2048 + l * 16); kA[hh][1] = *(const u4*)(lds + L_KX + (2 * (hh) + wn) * 2048 + 1024 + l * 16); SBAR(); } while (0)
        KRD(0);
        f4 dn[16], x0[16], x1[16];
#define OM(n) do { dn[n] = MFMA16(BC8(zA[(n) >> 3]), BC8(pf[(n) & 7][(n) >> 3]), (f4)(1.44269504e-6f)); x0[n] = MFMA16(BC8(kA[(n) >> 3][0]), BC8(pf[(n) & 7][(n) >> 3]), (f4)(0.f)); \
          x1[n] = MFMA16(BC8(kA[(n) >> 3][1]), BC8(pf[(n) & 7][(n) >> 3]), (f4)(0.f)); } while (0)
        OM(0); OM(1);
#pragma unroll
        for (int n = 0; n < 16; ++n) {
          SBAR();
          const float inv = __builtin_amdgcn_rcpf(dn[n][0]);
          asm volatile("" :: "v"(dn[n]));
          if (n + 2 < 16 && n != 6 && n != 7) OM(n + 2);
          float y[8];
#pragma unroll
          for (int e = 0; e < 4; ++e) { y[e] = x0[n][e] * inv; y[4 + e] = x1[n][e] * inv; PIN(y[e]); PIN(y[4 + e]); }
          ST128(((u4){pk2(y[0], y[1]), pk2(y[2], y[3]), pk2(y[4], y[5]), pk2(y[6], y[7])}), oa[n & 7] + (n >> 3) * 2048);
          if (n == FI) { SBAR(); WAITV(FI - 7); ST128(((u4){tg[0], tg[1], tg[0], tg[1]}), ofl); SBAR(); STMP(24); }
          if (n == 7) { SBAR(); BAR(); KRD(1); OM(8); OM(9); }
        }
        SBAR();
        STMP(23);
        WAITV(0);
        ST128(((u4){tg[0], tg[1], tg[0], tg[1]}), ofl + 128);
        SBAR();
        CPL(2);
        STMP(5);
      }
    }
  } else {
    LANE();
    const u16 *wr = (const u16*)ka[3], *wg = (const u16*)ka[4], *wu = (const u16*)ka[5], *wd = (const u16*)ka[6];
    const int jm = j - 24, jsl = 7 - xcd, c0 = 4 - (int)(((size_t)wg >> 7) & 7) + xcd;
    const bool master = jm == 7 && jsl == 7, help = jm == 7 && jsl != 7, half = jm == 7 || jm == jsl;
    const int jc = help ? jsl : jm, hc = jc * 8 + ((c0 + jc) & 7);
    const int row0 = (xcd & 1) * 1024 + (xq * 8 + jm) * 32 + w * 4;
    u4 cw[12], xr[8], wv[2];
    const u4 rsW = mkrs(ws, WS_END), rsR = mkrs((const char*)x, S * D * 2), rsQ = mkrs((const char*)wqkv, D * 6144);
    const int cb = CBX ? (xcd & 6) | ((xcd + jm + 1) & 1) : xcd;
    const unsigned co = ((jm * 32 + w * 4 + g) * 3072 + cb * 128 + np * 8) * 2;
#define CI(n) (((n) % 3) * 4 + (n) / 3)
#define CL(a, b) do { _Pragma("unroll") for (int n = a; n < b; ++n) \
      asm volatile("buffer_load_dwordx4 %0, %1, %2, %3 offen" : "=v"(cw[CI(n)]) : "v"(co), "s"(rsQ), "s"((unsigned)(((CI(n) & 3) * 768 + ((CI(n) >> 2) + 1) % 3) * 2048)) : "memory"); } while (0)
    if (w == 0 && l < 8) ((unsigned*)(lds + L_CNT))[l] = 0u;
    CL(0, MORD);
    LBAR();
    u2 ep;
    { const unsigned xo = row0 * 2048 + l * 16, l16 = l * 16; unsigned z0 = 0; PIN(z0);
#pragma unroll
      for (int i = 0; i < 8; ++i) { const int n = (i & 3) * 2 + (i >> 2);
        asm volatile("buffer_load_dwordx4 %0, %1, %2, %3 offen offset:%4" : "=v"(xr[n]) : "v"(xo), "s"(rsR), "s"((unsigned)((n >> 2) * 4096)), "i"((n & 3) * 1024) : "memory"); }
      asm volatile("global_load_dwordx4 %0, %2, %3\n global_load_dwordx4 %1, %2, %3 offset:1024" : "=&v"(wv[0]), "=&v"(wv[1]) : "v"(l16), "s"(uptr((const char*)wr)) : "memory");
      ALD64(ep, z0); }
    CL(MORD, CL0);
    WAITV(CL0 - MORD);
#pragma unroll
    for (int i = 0; i < 8; ++i) PIN(xr[i]);
    PIN(wv[0]); PIN(wv[1]); PIN(ep);
    const u64 tag = (((u64)RFL(ep[1]) << 32) | RFL(ep[0])) + TAG_STEP;
    const unsigned tlo = (unsigned)tag, thi = (unsigned)(tag >> 32);
    const u2 tgv = (u2){tlo, thi};
    {
      float a[13];
#pragma unroll
      for (int i = 0; i < 13; ++i) a[i] = 0.f;
      unsigned one2 = 0x3F803F80u; PIN(one2);
#pragma unroll
      for (int jj = 0; jj < 8; ++jj) {
        const unsigned wv_ = wv[jj >> 2][jj & 3];
#pragma unroll
        for (int i = 0; i < 4; ++i) {
          const unsigned xv = xr[2 * i + (jj >> 2)][jj & 3];
          a[3 * i] = DOT2(xv, one2, a[3 * i]); a[3 * i + 1] = DOT2(xv, xv, a[3 * i + 1]); a[3 * i + 2] = DOT2(xv, wv_, a[3 * i + 2]);
        }
        a[12] = DOT2(wv_, one2, a[12]);
      }
#define RSTEP(n, ctrl) _Pragma("unroll") for (int i = 0; i < n; ++i) a[i] += DPPF(a[i], ctrl, 0xf)
      RSTEP(13, 0xB1); RSTEP(13, 0x4E);
#pragma unroll
      for (int i = 0; i < 3; ++i) { const float u0 = (l & 1) ? a[3 + i] : a[i], u1 = (l & 1) ? a[9 + i] : a[6 + i]; a[i] = (l & 2) ? u1 : u0; }
      a[3] = a[12];
      RSTEP(4, 0x124); RSTEP(4, 0x128);
#pragma unroll
      for (int i = 0; i < 4; ++i) {
        auto s16 = __builtin_amdgcn_permlane16_swap(F2U(a[i]), F2U(a[i]), false, false);
        a[i] = U2F(s16[0]) + U2F(s16[1]);
        auto s32 = __builtin_amdgcn_permlane32_swap(F2U(a[i]), F2U(a[i]), false, false);
        a[i] = U2F(s32[0]) + U2F(s32[1]);
      }
      const float mf = a[0] * (1.f / D), m = rbf(mf);
      const float var = rbf(fmaxf(a[1] * (1.f / D) - mf * mf, 0.f));
      const float rs = __builtin_amdgcn_rcpf(rbf(__builtin_amdgcn_sqrtf(rbf(var + 1e-5f))));
      const float dsc = rs * (a[2] - m * a[3]);
      ST128(((u4){pk2(dsc, m), tlo, F2U(rs), thi}), l < 4 ? WS_MS + (row0 + l) * 16 : WS_OFF);
    }
    SBAR();
    STMP(18);
    CL(CL0, 12);
    u4 gw[4][8], gf[4][8], dw[2][8];
    const u16* wp = ((np >> 3) ? wu : wg) + (size_t)(128 * w + (help ? 64 : 0) + 8 * g) * F + 64 * hc + 8 * sl;
#define GL(b, on) GL2(b, on, 0, 8)
#define GL2(b, on, i0, i1) do { const u16* p_ = (on) ? wp + (size_t)(32 * (b)) * F : wr; const unsigned st_ = (on) ? F : 0; \
      _Pragma("unroll") for (int i_ = i0; i_ < i1; ++i_) asm volatile("global_load_dwordx4 %0, %1, off nt" : "=v"(gw[b][i_]) : "v"(p_ + (size_t)(i_ * st_)) : "memory"); } while (0)
#define GT(b, NV) do { WAITV(NV); _Pragma("unroll") for (int c_ = 0; c_ < 8; ++c_) PIN(gw[b][c_]); _Pragma("unroll") for (int c_ = 0; c_ < 8; ++c_) { TRFRAG(gf[b][c_], gw[b], c_); PIN(gf[b][c_]); } SBAR(); } while (0)
    GL2(0, !master, 0, G0A);
    WAITV(G0A);
#pragma unroll
    for (int i = 0; i < 12; ++i) PIN(cw[i]);
    char* scr = lds + L_WO + ((lq & 2) ? 0 : 65536);
    float* red = (float*)scr;
    {
      u4 sel8;
      const unsigned one = (np & 1) ? 0x3F800000u : 0x00003F80u;
#pragma unroll
      for (int e = 0; e < 4; ++e) sel8[e] = ((np >> 1) == e) ? one : 0u;
#pragma unroll
      for (int p = 0; p < 3; ++p) {
        f4 c = (f4)(0.f);
#pragma unroll
        for (int i = 0; i < 4; ++i) c = MFMA16(BC8(sel8), BC8(cw[p * 4 + i]), c);
        if (g < 2) *(f4*)(red + (w * 3 + p) * 128 + 8 * np + 4 * g) = c;
      }
    }
    lds_arrive((LAS unsigned*)(lds + L_CNT) + 2);
    if (w < 3) {
      lds_wait<1>((LAS unsigned*)(lds + L_CNT) + 2, 8);
      if (w == 0) ST64(tgv, l == 0 ? WS_FLAG + gb * 8 : WS_OFF);
      const float* rp = red + w * 128 + g * 32 + np;
      float c0 = rp[0], c1 = rp[16];
#pragma unroll
      for (int i = 1; i < 8; ++i) { c0 += rp[i * 384]; c1 += rp[i * 384 + 16]; }
      ST128(((u4){F2U(c0), tlo, F2U(c1), thi}), WS_CS + ((jm * 96 + (w + 1) % 3 * 32 + cb * 4) * 16 + l) * 16);
    }
    SBAR();
    STMP(17);
    if (master) {
      u4 rv[4];
      const unsigned ra = WS_MS + (w * 256 + l) * 16;
      unsigned bad = 15;
#pragma nounroll
      for (unsigned it = 0; it < SPIN_CAP; ++it) {
#pragma unroll
        for (int i = 0; i < 4; ++i) {
          const u4 v = LD128((bad >> i) & 1u ? ra + i * 1024 : WS_OFF);
          rv[i] = (bad >> i) & 1u ? v : rv[i];
        }
        bad = 0;
#pragma unroll
        for (int i = 0; i < 4; ++i) bad |= GBAD(rv[i]) ? 1u << i : 0u;
        if (all64(bad == 0u)) break;
        __builtin_amdgcn_s_sleep(4);
        CLOB();
      }
      STMP(10);
      unsigned key[4];
#pragma unroll
      for (int i = 0; i < 4; ++i) {
        const unsigned b = rv[i][0] << 16;
        key[i] = ((b ^ ((unsigned)((int)b >> 31) | 0x80000000u)) & 0xFFFF0000u) | (unsigned)(0xFFFF - (w * 256 + i * 64 + l));
      }
      unsigned* cand = (unsigned*)(lds + L_ROWM);
      LAS unsigned* cnt = (LAS unsigned*)(lds + L_CNT) + 1;
      { const unsigned m = wave_max(max(mx3(key[0], key[1], key[2]), key[3]));
        cand[l < 8 ? w * 8 + l : 96 + w] = l < 8 ? 0u : m; }
      LBAR();
      { const u4 m0 = *(const u4*)(cand + 96), m1 = *(const u4*)(cand + 100);
        const unsigned T = min(min(min(m0[0], m0[1]), min(m0[2], m0[3])), min(min(m1[0], m1[1]), min(m1[2], m1[3])));
        unsigned pos[4], n = 0, base;
#pragma unroll
        for (int i = 0; i < 4; ++i) { const u64 b = __builtin_amdgcn_ballot_w64(key[i] >= T);
          pos[i] = __builtin_amdgcn_mbcnt_hi((unsigned)(b >> 32), __builtin_amdgcn_mbcnt_lo((unsigned)b, n)); n += __builtin_popcountll(b); }
        asm volatile("s_mov_b64 exec, 1\n ds_add_rtn_u32 %0, %1, %2\n s_mov_b64 exec, -1\n s_waitcnt lgkmcnt(0)" : "=v"(base) : "v"((unsigned)(size_t)cnt), "v"(n) : "memory");
        base = RFL(base);
#pragma unroll
        for (int i = 0; i < 4; ++i) cand[key[i] >= T ? min(base + pos[i], 72u) : 72u + np] = key[i]; }
      LBAR();
      STMP(11);
      if (RFL(*cnt) > 64u || FORCE_SLOW) {
#define CE(i, j) do { const unsigned hi_ = max(key[i], key[j]), lo_ = min(key[i], key[j]); key[i] = hi_; key[j] = lo_; } while (0)
        CE(0, 1); CE(2, 3); CE(0, 2); CE(1, 3); CE(1, 2);
        unsigned cv = 0;
#pragma nounroll
        for (int it = 0; it < 8; ++it) {
          const unsigned m = wave_max(key[0]);
          const bool win = key[0] == m;
          key[0] = win ? key[1] : key[0]; key[1] = win ? key[2] : key[1]; key[2] = win ? key[3] : key[2]; key[3] = win ? 0u : key[3];
          cv = sl == it ? m : cv;
        }
        if (l < 8) cand[w * 8 + l] = cv;
        LBAR();
      }
      {
        const unsigned c = cand[w * 8 + sl];
        const u4 c0 = *(const u4*)(cand + rr * 8), c1 = *(const u4*)(cand + rr * 8 + 4);
        unsigned r = 0;
#pragma unroll
        for (int e = 0; e < 4; ++e) r += (c0[e] > c ? 1u : 0u) + (c1[e] > c ? 1u : 0u);
        r += (unsigned)__builtin_amdgcn_update_dpp(0, (int)r, 0x128, 0xf, 0xf, false);
        auto a16 = __builtin_amdgcn_permlane16_swap(r, r, false, false); r = a16[0] + a16[1];
        auto a32 = __builtin_amdgcn_permlane32_swap(r, r, false, false); r = a32[0] + a32[1];
        ST64(((u2){c, tlo}), r < 8u ? WS_MSG + rr * 256 + 64 + r * 8 : WS_OFF);
        STMP(29); }
    }
    if (master && w != 4) lds_wait<4>((LAS unsigned*)(lds + L_CNT) + 3, 1);
    if (master && w == 4) {
      bool ok = false;
#pragma nounroll
      for (unsigned it = 0; it < SPIN_CAP; ++it) {
        if (!ok) {
          u4 f0 = LD128(WS_FLAG + l * 32), f1 = LD128(WS_FLAG + l * 32 + 16);
          ok = f0[0] == tlo && f0[1] == thi && f0[2] == tlo && f0[3] == thi && f1[0] == tlo && f1[1] == thi && f1[2] == tlo && f1[3] == thi;
        }
        if (all64(ok)) break;
        __builtin_amdgcn_s_sleep(8);
        CLOB();
      }
      CLOB();
      if (l < 8) ST64(tgv, WS_MSG + l * 256);
      if (l == 0) ST64(tgv, WS_EP);
      STMP(30);
      lds_arrive((LAS unsigned*)(lds + L_CNT) + 3);
    }
    GL2(0, !master, G0A, 8);
    GL(1, !master);
    { WO_SETUP(); WOR(w, 32, 8); }
    GT(0, 12);
    GL(2, !half);
    GT(1, 12);
    u2 f;
    { const unsigned m_ = msg; ALD64(f, m_); }
    GL(3, !half);
    GT(2, 9);
    WAITV(8); PIN(f);
    if (!KEYOK(f)) {
#pragma nounroll
      for (unsigned it = 0; it < SPIN_CAP; ++it) {
        CLOB();
        f = LD64(msg);
        if (KEYOK(f)) break;
        __builtin_amdgcn_s_sleep(16);
      }
    }
    CLOB();
    STMP(9);
    if (w == 0) {
      if (l < 8) ((unsigned*)(lds + L_TAG + 16))[l] = f[0];
      if (l == 8) *(u2*)(lds + L_TAG) = tgv;
    }
    u4 df[2][8];
    u16* hb = (u16*)(lds + L_KX);
#define DT(b) do { _Pragma("unroll") for (int c_ = 0; c_ < 8; ++c_) PIN(dw[b][c_]); _Pragma("unroll") for (int c_ = 0; c_ < 8; ++c_) { TRFRAG(df[b][c_], dw[b], c_); PIN(df[b][c_]); } SBAR(); } while (0)
    {
      const unsigned key_ = (unsigned)__builtin_amdgcn_update_dpp((int)f[0], (int)f[0], 0x128, 0xf, 0xc, false);
      const int idx = (0xFFFF - (int)(key_ & 0xFFFF)) & 2047;
      u4 r0, r1, msx;
      { const u4 rsD = mkrs((const char*)wd + (size_t)hc * 64 * 2048 + w * 256, jm == 7 ? 0 : 63 * 2048 + 256);
        const unsigned xo = (idx * D + 128 * w + (help ? 64 : 0) + 32 * (np >> 3) + 8 * g) * 2, mo = WS_MS + idx * 16, o_ = g * 8 * 2048 + np * 16;
        asm volatile("buffer_load_dwordx4 %0, %2, %3, 0 offen\n buffer_load_dwordx4 %1, %2, %3, 0 offen offset:128" : "=&v"(r0), "=&v"(r1) : "v"(xo), "s"(rsR) : "memory");
        ALD128(msx, mo);
#pragma unroll
        for (int i = 0; i < 16; ++i)
          asm volatile("buffer_load_dwordx4 %0, %1, %2, %3 offen offset:%4 nt" : "=v"(dw[i >> 3][i & 7]) : "v"(o_), "s"(rsD), "s"((unsigned)((i >> 3) * 65536 + (i & 6) * 2048)), "i"((i & 1) * 2048) : "memory"); }
      WAITV(16); PIN(r0); PIN(r1);
#pragma unroll
      for (int i = 0; i < 8; ++i) PIN(gw[3][i]);
      f4 acc[4];
#pragma unroll
      for (int c2 = 0; c2 < 4; ++c2) acc[c2] = (f4)(0.f);
      PIN(msx);
      const float m = hi(msx[0]), rs = U2F(msx[2]);
#pragma unroll
      for (int b2 = 0; b2 < 2; ++b2) {
        if (b2 && half) break;
        u4 xl[2], xh[2];
#pragma unroll
        for (int e = 0; e < 4; ++e) {
          const unsigned r = b2 ? r1[e] : r0[e];
          const unsigned v = pk2((lo(r) - m) * rs, (hi(r) - m) * rs);
          const unsigned o = (unsigned)__builtin_amdgcn_update_dpp(0, (int)v, 0x128, 0xf, 0xf, false);
          xl[0][e] = np < 8 ? v : 0u; xh[0][e] = np < 8 ? 0u : o; xl[1][e] = np < 8 ? o : 0u; xh[1][e] = np < 8 ? 0u : v;
        }
#pragma unroll
        for (int it = 0; it < 2; ++it)
#pragma unroll
          for (int c = 0; c < 8; ++c) {
            if (2 * b2 + it == 3) { TRFRAG(gf[3][c], gw[3], c); }
            acc[c >> 1] = MFMA16(BC8((c & 1) ? xh[it] : xl[it]), BC8(gf[2 * b2 + it][c]), acc[c >> 1]);
          }
        SBAR();
      }
      STMP(12);
      red += 3072;
#pragma unroll
      for (int c2 = 0; c2 < 4; ++c2) *(f4*)(red + (w * 4 + c2) * 256 + l * 4) = acc[c2];
      SBAR();
      WAITV(8);
      DT(0);
      LBAR();
      f2 sv = *(const f2*)(red + (w >> 1) * 256 + l * 4 + (w & 1) * 2);
#pragma unroll
      for (int i = 1; i < 8; ++i) sv += *(const f2*)(red + (i * 4 + (w >> 1)) * 256 + l * 4 + (w & 1) * 2);
      const unsigned gp = WS_GP + (xcd * 512 + w * 64 + l) * 16;
      if (jm == 7) ST128(((u4){F2U(sv[0]), tlo, F2U(sv[1]), thi}), help ? gp : WS_OFF);
      else {
      if (half) {
        u4 pv;
#pragma nounroll
        for (unsigned it = 0; it < SPIN_CAP; ++it) {
          pv = LD128(gp);
          if (all64(GBAD(pv) == 0u)) break;
          __builtin_amdgcn_s_sleep(8);
          CLOB();
        }
        sv += (f2){U2F(pv[0]), U2F(pv[2])};
      }
#pragma unroll
      for (int e = 0; e < 2; ++e) {
        const float gg = rbf(sv[e]), u1 = DPPF(gg, 0x128, 0xf);
        const float hv = rbf(gg * __builtin_amdgcn_rcpf(1.f + __builtin_amdgcn_exp2f(gg * -1.44269504f))) * u1;
        if (np < 8) hb[(4 * (g & 1) + 2 * (w & 1) + e) * 64 + 8 * np + 2 * (w >> 1) + (g >> 1)] = (u16)(pk2(hv, hv) & 0xFFFFu);
      }
      }
      SBAR();
      WAITV(0);
      DT(1);
    }
    LBAR();
    STMP(13);
    u4 hu;
    XOWN();
    { const unsigned ho = w < 4 ? WS_HU + xcd * 128 : WS_OFF; ALD128(hu, ho); }
    if (w >= 4) { WO_SETUP(); WOR(28 + w, YO, 4); }
    u4 yo[4];
    if (jm < 7) {
      f4 acd[4];
#pragma unroll
      for (int c = 0; c < 4; ++c) acd[c] = (f4)(0.f);
#pragma unroll
      for (int fb = 0; fb < 2; ++fb) {
        const u4 ha = *(const u4*)(hb + sl * 64 + 32 * fb + 8 * g);
        u4 hl, hh;
#pragma unroll
        for (int e = 0; e < 4; ++e) { hl[e] = np < 8 ? ha[e] : 0u; hh[e] = np < 8 ? 0u : ha[e]; }
#pragma unroll
        for (int c = 0; c < 4; ++c) acd[c] = MFMA16(BC8(hh), BC8(df[fb][c + 4]), MFMA16(BC8(hl), BC8(df[fb][c]), acd[c]));
      }
#pragma unroll
      for (int e = 0; e < 4; ++e) { yo[e] = tag4((f4){acd[0][e], acd[1][e], acd[2][e], acd[3][e]}, tlo); PIN(yo[e]); }
    }
#pragma unroll
    for (int e = 0; e < 4; ++e) ST128(yo[e], jm < 7 ? WS_YP + (hc * 8 + 4 * (g & 1) + e) * 4096 + w * 512 + (g >> 1) * 256 + np * 16 : WS_OFF);
    SBAR();
    STMP(14);
    if (w >= 4 && YO < 128) { WO_SETUP(); WOR(YO - 4 + w, 128, 4); WAITV(0); }
    WAITV(4); PIN(hu); PIN(st2[0]); PIN(st2[1]);
    if (w >= 4) lds_arrive((LAS unsigned*)(lds + L_CNT) + 2);
    else {
      OINIT();
      if (!all64(hu[0] == tlo && hu[1] == thi)) {
#pragma nounroll
        for (unsigned it = 0; it < SPIN_CAP; ++it) {
          CLOB();
          hu = LD128(WS_HU + xcd * 128); PIN(hu);
          if (all64(hu[0] == tlo && hu[1] == thi)) break;
          __builtin_amdgcn_s_sleep(16);
        }
      }
      tpoll = RFL(hu[2]);
      const unsigned now = (unsigned)__builtin_amdgcn_s_memrealtime(); if (tpoll - now > PDK) tpoll = now;
    }
    STMP(21);
    CPE(4);
    STMP(20);
  }

  LANE();
  LAS unsigned* cnt = (LAS unsigned*)(lds + L_CNT);
  if (ow >= 4) {
#define YD 16
    lds_wait<4>(cnt + 4, 1);
    PACE(YD);
    const unsigned mykey = ((const unsigned*)(lds + L_TAG + 16))[sl], tlo = RFL(*(const unsigned*)(lds + L_TAG));
#pragma unroll 1
    for (int i = ow - 4; i < 8; i += 4) {
      const unsigned kk = (unsigned)__builtin_amdgcn_readlane((int)mykey, i);
      const int ti = 0xFFFF - (int)(kk & 0xFFFF);
      if ((ti >> 7) != mb2) continue;
      const unsigned yb = WS_YP + (g * 14 * 8 + i) * 4096 + (nb >> 1) * 512 + (np >> 3) * 256 + ((nb & 1) * 8 + sl) * 16;
      f4 y;
#pragma nounroll
      for (unsigned it = 0; it < SPIN_CAP; ++it) {
        u4 yv[14];
#pragma unroll
        for (int k = 0; k < 14; ++k) yv[k] = LD128(yb + k * 32768);
        SBAR();
        unsigned bad = 0;
        y = untag4(yv[0], tlo, bad);
#pragma unroll
        for (int k = 1; k < 14; ++k) { const f4 t = untag4(yv[k], tlo, bad); y = (f4){add1(y[0], t[0]), add1(y[1], t[1]), add1(y[2], t[2]), add1(y[3], t[3])}; }
        if (tagok(bad)) break;
        __builtin_amdgcn_s_sleep(YSL);
        CLOB();
      }
#pragma unroll
      for (int e = 0; e < 4; ++e) {
        auto a16 = __builtin_amdgcn_permlane16_swap(F2U(y[e]), F2U(y[e]), false, false);
        y[e] = add1(U2F(a16[0]), U2F(a16[1]));
        auto a32 = __builtin_amdgcn_permlane32_swap(F2U(y[e]), F2U(y[e]), false, false);
        y[e] = add1(U2F(a32[0]), U2F(a32[1]));
      }
      const unsigned mono = kk >> 16;
      const float rv = U2F(((mono & 0x8000u) ? (mono & 0x7FFFu) : (~mono & 0xFFFFu)) << 16);
      const float sg = rbf(__builtin_amdgcn_rcpf(1.f + __builtin_amdgcn_exp2f(rv * -1.44269504f)));
#pragma unroll
      for (int e = 0; e < 4; ++e) y[e] = rbf(mul1(rbf(y[e]), sg));
      if (g == 0) *(f4*)(lds + L_YS + i * 256 + sl * 32 + (np >> 3) * 16) = y;
    }
    if (ow == 4) ((unsigned*)(lds + L_YS + 2048))[l] = 0u;
    lds_arrive(cnt);
    CPE(5);
    STMP(32);
    STDUMP();
    return;
  }
  const u2 tg = *(const u2*)(lds + L_TAG);
  const int trow = 0xFFFF - (int)(((const unsigned*)(lds + L_TAG + 16))[sl] & 0xFFFFu);
  const unsigned hit = (unsigned)__builtin_amdgcn_ballot_w64((trow >> 5) == mb2 * 4 + ow) & 0xFFu;
  unsigned ya[2] = {8u, 8u}, xw[4], xr_ = ldsb + L_XT + (ow * 32 + rr) * 128 + ((sl ^ rr) * 16), oo[4], cA = ldsb + L_CNT;
  if (hit) {
#pragma unroll 1
    for (int i = 0; i < 8; ++i) { const int row = __builtin_amdgcn_readlane(trow, i) - mb2 * 128 - ow * 32 - np; ya[0] = row == 0 ? i : ya[0]; ya[1] = row == 16 ? i : ya[1]; }
  }
#pragma unroll
  for (int i = 0; i < 4; ++i) { xw[i] = ldsb + L_XT + (ow * 32 + np) * 128 + (g & 1) * 8 + (((2 * i + (g >> 1)) ^ sl) * 16);
    oo[i] = (mb2 * 128 + ow * 32 + 8 * i + rr) * 2048 + nb * 128 + sl * 16; PIN(xw[i]); PIN(oo[i]); }
  ya[0] = ldsb + L_YS + ya[0] * 256 + g * 16; ya[1] = ldsb + L_YS + ya[1] * 256 + g * 16;
  PIN(ya[0]); PIN(ya[1]); PIN(xr_); PIN(cA);
  asm volatile("" : "+s"(ka));
  const __amdgpu_buffer_rsrc_t ors = __builtin_amdgcn_make_buffer_rsrc((u16*)ka[8], 0, S * D * 2, 0x00020000);
  unsigned woad[2][4];
  { const int q = np >> 2, p = l & 3, row = 4 * g + q;
#pragma unroll
    for (int ct = 0; ct < 4; ++ct) { woad[0][ct] = ldsb + L_WO + row * 128 + ((ct ^ ((row >> 1) & 3)) * 32) + p * 8; woad[1][ct] = woad[0][ct] + 65536; } }
  s4 b[2][16];
#define K3RD(hc, i) RDTR(b[(hc) & 1][i], woad[((hc) >> 2) & 1][((i) >> 1) & 3], ((((hc) & 3) * 4 + ((hc) >> 3) * 2 + ((i) >> 3)) * 4096 + ((i) & 1) * 2048))
#define K3FIRST() do { lds_wait<0>(cnt + 2, wneed); K3RD(0, 0); K3RD(0, 1); K3RD(0, 2); K3RD(0, 3); K3RD(0, 4); K3RD(0, 5); K3RD(0, 6); K3RD(0, 7); \
    K3RD(0, 8); K3RD(0, 9); K3RD(0, 10); K3RD(0, 11); K3RD(0, 12); K3RD(0, 13); K3RD(0, 14); K3RD(0, 15); WAITL(0); \
    _Pragma("unroll") for (int i = 0; i < 16; ++i) PIN(b[0][i]); } while (0)
  const unsigned wneed = j < 16 ? 7 : j < 24 ? 0 : 12;
  const bool wlate = j >= 24 && (int)((unsigned)__builtin_amdgcn_s_memrealtime() + 60u - tpoll) > 0;
  if (!wlate) K3FIRST();
  const unsigned flg = l < 32 ? WS_OFL + (mb2 * 32 + np * 2 + g) * 128 : WS_OFF;
#pragma nounroll
  while ((int)((unsigned)__builtin_amdgcn_s_memrealtime() - tpoll) < 0) __builtin_amdgcn_s_sleep(1);
  STMP(26);
  unsigned rdy = 0;
#define OPOLL(MASK, SL) _Pragma("nounroll") for (unsigned it = 0; it < SPIN_CAP && (rdy & (MASK)) != (MASK); ++it) { \
    u2 f_ = LD64(((((MASK) & ~rdy) >> (l & 31)) & 1u) ? flg : WS_OFF); \
    rdy |= (unsigned)__builtin_amdgcn_ballot_w64(f_[0] == tg[0] && f_[1] == tg[1]); \
    if ((SL) && rdy == 0) __builtin_amdgcn_s_sleep(SL); \
    CLOB(); }
#define OG 16
  OPOLL((1u << (OG / 2)) - 1, OSL);
  CLOB();
  STMP(6);
#define OPF 3
#define OW0 6
#define OW 12
  u4 a[32]; u2 fl;
  const unsigned abase = WS_OF + (mb2 * 8 + ow * 2) * 32768 + l * 16, abase1 = abase + 32768;
#define OL1(f) a[(f) & 31] = __builtin_amdgcn_raw_buffer_load_b128(wrs, (((f) & 1) ? abase1 : abase) + (((f) >> 5) * 2 + (((f) >> 1) & 1)) * 1024, (((f) >> 2) & 7) * 4096, 16)
#define OMK(f) ((unsigned)((1ull << (((f) + OG) / 2)) - 1))
#define OLD(f) do { if ((f) < 64) { if ((f) && !((f) % OG) && (rdy & OMK(f)) != OMK(f)) { rdy |= (unsigned)__builtin_amdgcn_ballot_w64(fl[0] == tg[0] && fl[1] == tg[1]); OPOLL(OMK(f), 0); CLOB(); } \
    OL1(f); \
    if (((f) % OG) == OPF && (f) < 64 - OG) fl = LD64((((~rdy) >> (l & 31)) & 1u) ? flg : WS_OFF); } } while (0)
#pragma unroll
  for (int f = 0; f < OW0; ++f) { OLD(f); SBAR(); }
  if (ow == 0) lds_arrive(cnt + 4);
  if (wlate) K3FIRST();
#define OHI(c) ((OW0 + 2 * (c)) < (OW + (c)) ? (OW0 + 2 * (c)) : (OW + (c)))
#define K3R(i) (((i) ^ ((i) >> 1)) & 1)
#define K3C(hc, i) ((hc) * 4 + ((i) >> 2))
#define K3M(hc, i) do { if (!((i) & 1)) WAITL((hc) < 15 ? 14 : 14 - (i)); SBAR(); \
    oc[K3R(i)][((i) >> 1) & 3] = MFMA16(CAT(b[(hc) & 1][(i) & ~1], b[(hc) & 1][(i) | 1]), BC8(a[((hc) * 4 + ((i) >> 3) * 2 + K3R(i)) & 31]), oc[K3R(i)][((i) >> 1) & 3]); SBAR(); \
    if ((hc) < 15) { K3RD((hc) + 1, i); } \
    if (((i) & 3) == 3) { OLD(OHI(K3C(hc, i) + 1) - 1); } else if (((i) & 3) == 1 && OHI(K3C(hc, i) + 1) - OHI(K3C(hc, i)) == 2) { OLD(OHI(K3C(hc, i))); } \
    SBAR(); } while (0)
#define K3H(hc) do { K3M(hc, 0); K3M(hc, 1); K3M(hc, 2); K3M(hc, 3); K3M(hc, 4); K3M(hc, 5); K3M(hc, 6); K3M(hc, 7); \
    K3M(hc, 8); K3M(hc, 9); K3M(hc, 10); K3M(hc, 11); K3M(hc, 12); K3M(hc, 13); K3M(hc, 14); K3M(hc, 15); } while (0)
  STMP(28);
  K3H(0); STMP(27); K3H(1); K3H(2); K3H(3); K3H(4); K3H(5); K3H(6); K3H(7); STMP(31); STMC(42); K3H(8); K3H(9); K3H(10); K3H(11); K3H(12); K3H(13); K3H(14);
  f4 yv[2][4]; unsigned yf = 4u;
#define K3L(r, n) do { oc[r][(n) & 3] = MFMA16(CAT(b[1][2 * (n)], b[1][2 * (n) + 1]), BC8(a[28 + ((n) >> 2) * 2 + (r)]), oc[r][(n) & 3]); SBAR(); } while (0)
#define YRD() do { _Pragma("unroll") for (int i = 0; i < 8; ++i) RD128(yv[i >> 2][i & 3], ya[i >> 2], (i & 3) * 64); } while (0)
#define OWR(r, ct) do { const u2 v_ = (u2){pk2(oc[r][ct][0], oc[r][ct][1]), pk2(oc[r][ct][2], oc[r][ct][3])}; \
    asm volatile("ds_write_b64 %0, %1 offset:%2" :: "v"(xw[ct]), "v"(v_), "i"((r) * 2048) : "memory"); SBAR(); } while (0)
  u4 ov[4];
  WAITL(8); SBAR(); K3L(0, 0); K3L(0, 1); K3L(0, 2); K3L(0, 3); WAITL(0);
  if (hit) { RD32(yf, cA, 0); YRD(); }
  SBAR();
  K3L(0, 4); K3L(0, 5); K3L(0, 6); K3L(0, 7); K3L(1, 0); K3L(1, 1); K3L(1, 2); K3L(1, 3);
  if (hit) {
    K3L(1, 4); K3L(1, 5); K3L(1, 6); K3L(1, 7);
    WAITL(0); PIN(yf);
    if (RFL(yf) < 4u) { lds_wait<0>(cnt, 4); YRD(); WAITL(0); }
#pragma unroll
    for (int i = 0; i < 8; ++i) { PIN(yv[i >> 2][i & 3]); oc[i >> 2][i & 3] += yv[i >> 2][i & 3]; }
    OWR(0, 0); OWR(0, 1); OWR(0, 2); OWR(0, 3);
  } else {
    K3L(1, 4); OWR(0, 0); K3L(1, 5); OWR(0, 1); K3L(1, 6); OWR(0, 2); K3L(1, 7); OWR(0, 3);
  }
  STMP(7); STMC(43);
  RD128(ov[0], xr_, 0); RD128(ov[1], xr_, 1024);
  OWR(1, 0); OWR(1, 1); OWR(1, 2); OWR(1, 3);
  RD128(ov[2], xr_, 2048); RD128(ov[3], xr_, 3072);
  WAITL(6); PIN(ov[0]); PIN(ov[1]);
  __builtin_amdgcn_raw_buffer_store_b128(ov[0], ors, oo[0], 0, 16); __builtin_amdgcn_raw_buffer_store_b128(ov[1], ors, oo[1], 0, 16);
  SBAR();
  WAITL(0); PIN(ov[2]); PIN(ov[3]);
  __builtin_amdgcn_raw_buffer_store_b128(ov[2], ors, oo[2], 0, 16); __builtin_amdgcn_raw_buffer_store_b128(ov[3], ors, oo[3], 0, 16);
  CPE(6);
  STMP(16);
  STDUMP();
}

extern "C" void kernel_launch(void* const* d_in, const int* in_sizes, int n_in, void* d_out, int out_size,
                              void* d_ws, size_t ws_size, hipStream_t stream) {
  const u16* x = (const u16*)d_in[0];
  const u16* wqkv = (const u16*)d_in[1];
  const u16* wo = (const u16*)d_in[2];
  const u16* wr = (const u16*)d_in[4];
  const u16* wg = (const u16*)d_in[5];
  const u16* wu = (const u16*)d_in[6];
  const u16* wd = (const u16*)d_in[7];
  (void)hipFuncSetAttribute((const void*)fused, hipFuncAttributeMaxDynamicSharedMemorySize, K_LDS);
  fused<<<256, 512, K_LDS, stream>>>(x, wqkv, wo, wr, wg, wu, wd, (char*)d_ws, (u16*)d_out);
}
```
